```python
import torch

def _softmax(x, axis=-1):
    return torch.softmax(x, dim=axis)
def _gelu(x):
    return torch.nn.functional.gelu(x, approximate="tanh")
def _silu(x):
    return torch.nn.functional.silu(x)
def _sigmoid(x):
    return torch.sigmoid(x)
def _layernorm(x, axis=-1, eps=1e-5):
    m = x.mean(dim=axis, keepdim=True)
    v = x.var(dim=axis, keepdim=True, unbiased=False)
    return (x - m) / torch.sqrt(v + eps)
def _scatter_add(tgt, idx, src, axis=0):
    return tgt.clone().index_add_(axis, idx, src)
def _segment_sum(src, seg, n):
    out = torch.zeros((n,) + src.shape[1:], dtype=src.dtype)
    return out.index_add_(0, seg, src)
def _pad(x, pw):
    flat = tuple(v for pair in reversed(pw) for v in pair)
    return torch.nn.functional.pad(x, flat)

kernel_name = 'concat_heads__mha_block_cached__gqa_block_cached__near_ridge'

def setup_inputs():

    _w = (lambda a: (lambda t: t.to(torch.bfloat16) if t.is_floating_point() else t)(torch.as_tensor(a)))
    def _A_setup():
        B, Sq, D = 16, 112, 576
        H, Sk, Dh = 32, 512, 128
        return {
            "x": _w((lambda *s: torch.randn(*s, dtype=torch.bfloat16) * 0.8)(B, Sq, D)),
            "k_cache": _w((lambda *s: torch.randn(*s, dtype=torch.bfloat16) * 0.8)(B, H, Sk, Dh)),
            "v_cache": _w((lambda *s: torch.randn(*s, dtype=torch.bfloat16) * 0.05)(B, H, Sk, Dh)),
            "wq": _w((lambda *s: torch.randn(*s, dtype=torch.bfloat16) * 0.8)(D, H * Dh) * (D ** -0.5)),
            "wo": _w((lambda *s: torch.randn(*s, dtype=torch.bfloat16) * 0.05)(H * Dh, D) * ((H * Dh) ** -0.5)),
        }
    _A_in = _A_setup()

    def _B_setup():
        B, Sq, D = 16, 112, 576
        Hq, Hkv, Sk, Dh = 32, 32, 512, 128
        assert Hq % Hkv == 0, (Hq, Hkv)
        return {
            "x": _w((lambda *s: torch.randn(*s, dtype=torch.bfloat16) * 0.8)(B, Sq, D)),
            "k_cache": _w((lambda *s: torch.randn(*s, dtype=torch.bfloat16) * 0.8)(B, Hkv, Sk, Dh)),
            "v_cache": _w((lambda *s: torch.randn(*s, dtype=torch.bfloat16) * 0.05)(B, Hkv, Sk, Dh)),
            "wq": _w((lambda *s: torch.randn(*s, dtype=torch.bfloat16) * 0.8)(D, Hq * Dh) * (D ** -0.5)),
            "wo": _w((lambda *s: torch.randn(*s, dtype=torch.bfloat16) * 0.05)(Hq * Dh, D) * ((Hq * Dh) ** -0.5)),
        }
    _B_in = _B_setup()

    return {
        "A_x": _A_in["x"],
        "A_k_cache": _A_in["k_cache"],
        "A_v_cache": _A_in["v_cache"],
        "A_wq": _A_in["wq"],
        "A_wo": _A_in["wo"],
        "B_x": _B_in["x"],
        "B_k_cache": _B_in["k_cache"],
        "B_v_cache": _B_in["v_cache"],
        "B_wq": _B_in["wq"],
        "B_wo": _B_in["wo"],
    }

def reference(A_x, A_k_cache, A_v_cache, A_wq, A_wo, B_x, B_k_cache, B_v_cache, B_wq, B_wo):
    def _A_ref(x, k_cache, v_cache, wq, wo):
        B, Sq, D = x.shape
        H, Dh = 32, 128
        q = (lambda x, *d: x.permute(*d))((x @ wq).reshape(B, Sq, H, Dh), 0, 2, 1, 3)
        scores = torch.einsum("bhqd,bhkd->bhqk", q, k_cache) * (Dh ** -0.5)
        p = _softmax(scores, axis=-1)
        o = torch.einsum("bhqk,bhkd->bhqd", p, v_cache)
        return (lambda x, *d: x.permute(*d))(o, 0, 2, 1, 3).reshape(B, Sq, H * Dh) @ wo

    def _B_ref(x, k_cache, v_cache, wq, wo):
        B, Sq, D = x.shape
        Hq, Dh = 32, 128
        Hkv = k_cache.shape[1]
        rep = Hq // Hkv
        q = (lambda x, *d: x.permute(*d))((x @ wq).reshape(B, Sq, Hq, Dh), 0, 2, 1, 3)
        k = (lambda x, n, axis: torch.repeat_interleave(x, n, dim=axis))(k_cache, rep, axis=1)
        v = (lambda x, n, axis: torch.repeat_interleave(x, n, dim=axis))(v_cache, rep, axis=1)
        scores = torch.einsum("bhqd,bhkd->bhqk", q, k) * (Dh ** -0.5)
        p = _softmax(scores, axis=-1)
        o = torch.einsum("bhqk,bhkd->bhqd", p, v)
        return (lambda x, *d: x.permute(*d))(o, 0, 2, 1, 3).reshape(B, Sq, Hq * Dh) @ wo

    a = _A_ref(A_x, A_k_cache, A_v_cache, A_wq, A_wo)
    b = _B_ref(B_x, B_k_cache, B_v_cache, B_wq, B_wo)
    return torch.concatenate([a, b], axis=-1)
```

```cpp
#include <hip/hip_runtime.h>
#include <hip/hip_bf16.h>

struct P1 { const char* x[2]; const char* k[2]; const char* v[2]; const char* wq[2]; const char* wo[2]; char* ws; char* out; };

__global__ __launch_bounds__(256, 1) void k_attn(P1 p) {
  const unsigned wave = __builtin_amdgcn_readfirstlane(threadIdx.x >> 6);
  const unsigned xcd = blockIdx.x & 7, idx = blockIdx.x >> 3;
  const unsigned br = xcd >> 2, b = (xcd & 1) * 8 + (idx >> 2), sl = 4 * ((xcd >> 1) & 1) + (idx & 3), h = 4 * sl + wave;
  const unsigned c0 = (0x7420u >> (4 * (((idx >> 2) + idx) & 3))) & 15, n0 = 2 * (idx >> 2);
  const unsigned role = sl ^ ((b & 1) << 2);
  const unsigned long long px = (unsigned long long)(br ? p.x[1] : p.x[0]) + (size_t)b * (112 * 576 * 2);
  const unsigned long long pw = (unsigned long long)(br ? p.wq[1] : p.wq[0]) + h * 256;
  const unsigned long long pk = (unsigned long long)(br ? p.k[1] : p.k[0]) + (size_t)(b * 32 + h) * 131072;
  const unsigned long long pv = (unsigned long long)(br ? p.v[1] : p.v[0]) + (size_t)(b * 32 + h) * 131072;
  const unsigned long long pu = (unsigned long long)(br ? p.wo[1] : p.wo[0]) + (size_t)sl * (512 * 1152);
  const unsigned long long po = (unsigned long long)p.out + (size_t)b * (112 * 2304) + br * 1152;
  asm volatile(
  "L_start:\n"
  "s_and_b32 s74, %7, 3\n"
  "s_and_b32 s98, %8, 15\n"
  "s_and_b32 s85, %8, 0xffff00\n"
  "s_mov_b64 s[40:41], %1\n"
  "s_and_b32 s41, s41, 0xffff\n"
  "s_mov_b32 s42, s85\n"
  "s_mov_b32 s43, 0x20000\n"
  "s_mov_b64 s[36:37], %0\n"
  "s_and_b32 s37, s37, 0xffff\n"
  "s_mov_b32 s38, 129024\n"
  "s_mov_b32 s39, 0x20000\n"
  "v_mbcnt_lo_u32_b32 v252, -1, 0\n"
  "v_mbcnt_hi_u32_b32 v252, -1, v252\n"
  "v_lshrrev_b32 v240, 4, v252\n"
  "v_and_b32 v241, 15, v252\n"
  "v_lshlrev_b32 v242, 16, v240\n"
  "v_lshl_add_u32 v228, v241, 4, v242\n"
  "v_add_u32 v229, 8192, v228\n"
  "v_add_u32 v230, 16384, v228\n"
  "v_add_u32 v231, 24576, v228\n"
  "v_add_u32 v232, 32768, v228\n"
  "v_add_u32 v233, 40960, v228\n"
  "v_add_u32 v234, 49152, v228\n"
  "v_add_u32 v235, 57344, v228\n"
  "s_lshl_b32 s61, s98, 19\n"
  "buffer_load_dwordx4 v[0:3], v228, s[40:43], s61 offen\n"
  "buffer_load_dwordx4 v[4:7], v229, s[40:43], s61 offen\n"
  "buffer_load_dwordx4 v[8:11], v230, s[40:43], s61 offen\n"
  "buffer_load_dwordx4 v[12:15], v231, s[40:43], s61 offen\n"
  "buffer_load_dwordx4 v[16:19], v232, s[40:43], s61 offen\n"
  "buffer_load_dwordx4 v[20:23], v233, s[40:43], s61 offen\n"
  "buffer_load_dwordx4 v[24:27], v234, s[40:43], s61 offen\n"
  "buffer_load_dwordx4 v[28:31], v235, s[40:43], s61 offen\n"
  "s_and_b32 s86, s74, 1\n"
  "s_lshl_b32 s87, s86, 2\n"
  "v_add_u32 v242, s87, v240\n"
  "v_and_b32 v243, 7, v252\n"
  "v_xor_b32 v242, v242, v243\n"
  "v_lshrrev_b32 v243, 3, v252\n"
  "v_mul_u32_u24 v243, 1152, v243\n"
  "v_lshl_add_u32 v236, v242, 4, v243\n"
  "s_bfe_u32 s92, %8, 0x30018\n"
  "s_add_u32 s97, s92, 1\n"
  "s_cmp_ge_u32 s97, 7\n"
  "s_cselect_b32 s97, 0, s97\n"
  "s_add_u32 s87, s74, 0\n"
  "s_lshl_b32 s62, s87, 10\n"
  "s_lshl1_add_u32 s87, s97, s87\n"
  "s_sub_u32 s66, s87, 14\n"
  "s_cselect_b32 s87, s87, s66\n"
  "s_mul_i32 s66, s87, 9216\n"
  "s_add_u32 s87, s74, 4\n"
  "s_lshl_b32 s63, s87, 10\n"
  "s_lshl1_add_u32 s87, s97, s87\n"
  "s_sub_u32 s67, s87, 14\n"
  "s_cselect_b32 s87, s87, s67\n"
  "s_mul_i32 s67, s87, 9216\n"
  "s_add_u32 s87, s74, 8\n"
  "s_lshl_b32 s64, s87, 10\n"
  "s_lshl1_add_u32 s87, s97, s87\n"
  "s_sub_u32 s68, s87, 14\n"
  "s_cselect_b32 s87, s87, s68\n"
  "s_mul_i32 s68, s87, 9216\n"
  "s_add_u32 s87, s86, 12\n"
  "s_lshl_b32 s65, s87, 10\n"
  "s_lshl1_add_u32 s87, s97, s87\n"
  "s_sub_u32 s69, s87, 14\n"
  "s_cselect_b32 s87, s87, s69\n"
  "s_mul_i32 s69, s87, 9216\n"
  "s_lshl_b32 s73, s98, 7\n"
  "v_add_u32 v239, s73, v236\n"
  "s_add_u32 m0, s62, 34816\n"
  "s_nop 0\n"
  "buffer_load_dwordx4 v239, s[36:39], s66 offen lds\n"
  "s_add_u32 m0, s63, 34816\n"
  "s_nop 0\n"
  "buffer_load_dwordx4 v239, s[36:39], s67 offen lds\n"
  "s_add_u32 m0, s64, 34816\n"
  "s_nop 0\n"
  "buffer_load_dwordx4 v239, s[36:39], s68 offen lds\n"
  "s_add_u32 m0, s65, 34816\n"
  "s_nop 0\n"
  "buffer_load_dwordx4 v239, s[36:39], s69 offen lds\n"
  "s_lshr_b32 s84, %7, 5\n"
  "s_lshl_b32 s84, s84, 2\n"
  "s_or_b32 s84, s84, s74\n"
  "s_getpc_b64 s[56:57]\n"
  "s_and_b32 s57, s57, 0xffff\n"
  "s_mov_b32 s58, L_rare_even-L_start-1024\n"
  "s_mov_b32 s59, 0x20000\n"
  "s_lshl_b32 s87, s84, 9\n"
  "v_min_u32 v242, 3, v252\n"
  "v_lshlrev_b32 v242, 7, v242\n"
  "buffer_load_dword v160, v242, s[56:59], s87 offen\n"
  "s_add_u32 s61, s61, 0x40000\n"
  "buffer_load_dwordx4 v[32:35], v228, s[40:43], s61 offen\n"
  "buffer_load_dwordx4 v[36:39], v229, s[40:43], s61 offen\n"
  "buffer_load_dwordx4 v[40:43], v230, s[40:43], s61 offen\n"
  "buffer_load_dwordx4 v[44:47], v231, s[40:43], s61 offen\n"
  "buffer_load_dwordx4 v[48:51], v232, s[40:43], s61 offen\n"
  "buffer_load_dwordx4 v[52:55], v233, s[40:43], s61 offen\n"
  "buffer_load_dwordx4 v[56:59], v234, s[40:43], s61 offen\n"
  "buffer_load_dwordx4 v[60:63], v235, s[40:43], s61 offen\n"
  "s_add_u32 s73, s73, 128\n"
  "s_cmp_eq_u32 s73, 1152\n"
  "s_cselect_b32 s73, 0, s73\n"
  "v_add_u32 v239, s73, v236\n"
  "s_add_u32 m0, s62, 49152\n"
  "s_nop 0\n"
  "buffer_load_dwordx4 v239, s[36:39], s66 offen lds\n"
  "s_add_u32 m0, s63, 49152\n"
  "s_nop 0\n"
  "buffer_load_dwordx4 v239, s[36:39], s67 offen lds\n"
  "s_add_u32 m0, s64, 49152\n"
  "s_nop 0\n"
  "buffer_load_dwordx4 v239, s[36:39], s68 offen lds\n"
  "s_add_u32 m0, s65, 49152\n"
  "s_nop 0\n"
  "buffer_load_dwordx4 v239, s[36:39], s69 offen lds\n"
  "s_mov_b64 s[44:45], %2\n"
  "s_and_b32 s45, s45, 0xffff\n"
  "s_mov_b32 s46, 0x20000\n"
  "s_mov_b32 s47, 0x20000\n"
  "s_mov_b64 s[48:49], %3\n"
  "s_and_b32 s49, s49, 0xffff\n"
  "s_mov_b32 s50, 0x20000\n"
  "s_mov_b32 s51, 0x20000\n"
  "s_mov_b64 s[56:57], %2\n"
  "s_and_b32 s57, s57, 0xffff\n"
  "s_mov_b32 s58, 0x20000\n"
  "s_mov_b32 s59, 0x20000\n"
  "s_and_b32 s75, s84, 15\n"
  "s_bfe_u32 s99, %8, 0x40004\n"
  "s_lshl_b32 s86, s99, 1\n"
  "s_or_b32 s86, s86, s74\n"
  "s_mul_i32 s86, s86, 18432\n"
  "s_mov_b64 s[94:95], %4\n"
  "s_add_u32 s94, s94, s86\n"
  "s_addc_u32 s95, s95, 0\n"
  "s_and_b32 s95, s95, 0xffff\n"
  "s_lshr_b32 s86, %8, 24\n"
  "s_lshl_b32 s86, s86, 2\n"
  "s_or_b32 s86, s86, s74\n"
  "s_lshl_b32 s86, s86, 15\n"
  "s_mov_b64 s[52:53], %5\n"
  "s_and_b32 s53, s53, 0xffff\n"
  "s_mov_b32 s54, 0x8000\n"
  "s_mov_b32 s55, 0x20000\n"
  "s_add_u32 s52, s52, s86\n"
  "s_addc_u32 s53, s53, 0\n"
  "s_lshr_b32 s86, %8, 27\n"
  "s_lshl_b32 s86, s86, 2\n"
  "s_or_b32 s86, s86, s74\n"
  "s_lshl_b32 s86, s86, 11\n"
  "s_add_u32 s86, s86, 50331648\n"
  "s_mov_b64 s[88:89], %5\n"
  "s_and_b32 s89, s89, 0xffff\n"
  "s_mov_b32 s90, 2048\n"
  "s_mov_b32 s91, 0x20000\n"
  "s_add_u32 s88, s88, s86\n"
  "s_addc_u32 s89, s89, 0\n"
  "s_mov_b32 s70, 0x05040100\n"
  "s_mov_b32 s71, 0x07060302\n"
  "s_mov_b32 s80, 0x3e0293ee\n"
  "s_mov_b32 s81, 0x3e0293ee\n"
  "s_mov_b32 s82, 0x40004000\n"
  "s_mul_i32 s72, s74, 8256\n"
  "v_lshrrev_b32 v242, 1, v241\n"
  "v_xor_b32 v242, v242, v240\n"
  "v_lshlrev_b32 v242, 4, v242\n"
  "v_lshl_add_u32 v242, v241, 7, v242\n"
  "s_mov_b32 s86, 34816\n"
  "v_xor_b32 v243, 64, v242\n"
  "v_add_u32 v237, s86, v242\n"
  "v_add_u32 v238, s86, v243\n"
  "v_and_b32 v243, 1, v240\n"
  "v_lshrrev_b32 v242, 1, v240\n"
  "v_lshl_or_b32 v242, v243, 3, v242\n"
  "v_xor_b32 v242, v242, v241\n"
  "v_lshlrev_b32 v242, 4, v242\n"
  "v_lshl_add_u32 v244, v240, 8, v242\n"
  "v_lshlrev_b32 v242, 2, v240\n"
  "v_xor_b32 v242, v242, v241\n"
  "v_lshlrev_b32 v242, 4, v242\n"
  "v_lshl_add_u32 v245, v240, 8, v242\n"
  "v_and_b32 v253, 3, v241\n"
  "v_lshrrev_b32 v254, 2, v241\n"
  "v_and_b32 v243, 1, v253\n"
  "v_lshrrev_b32 v242, 1, v253\n"
  "v_lshl_or_b32 v242, v243, 3, v242\n"
  "v_lshlrev_b32 v243, 2, v240\n"
  "v_xor_b32 v242, v242, v243\n"
  "v_lshlrev_b32 v242, 4, v242\n"
  "v_xor_b32 v247, 16, v242\n"
  "v_and_b32 v243, 1, v254\n"
  "v_mul_u32_u24 v243, 4128, v243\n"
  "v_lshl_add_u32 v243, v253, 8, v243\n"
  "v_lshrrev_b32 v255, 1, v254\n"
  "v_lshl_add_u32 v243, v255, 11, v243\n"
  "v_add_u32 v243, s72, v243\n"
  "v_add_u32 v246, v242, v243\n"
  "v_add_u32 v247, v247, v243\n"
  "v_and_b32 v243, 1, v240\n"
  "v_mul_u32_u24 v243, 4128, v243\n"
  "v_lshl_add_u32 v243, v254, 8, v243\n"
  "v_lshrrev_b32 v242, 1, v240\n"
  "v_lshl_add_u32 v243, v242, 11, v243\n"
  "v_lshl_add_u32 v243, v253, 3, v243\n"
  "s_add_u32 s86, s72, 66048\n"
  "v_add_u32 v243, s86, v243\n"
  "v_xor_b32 v242, 0, v254\n"
  "v_lshl_add_u32 v248, v242, 6, v243\n"
  "v_xor_b32 v242, 1, v254\n"
  "v_lshl_add_u32 v249, v242, 6, v243\n"
  "v_xor_b32 v242, 2, v254\n"
  "v_lshl_add_u32 v250, v242, 6, v243\n"
  "v_xor_b32 v242, 3, v254\n"
  "v_lshl_add_u32 v251, v242, 6, v243\n"
  "s_lshl_b32 s76, s75, 13\n"
  "s_add_u32 s76, s76, 0x1e000\n"
  "s_mov_b32 s78, s76\n"
  "s_mov_b32 s60, 0\n"
  "v_mov_b32 v242, s72\n"
  "v_mov_b32 v243, 0\n"
  "ds_write_b32 v242, v243 offset:8224\n"
  "ds_write_b32 v242, v243 offset:8228\n"
  "s_waitcnt lgkmcnt(0)\n"
  "s_lshl_b32 s86, s92, 8\n"
  "v_lshrrev_b32 v255, 3, v252\n"
  "v_lshl_add_u32 v255, v255, 9, v252\n"
  "v_lshl_add_u32 v255, v255, 2, s86\n"
  "buffer_load_dword v254, v255, s[88:91], 0 offen offset:128 sc1\n"
  "s_waitcnt vmcnt(14)\n"
  "v_perm_b32 v220, v4, v0, s70\n"
  "v_perm_b32 v221, v12, v8, s70\n"
  "v_perm_b32 v222, v20, v16, s70\n"
  "v_perm_b32 v223, v28, v24, s70\n"
  "s_barrier\n"
  "ds_read_b128 a[224:227], v237 offset:0\n"
  "ds_read_b128 a[228:231], v237 offset:2048\n"
  "ds_read_b128 a[232:235], v237 offset:4096\n"
  "ds_read_b128 a[236:239], v237 offset:6144\n"
  "ds_read_b128 a[240:243], v237 offset:8192\n"
  "ds_read_b128 a[244:247], v237 offset:10240\n"
  "ds_read_b128 a[248:251], v237 offset:12288\n"
  "s_waitcnt lgkmcnt(0)\n"
  "v_mfma_f32_16x16x32_bf16 a[0:3], v[220:223], a[224:227], 0\n"
  "s_add_u32 s61, s61, 0x40000\n"
  "ds_read_b128 v[192:195], v238 offset:0\n"
  "ds_read_b128 v[196:199], v238 offset:2048\n"
  "v_mfma_f32_16x16x32_bf16 a[32:35], v[220:223], a[228:231], 0\n"
  "s_cmp_eq_u32 s61, 0x480000\n"
  "v_perm_b32 v224, v4, v0, s71\n"
  "v_perm_b32 v225, v12, v8, s71\n"
  "v_mfma_f32_16x16x32_bf16 a[64:67], v[220:223], a[232:235], 0\n"
  "s_cselect_b32 s61, 0, s61\n"
  "v_perm_b32 v226, v20, v16, s71\n"
  "v_perm_b32 v227, v28, v24, s71\n"
  "v_mfma_f32_16x16x32_bf16 a[96:99], v[220:223], a[236:239], 0\n"
  "buffer_load_dwordx4 v[64:67], v228, s[40:43], s61 offen\n"
  "v_mfma_f32_16x16x32_bf16 a[128:131], v[220:223], a[240:243], 0\n"
  "ds_read_b128 v[200:203], v238 offset:4096\n"
  "ds_read_b128 v[204:207], v238 offset:6144\n"
  "v_mfma_f32_16x16x32_bf16 a[160:163], v[220:223], a[244:247], 0\n"
  "ds_read_b128 v[208:211], v238 offset:8192\n"
  "ds_read_b128 v[212:215], v238 offset:10240\n"
  "v_mfma_f32_16x16x32_bf16 a[192:195], v[220:223], a[248:251], 0\n"
  "buffer_load_dwordx4 v[68:71], v229, s[40:43], s61 offen\n"
  "ds_read_b128 v[216:219], v238 offset:12288\n"
  "v_mfma_f32_16x16x32_bf16 a[4:7], v[224:227], a[224:227], 0\n"
  "v_mfma_f32_16x16x32_bf16 a[36:39], v[224:227], a[228:231], 0\n"
  "v_perm_b32 v220, v5, v1, s70\n"
  "v_perm_b32 v221, v13, v9, s70\n"
  "v_mfma_f32_16x16x32_bf16 a[68:71], v[224:227], a[232:235], 0\n"
  "v_perm_b32 v222, v21, v17, s70\n"
  "v_perm_b32 v223, v29, v25, s70\n"
  "v_mfma_f32_16x16x32_bf16 a[100:103], v[224:227], a[236:239], 0\n"
  "v_mfma_f32_16x16x32_bf16 a[132:135], v[224:227], a[240:243], 0\n"
  "buffer_load_dwordx4 v[72:75], v230, s[40:43], s61 offen\n"
  "v_mfma_f32_16x16x32_bf16 a[164:167], v[224:227], a[244:247], 0\n"
  "v_mfma_f32_16x16x32_bf16 a[196:199], v[224:227], a[248:251], 0\n"
  "v_mfma_f32_16x16x32_bf16 a[8:11], v[220:223], a[224:227], 0\n"
  "buffer_load_dwordx4 v[76:79], v231, s[40:43], s61 offen\n"
  "v_mfma_f32_16x16x32_bf16 a[40:43], v[220:223], a[228:231], 0\n"
  "v_perm_b32 v224, v5, v1, s71\n"
  "v_perm_b32 v225, v13, v9, s71\n"
  "v_mfma_f32_16x16x32_bf16 a[72:75], v[220:223], a[232:235], 0\n"
  "v_perm_b32 v226, v21, v17, s71\n"
  "v_perm_b32 v227, v29, v25, s71\n"
  "v_mfma_f32_16x16x32_bf16 a[104:107], v[220:223], a[236:239], 0\n"
  "v_mfma_f32_16x16x32_bf16 a[136:139], v[220:223], a[240:243], 0\n"
  "buffer_load_dwordx4 v[80:83], v232, s[40:43], s61 offen\n"
  "v_mfma_f32_16x16x32_bf16 a[168:171], v[220:223], a[244:247], 0\n"
  "v_mfma_f32_16x16x32_bf16 a[200:203], v[220:223], a[248:251], 0\n"
  "v_mfma_f32_16x16x32_bf16 a[12:15], v[224:227], a[224:227], 0\n"
  "buffer_load_dwordx4 v[84:87], v233, s[40:43], s61 offen\n"
  "v_mfma_f32_16x16x32_bf16 a[44:47], v[224:227], a[228:231], 0\n"
  "v_perm_b32 v220, v6, v2, s70\n"
  "v_perm_b32 v221, v14, v10, s70\n"
  "v_mfma_f32_16x16x32_bf16 a[76:79], v[224:227], a[232:235], 0\n"
  "v_perm_b32 v222, v22, v18, s70\n"
  "v_perm_b32 v223, v30, v26, s70\n"
  "v_mfma_f32_16x16x32_bf16 a[108:111], v[224:227], a[236:239], 0\n"
  "v_mfma_f32_16x16x32_bf16 a[140:143], v[224:227], a[240:243], 0\n"
  "v_mfma_f32_16x16x32_bf16 a[172:175], v[224:227], a[244:247], 0\n"
  "buffer_load_dwordx4 v[88:91], v234, s[40:43], s61 offen\n"
  "v_mfma_f32_16x16x32_bf16 a[204:207], v[224:227], a[248:251], 0\n"
  "v_mfma_f32_16x16x32_bf16 a[16:19], v[220:223], a[224:227], 0\n"
  "v_mfma_f32_16x16x32_bf16 a[48:51], v[220:223], a[228:231], 0\n"
  "v_perm_b32 v224, v6, v2, s71\n"
  "v_perm_b32 v225, v14, v10, s71\n"
  "v_mfma_f32_16x16x32_bf16 a[80:83], v[220:223], a[232:235], 0\n"
  "v_perm_b32 v226, v22, v18, s71\n"
  "v_perm_b32 v227, v30, v26, s71\n"
  "v_mfma_f32_16x16x32_bf16 a[112:115], v[220:223], a[236:239], 0\n"
  "buffer_load_dwordx4 v[92:95], v235, s[40:43], s61 offen\n"
  "v_mfma_f32_16x16x32_bf16 a[144:147], v[220:223], a[240:243], 0\n"
  "v_mfma_f32_16x16x32_bf16 a[176:179], v[220:223], a[244:247], 0\n"
  "s_add_u32 s61, s61, 0x40000\n"
  "v_mfma_f32_16x16x32_bf16 a[208:211], v[220:223], a[248:251], 0\n"
  "buffer_load_dwordx4 v[96:99], v228, s[40:43], s61 offen\n"
  "v_mfma_f32_16x16x32_bf16 a[20:23], v[224:227], a[224:227], 0\n"
  "v_mfma_f32_16x16x32_bf16 a[52:55], v[224:227], a[228:231], 0\n"
  "v_perm_b32 v220, v7, v3, s70\n"
  "v_perm_b32 v221, v15, v11, s70\n"
  "v_mfma_f32_16x16x32_bf16 a[84:87], v[224:227], a[232:235], 0\n"
  "v_perm_b32 v222, v23, v19, s70\n"
  "v_perm_b32 v223, v31, v27, s70\n"
  "v_mfma_f32_16x16x32_bf16 a[116:119], v[224:227], a[236:239], 0\n"
  "v_mfma_f32_16x16x32_bf16 a[148:151], v[224:227], a[240:243], 0\n"
  "buffer_load_dwordx4 v[100:103], v229, s[40:43], s61 offen\n"
  "v_mfma_f32_16x16x32_bf16 a[180:183], v[224:227], a[244:247], 0\n"
  "v_mfma_f32_16x16x32_bf16 a[212:215], v[224:227], a[248:251], 0\n"
  "v_mfma_f32_16x16x32_bf16 a[24:27], v[220:223], a[224:227], 0\n"
  "buffer_load_dwordx4 v[104:107], v230, s[40:43], s61 offen\n"
  "v_mfma_f32_16x16x32_bf16 a[56:59], v[220:223], a[228:231], 0\n"
  "v_perm_b32 v224, v7, v3, s71\n"
  "v_perm_b32 v225, v15, v11, s71\n"
  "v_mfma_f32_16x16x32_bf16 a[88:91], v[220:223], a[232:235], 0\n"
  "v_perm_b32 v226, v23, v19, s71\n"
  "v_perm_b32 v227, v31, v27, s71\n"
  "v_mfma_f32_16x16x32_bf16 a[120:123], v[220:223], a[236:239], 0\n"
  "v_mfma_f32_16x16x32_bf16 a[152:155], v[220:223], a[240:243], 0\n"
  "buffer_load_dwordx4 v[108:111], v231, s[40:43], s61 offen\n"
  "v_mfma_f32_16x16x32_bf16 a[184:187], v[220:223], a[244:247], 0\n"
  "v_mfma_f32_16x16x32_bf16 a[216:219], v[220:223], a[248:251], 0\n"
  "v_mfma_f32_16x16x32_bf16 a[28:31], v[224:227], a[224:227], 0\n"
  "buffer_load_dwordx4 v[112:115], v232, s[40:43], s61 offen\n"
  "v_mfma_f32_16x16x32_bf16 a[60:63], v[224:227], a[228:231], 0\n"
  "s_waitcnt vmcnt(18)\n"
  "v_mfma_f32_16x16x32_bf16 a[92:95], v[224:227], a[232:235], 0\n"
  "v_perm_b32 v220, v36, v32, s70\n"
  "v_perm_b32 v221, v44, v40, s70\n"
  "v_mfma_f32_16x16x32_bf16 a[124:127], v[224:227], a[236:239], 0\n"
  "v_perm_b32 v222, v52, v48, s70\n"
  "v_perm_b32 v223, v60, v56, s70\n"
  "v_mfma_f32_16x16x32_bf16 a[156:159], v[224:227], a[240:243], 0\n"
  "v_mfma_f32_16x16x32_bf16 a[188:191], v[224:227], a[244:247], 0\n"
  "buffer_load_dwordx4 v[116:119], v233, s[40:43], s61 offen\n"
  "v_mfma_f32_16x16x32_bf16 a[220:223], v[224:227], a[248:251], 0\n"
  "s_waitcnt lgkmcnt(0)\n"
  "s_waitcnt vmcnt(15)\n"
  "s_barrier\n"
  "v_mfma_f32_16x16x32_bf16 a[0:3], v[220:223], v[192:195], a[0:3]\n"
  "ds_read_b128 a[224:227], v237 offset:14336\n"
  "ds_read_b128 a[228:231], v237 offset:16384\n"
  "v_mfma_f32_16x16x32_bf16 a[32:35], v[220:223], v[196:199], a[32:35]\n"
  "v_perm_b32 v224, v36, v32, s71\n"
  "v_perm_b32 v225, v44, v40, s71\n"
  "v_mfma_f32_16x16x32_bf16 a[64:67], v[220:223], v[200:203], a[64:67]\n"
  "v_perm_b32 v226, v52, v48, s71\n"
  "v_perm_b32 v227, v60, v56, s71\n"
  "v_mfma_f32_16x16x32_bf16 a[96:99], v[220:223], v[204:207], a[96:99]\n"
  "buffer_load_dwordx4 v[120:123], v234, s[40:43], s61 offen\n"
  "v_mfma_f32_16x16x32_bf16 a[128:131], v[220:223], v[208:211], a[128:131]\n"
  "ds_read_b128 a[232:235], v237 offset:18432\n"
  "ds_read_b128 a[236:239], v237 offset:20480\n"
  "v_mfma_f32_16x16x32_bf16 a[160:163], v[220:223], v[212:215], a[160:163]\n"
  "ds_read_b128 a[240:243], v237 offset:22528\n"
  "ds_read_b128 a[244:247], v237 offset:24576\n"
  "v_mfma_f32_16x16x32_bf16 a[192:195], v[220:223], v[216:219], a[192:195]\n"
  "buffer_load_dwordx4 v[124:127], v235, s[40:43], s61 offen\n"
  "ds_read_b128 a[248:251], v237 offset:26624\n"
  "v_mfma_f32_16x16x32_bf16 a[4:7], v[224:227], v[192:195], a[4:7]\n"
  "v_mfma_f32_16x16x32_bf16 a[36:39], v[224:227], v[196:199], a[36:39]\n"
  "v_perm_b32 v220, v37, v33, s70\n"
  "v_perm_b32 v221, v45, v41, s70\n"
  "v_mfma_f32_16x16x32_bf16 a[68:71], v[224:227], v[200:203], a[68:71]\n"
  "v_perm_b32 v222, v53, v49, s70\n"
  "v_perm_b32 v223, v61, v57, s70\n"
  "s_add_u32 s73, s73, 128\n"
  "s_cmp_eq_u32 s73, 1152\n"
  "s_cselect_b32 s73, 0, s73\n"
  "v_add_u32 v239, s73, v236\n"
  "v_mfma_f32_16x16x32_bf16 a[100:103], v[224:227], v[204:207], a[100:103]\n"
  "s_add_u32 m0, s62, 63488\n"
  "v_mfma_f32_16x16x32_bf16 a[132:135], v[224:227], v[208:211], a[132:135]\n"
  "buffer_load_dwordx4 v239, s[36:39], s66 offen lds\n"
  "v_mfma_f32_16x16x32_bf16 a[164:167], v[224:227], v[212:215], a[164:167]\n"
  "v_mfma_f32_16x16x32_bf16 a[196:199], v[224:227], v[216:219], a[196:199]\n"
  "s_add_u32 m0, s63, 63488\n"
  "v_mfma_f32_16x16x32_bf16 a[8:11], v[220:223], v[192:195], a[8:11]\n"
  "buffer_load_dwordx4 v239, s[36:39], s67 offen lds\n"
  "v_mfma_f32_16x16x32_bf16 a[40:43], v[220:223], v[196:199], a[40:43]\n"
  "v_perm_b32 v224, v37, v33, s71\n"
  "v_perm_b32 v225, v45, v41, s71\n"
  "v_mfma_f32_16x16x32_bf16 a[72:75], v[220:223], v[200:203], a[72:75]\n"
  "v_perm_b32 v226, v53, v49, s71\n"
  "v_perm_b32 v227, v61, v57, s71\n"
  "v_mfma_f32_16x16x32_bf16 a[104:107], v[220:223], v[204:207], a[104:107]\n"
  "s_add_u32 m0, s64, 63488\n"
  "v_mfma_f32_16x16x32_bf16 a[136:139], v[220:223], v[208:211], a[136:139]\n"
  "buffer_load_dwordx4 v239, s[36:39], s68 offen lds\n"
  "v_mfma_f32_16x16x32_bf16 a[168:171], v[220:223], v[212:215], a[168:171]\n"
  "v_mfma_f32_16x16x32_bf16 a[200:203], v[220:223], v[216:219], a[200:203]\n"
  "s_add_u32 m0, s65, 63488\n"
  "v_mfma_f32_16x16x32_bf16 a[12:15], v[224:227], v[192:195], a[12:15]\n"
  "buffer_load_dwordx4 v239, s[36:39], s69 offen lds\n"
  "v_mfma_f32_16x16x32_bf16 a[44:47], v[224:227], v[196:199], a[44:47]\n"
  "v_perm_b32 v220, v38, v34, s70\n"
  "v_perm_b32 v221, v46, v42, s70\n"
  "v_mfma_f32_16x16x32_bf16 a[76:79], v[224:227], v[200:203], a[76:79]\n"
  "s_add_u32 s61, s61, 0x40000\n"
  "v_perm_b32 v222, v54, v50, s70\n"
  "v_perm_b32 v223, v62, v58, s70\n"
  "v_mfma_f32_16x16x32_bf16 a[108:111], v[224:227], v[204:207], a[108:111]\n"
  "s_cmp_eq_u32 s61, 0x480000\n"
  "v_mfma_f32_16x16x32_bf16 a[140:143], v[224:227], v[208:211], a[140:143]\n"
  "s_cselect_b32 s61, 0, s61\n"
  "v_mfma_f32_16x16x32_bf16 a[172:175], v[224:227], v[212:215], a[172:175]\n"
  "buffer_load_dwordx4 v[128:131], v228, s[40:43], s61 offen\n"
  "v_mfma_f32_16x16x32_bf16 a[204:207], v[224:227], v[216:219], a[204:207]\n"
  "v_mfma_f32_16x16x32_bf16 a[16:19], v[220:223], v[192:195], a[16:19]\n"
  "v_mfma_f32_16x16x32_bf16 a[48:51], v[220:223], v[196:199], a[48:51]\n"
  "v_perm_b32 v224, v38, v34, s71\n"
  "v_perm_b32 v225, v46, v42, s71\n"
  "v_mfma_f32_16x16x32_bf16 a[80:83], v[220:223], v[200:203], a[80:83]\n"
  "v_perm_b32 v226, v54, v50, s71\n"
  "v_perm_b32 v227, v62, v58, s71\n"
  "v_mfma_f32_16x16x32_bf16 a[112:115], v[220:223], v[204:207], a[112:115]\n"
  "buffer_load_dwordx4 v[132:135], v229, s[40:43], s61 offen\n"
  "v_mfma_f32_16x16x32_bf16 a[144:147], v[220:223], v[208:211], a[144:147]\n"
  "v_mfma_f32_16x16x32_bf16 a[176:179], v[220:223], v[212:215], a[176:179]\n"
  "v_mfma_f32_16x16x32_bf16 a[208:211], v[220:223], v[216:219], a[208:211]\n"
  "buffer_load_dwordx4 v[136:139], v230, s[40:43], s61 offen\n"
  "v_mfma_f32_16x16x32_bf16 a[20:23], v[224:227], v[192:195], a[20:23]\n"
  "v_mfma_f32_16x16x32_bf16 a[52:55], v[224:227], v[196:199], a[52:55]\n"
  "v_perm_b32 v220, v39, v35, s70\n"
  "v_perm_b32 v221, v47, v43, s70\n"
  "v_mfma_f32_16x16x32_bf16 a[84:87], v[224:227], v[200:203], a[84:87]\n"
  "v_perm_b32 v222, v55, v51, s70\n"
  "v_perm_b32 v223, v63, v59, s70\n"
  "v_mfma_f32_16x16x32_bf16 a[116:119], v[224:227], v[204:207], a[116:119]\n"
  "v_mfma_f32_16x16x32_bf16 a[148:151], v[224:227], v[208:211], a[148:151]\n"
  "buffer_load_dwordx4 v[140:143], v231, s[40:43], s61 offen\n"
  "v_mfma_f32_16x16x32_bf16 a[180:183], v[224:227], v[212:215], a[180:183]\n"
  "v_mfma_f32_16x16x32_bf16 a[212:215], v[224:227], v[216:219], a[212:215]\n"
  "v_mfma_f32_16x16x32_bf16 a[24:27], v[220:223], v[192:195], a[24:27]\n"
  "buffer_load_dwordx4 v[144:147], v232, s[40:43], s61 offen\n"
  "v_mfma_f32_16x16x32_bf16 a[56:59], v[220:223], v[196:199], a[56:59]\n"
  "v_perm_b32 v224, v39, v35, s71\n"
  "v_perm_b32 v225, v47, v43, s71\n"
  "v_mfma_f32_16x16x32_bf16 a[88:91], v[220:223], v[200:203], a[88:91]\n"
  "v_perm_b32 v226, v55, v51, s71\n"
  "v_perm_b32 v227, v63, v59, s71\n"
  "v_mfma_f32_16x16x32_bf16 a[120:123], v[220:223], v[204:207], a[120:123]\n"
  "v_mfma_f32_16x16x32_bf16 a[152:155], v[220:223], v[208:211], a[152:155]\n"
  "buffer_load_dwordx4 v[148:151], v233, s[40:43], s61 offen\n"
  "v_mfma_f32_16x16x32_bf16 a[184:187], v[220:223], v[212:215], a[184:187]\n"
  "v_mfma_f32_16x16x32_bf16 a[216:219], v[220:223], v[216:219], a[216:219]\n"
  "v_mfma_f32_16x16x32_bf16 a[28:31], v[224:227], v[192:195], a[28:31]\n"
  "buffer_load_dwordx4 v[152:155], v234, s[40:43], s61 offen\n"
  "v_mfma_f32_16x16x32_bf16 a[60:63], v[224:227], v[196:199], a[60:63]\n"
  "s_waitcnt vmcnt(19)\n"
  "v_mfma_f32_16x16x32_bf16 a[92:95], v[224:227], v[200:203], a[92:95]\n"
  "v_perm_b32 v220, v68, v64, s70\n"
  "v_perm_b32 v221, v76, v72, s70\n"
  "v_mfma_f32_16x16x32_bf16 a[124:127], v[224:227], v[204:207], a[124:127]\n"
  "v_perm_b32 v222, v84, v80, s70\n"
  "v_perm_b32 v223, v92, v88, s70\n"
  "v_mfma_f32_16x16x32_bf16 a[156:159], v[224:227], v[208:211], a[156:159]\n"
  "v_mfma_f32_16x16x32_bf16 a[188:191], v[224:227], v[212:215], a[188:191]\n"
  "buffer_load_dwordx4 v[156:159], v235, s[40:43], s61 offen\n"
  "v_mfma_f32_16x16x32_bf16 a[220:223], v[224:227], v[216:219], a[220:223]\n"
  "s_waitcnt lgkmcnt(0)\n"
  "v_mfma_f32_16x16x32_bf16 a[0:3], v[220:223], a[224:227], a[0:3]\n"
  "ds_read_b128 v[192:195], v238 offset:14336\n"
  "ds_read_b128 v[196:199], v238 offset:16384\n"
  "v_mfma_f32_16x16x32_bf16 a[32:35], v[220:223], a[228:231], a[32:35]\n"
  "v_perm_b32 v224, v68, v64, s71\n"
  "v_perm_b32 v225, v76, v72, s71\n"
  "v_mfma_f32_16x16x32_bf16 a[64:67], v[220:223], a[232:235], a[64:67]\n"
  "s_add_u32 s61, s61, 0x40000\n"
  "v_perm_b32 v226, v84, v80, s71\n"
  "v_perm_b32 v227, v92, v88, s71\n"
  "v_mfma_f32_16x16x32_bf16 a[96:99], v[220:223], a[236:239], a[96:99]\n"
  "buffer_load_dwordx4 v[160:163], v228, s[40:43], s61 offen\n"
  "v_mfma_f32_16x16x32_bf16 a[128:131], v[220:223], a[240:243], a[128:131]\n"
  "ds_read_b128 v[200:203], v238 offset:18432\n"
  "ds_read_b128 v[204:207], v238 offset:20480\n"
  "v_mfma_f32_16x16x32_bf16 a[160:163], v[220:223], a[244:247], a[160:163]\n"
  "ds_read_b128 v[208:211], v238 offset:22528\n"
  "ds_read_b128 v[212:215], v238 offset:24576\n"
  "v_mfma_f32_16x16x32_bf16 a[192:195], v[220:223], a[248:251], a[192:195]\n"
  "buffer_load_dwordx4 v[164:167], v229, s[40:43], s61 offen\n"
  "ds_read_b128 v[216:219], v238 offset:26624\n"
  "v_mfma_f32_16x16x32_bf16 a[4:7], v[224:227], a[224:227], a[4:7]\n"
  "v_mfma_f32_16x16x32_bf16 a[36:39], v[224:227], a[228:231], a[36:39]\n"
  "v_perm_b32 v220, v69, v65, s70\n"
  "v_perm_b32 v221, v77, v73, s70\n"
  "v_mfma_f32_16x16x32_bf16 a[68:71], v[224:227], a[232:235], a[68:71]\n"
  "v_perm_b32 v222, v85, v81, s70\n"
  "v_perm_b32 v223, v93, v89, s70\n"
  "v_mfma_f32_16x16x32_bf16 a[100:103], v[224:227], a[236:239], a[100:103]\n"
  "s_waitcnt vmcnt(30)\n"
  "v_add_u32 v254, 1, v254\n"
  "v_mfma_f32_16x16x32_bf16 a[132:135], v[224:227], a[240:243], a[132:135]\n"
  "buffer_load_dwordx4 v[168:171], v230, s[40:43], s61 offen\n"
  "v_mfma_f32_16x16x32_bf16 a[164:167], v[224:227], a[244:247], a[164:167]\n"
  "v_mfma_f32_16x16x32_bf16 a[196:199], v[224:227], a[248:251], a[196:199]\n"
  "buffer_store_dword v254, v255, s[88:91], 0 offen sc1\n"
  "v_mfma_f32_16x16x32_bf16 a[8:11], v[220:223], a[224:227], a[8:11]\n"
  "buffer_load_dwordx4 v[172:175], v231, s[40:43], s61 offen\n"
  "v_mfma_f32_16x16x32_bf16 a[40:43], v[220:223], a[228:231], a[40:43]\n"
  "v_perm_b32 v224, v69, v65, s71\n"
  "v_perm_b32 v225, v77, v73, s71\n"
  "v_mfma_f32_16x16x32_bf16 a[72:75], v[220:223], a[232:235], a[72:75]\n"
  "v_perm_b32 v226, v85, v81, s71\n"
  "v_perm_b32 v227, v93, v89, s71\n"
  "v_mfma_f32_16x16x32_bf16 a[104:107], v[220:223], a[236:239], a[104:107]\n"
  "v_mfma_f32_16x16x32_bf16 a[136:139], v[220:223], a[240:243], a[136:139]\n"
  "buffer_load_dwordx4 v[176:179], v232, s[40:43], s61 offen\n"
  "v_mfma_f32_16x16x32_bf16 a[168:171], v[220:223], a[244:247], a[168:171]\n"
  "v_mfma_f32_16x16x32_bf16 a[200:203], v[220:223], a[248:251], a[200:203]\n"
  "v_mfma_f32_16x16x32_bf16 a[12:15], v[224:227], a[224:227], a[12:15]\n"
  "buffer_load_dwordx4 v[180:183], v233, s[40:43], s61 offen\n"
  "v_mfma_f32_16x16x32_bf16 a[44:47], v[224:227], a[228:231], a[44:47]\n"
  "v_perm_b32 v220, v70, v66, s70\n"
  "v_perm_b32 v221, v78, v74, s70\n"
  "v_mfma_f32_16x16x32_bf16 a[76:79], v[224:227], a[232:235], a[76:79]\n"
  "v_perm_b32 v222, v86, v82, s70\n"
  "v_perm_b32 v223, v94, v90, s70\n"
  "v_mfma_f32_16x16x32_bf16 a[108:111], v[224:227], a[236:239], a[108:111]\n"
  "v_mfma_f32_16x16x32_bf16 a[140:143], v[224:227], a[240:243], a[140:143]\n"
  "v_mfma_f32_16x16x32_bf16 a[172:175], v[224:227], a[244:247], a[172:175]\n"
  "buffer_load_dwordx4 v[184:187], v234, s[40:43], s61 offen\n"
  "v_mfma_f32_16x16x32_bf16 a[204:207], v[224:227], a[248:251], a[204:207]\n"
  "v_mfma_f32_16x16x32_bf16 a[16:19], v[220:223], a[224:227], a[16:19]\n"
  "v_mfma_f32_16x16x32_bf16 a[48:51], v[220:223], a[228:231], a[48:51]\n"
  "v_perm_b32 v224, v70, v66, s71\n"
  "v_perm_b32 v225, v78, v74, s71\n"
  "v_mfma_f32_16x16x32_bf16 a[80:83], v[220:223], a[232:235], a[80:83]\n"
  "v_perm_b32 v226, v86, v82, s71\n"
  "v_perm_b32 v227, v94, v90, s71\n"
  "v_mfma_f32_16x16x32_bf16 a[112:115], v[220:223], a[236:239], a[112:115]\n"
  "buffer_load_dwordx4 v[188:191], v235, s[40:43], s61 offen\n"
  "v_mfma_f32_16x16x32_bf16 a[144:147], v[220:223], a[240:243], a[144:147]\n"
  "s_add_u32 s73, s73, 128\n"
  "s_cmp_eq_u32 s73, 1152\n"
  "s_cselect_b32 s73, 0, s73\n"
  "v_add_u32 v239, s73, v236\n"
  "v_mfma_f32_16x16x32_bf16 a[176:179], v[220:223], a[244:247], a[176:179]\n"
  "s_add_u32 m0, s62, 77824\n"
  "v_mfma_f32_16x16x32_bf16 a[208:211], v[220:223], a[248:251], a[208:211]\n"
  "buffer_load_dwordx4 v239, s[36:39], s66 offen lds\n"
  "v_mfma_f32_16x16x32_bf16 a[20:23], v[224:227], a[224:227], a[20:23]\n"
  "v_mfma_f32_16x16x32_bf16 a[52:55], v[224:227], a[228:231], a[52:55]\n"
  "v_perm_b32 v220, v71, v67, s70\n"
  "v_perm_b32 v221, v79, v75, s70\n"
  "v_mfma_f32_16x16x32_bf16 a[84:87], v[224:227], a[232:235], a[84:87]\n"
  "v_perm_b32 v222, v87, v83, s70\n"
  "v_perm_b32 v223, v95, v91, s70\n"
  "v_mfma_f32_16x16x32_bf16 a[116:119], v[224:227], a[236:239], a[116:119]\n"
  "s_add_u32 m0, s63, 77824\n"
  "v_mfma_f32_16x16x32_bf16 a[148:151], v[224:227], a[240:243], a[148:151]\n"
  "buffer_load_dwordx4 v239, s[36:39], s67 offen lds\n"
  "v_mfma_f32_16x16x32_bf16 a[180:183], v[224:227], a[244:247], a[180:183]\n"
  "v_mfma_f32_16x16x32_bf16 a[212:215], v[224:227], a[248:251], a[212:215]\n"
  "s_add_u32 m0, s64, 77824\n"
  "v_mfma_f32_16x16x32_bf16 a[24:27], v[220:223], a[224:227], a[24:27]\n"
  "buffer_load_dwordx4 v239, s[36:39], s68 offen lds\n"
  "v_mfma_f32_16x16x32_bf16 a[56:59], v[220:223], a[228:231], a[56:59]\n"
  "v_perm_b32 v224, v71, v67, s71\n"
  "v_perm_b32 v225, v79, v75, s71\n"
  "v_mfma_f32_16x16x32_bf16 a[88:91], v[220:223], a[232:235], a[88:91]\n"
  "v_perm_b32 v226, v87, v83, s71\n"
  "v_perm_b32 v227, v95, v91, s71\n"
  "v_mfma_f32_16x16x32_bf16 a[120:123], v[220:223], a[236:239], a[120:123]\n"
  "s_add_u32 m0, s65, 77824\n"
  "v_mfma_f32_16x16x32_bf16 a[152:155], v[220:223], a[240:243], a[152:155]\n"
  "buffer_load_dwordx4 v239, s[36:39], s69 offen lds\n"
  "s_add_u32 s61, s61, 0x40000\n"
  "v_mfma_f32_16x16x32_bf16 a[184:187], v[220:223], a[244:247], a[184:187]\n"
  "s_cmp_eq_u32 s61, 0x480000\n"
  "v_mfma_f32_16x16x32_bf16 a[216:219], v[220:223], a[248:251], a[216:219]\n"
  "s_cselect_b32 s61, 0, s61\n"
  "v_mfma_f32_16x16x32_bf16 a[28:31], v[224:227], a[224:227], a[28:31]\n"
  "buffer_load_dwordx4 v[0:3], v228, s[40:43], s61 offen\n"
  "v_mfma_f32_16x16x32_bf16 a[60:63], v[224:227], a[228:231], a[60:63]\n"
  "s_waitcnt vmcnt(26)\n"
  "v_mfma_f32_16x16x32_bf16 a[92:95], v[224:227], a[232:235], a[92:95]\n"
  "v_perm_b32 v220, v100, v96, s70\n"
  "v_perm_b32 v221, v108, v104, s70\n"
  "v_mfma_f32_16x16x32_bf16 a[124:127], v[224:227], a[236:239], a[124:127]\n"
  "v_perm_b32 v222, v116, v112, s70\n"
  "v_perm_b32 v223, v124, v120, s70\n"
  "v_mfma_f32_16x16x32_bf16 a[156:159], v[224:227], a[240:243], a[156:159]\n"
  "v_mfma_f32_16x16x32_bf16 a[188:191], v[224:227], a[244:247], a[188:191]\n"
  "buffer_load_dwordx4 v[4:7], v229, s[40:43], s61 offen\n"
  "v_mfma_f32_16x16x32_bf16 a[220:223], v[224:227], a[248:251], a[220:223]\n"
  "s_waitcnt lgkmcnt(0)\n"
  "s_waitcnt vmcnt(23)\n"
  "s_barrier\n"
  "v_mfma_f32_16x16x32_bf16 a[0:3], v[220:223], v[192:195], a[0:3]\n"
  "ds_read_b128 a[224:227], v237 offset:28672\n"
  "ds_read_b128 a[228:231], v237 offset:30720\n"
  "v_mfma_f32_16x16x32_bf16 a[32:35], v[220:223], v[196:199], a[32:35]\n"
  "v_perm_b32 v224, v100, v96, s71\n"
  "v_perm_b32 v225, v108, v104, s71\n"
  "v_mfma_f32_16x16x32_bf16 a[64:67], v[220:223], v[200:203], a[64:67]\n"
  "v_perm_b32 v226, v116, v112, s71\n"
  "v_perm_b32 v227, v124, v120, s71\n"
  "v_mfma_f32_16x16x32_bf16 a[96:99], v[220:223], v[204:207], a[96:99]\n"
  "buffer_load_dwordx4 v[8:11], v230, s[40:43], s61 offen\n"
  "v_mfma_f32_16x16x32_bf16 a[128:131], v[220:223], v[208:211], a[128:131]\n"
  "ds_read_b128 a[232:235], v237 offset:32768\n"
  "ds_read_b128 a[236:239], v237 offset:34816\n"
  "v_mfma_f32_16x16x32_bf16 a[160:163], v[220:223], v[212:215], a[160:163]\n"
  "ds_read_b128 a[240:243], v237 offset:36864\n"
  "ds_read_b128 a[244:247], v237 offset:38912\n"
  "v_mfma_f32_16x16x32_bf16 a[192:195], v[220:223], v[216:219], a[192:195]\n"
  "buffer_load_dwordx4 v[12:15], v231, s[40:43], s61 offen\n"
  "ds_read_b128 a[248:251], v237 offset:40960\n"
  "v_mfma_f32_16x16x32_bf16 a[4:7], v[224:227], v[192:195], a[4:7]\n"
  "v_mfma_f32_16x16x32_bf16 a[36:39], v[224:227], v[196:199], a[36:39]\n"
  "v_perm_b32 v220, v101, v97, s70\n"
  "v_perm_b32 v221, v109, v105, s70\n"
  "v_mfma_f32_16x16x32_bf16 a[68:71], v[224:227], v[200:203], a[68:71]\n"
  "v_perm_b32 v222, v117, v113, s70\n"
  "v_perm_b32 v223, v125, v121, s70\n"
  "v_mfma_f32_16x16x32_bf16 a[100:103], v[224:227], v[204:207], a[100:103]\n"
  "v_mfma_f32_16x16x32_bf16 a[132:135], v[224:227], v[208:211], a[132:135]\n"
  "buffer_load_dwordx4 v[16:19], v232, s[40:43], s61 offen\n"
  "v_mfma_f32_16x16x32_bf16 a[164:167], v[224:227], v[212:215], a[164:167]\n"
  "v_mfma_f32_16x16x32_bf16 a[196:199], v[224:227], v[216:219], a[196:199]\n"
  "v_mfma_f32_16x16x32_bf16 a[8:11], v[220:223], v[192:195], a[8:11]\n"
  "buffer_load_dwordx4 v[20:23], v233, s[40:43], s61 offen\n"
  "v_mfma_f32_16x16x32_bf16 a[40:43], v[220:223], v[196:199], a[40:43]\n"
  "v_perm_b32 v224, v101, v97, s71\n"
  "v_perm_b32 v225, v109, v105, s71\n"
  "v_mfma_f32_16x16x32_bf16 a[72:75], v[220:223], v[200:203], a[72:75]\n"
  "v_perm_b32 v226, v117, v113, s71\n"
  "v_perm_b32 v227, v125, v121, s71\n"
  "v_mfma_f32_16x16x32_bf16 a[104:107], v[220:223], v[204:207], a[104:107]\n"
  "v_mfma_f32_16x16x32_bf16 a[136:139], v[220:223], v[208:211], a[136:139]\n"
  "buffer_load_dwordx4 v[24:27], v234, s[40:43], s61 offen\n"
  "v_mfma_f32_16x16x32_bf16 a[168:171], v[220:223], v[212:215], a[168:171]\n"
  "v_mfma_f32_16x16x32_bf16 a[200:203], v[220:223], v[216:219], a[200:203]\n"
  "v_mfma_f32_16x16x32_bf16 a[12:15], v[224:227], v[192:195], a[12:15]\n"
  "buffer_load_dwordx4 v[28:31], v235, s[40:43], s61 offen\n"
  "v_mfma_f32_16x16x32_bf16 a[44:47], v[224:227], v[196:199], a[44:47]\n"
  "v_perm_b32 v220, v102, v98, s70\n"
  "v_perm_b32 v221, v110, v106, s70\n"
  "v_mfma_f32_16x16x32_bf16 a[76:79], v[224:227], v[200:203], a[76:79]\n"
  "v_perm_b32 v222, v118, v114, s70\n"
  "v_perm_b32 v223, v126, v122, s70\n"
  "v_mfma_f32_16x16x32_bf16 a[108:111], v[224:227], v[204:207], a[108:111]\n"
  "s_add_u32 s73, s73, 128\n"
  "s_cmp_eq_u32 s73, 1152\n"
  "s_cselect_b32 s73, 0, s73\n"
  "v_add_u32 v239, s73, v236\n"
  "v_mfma_f32_16x16x32_bf16 a[140:143], v[224:227], v[208:211], a[140:143]\n"
  "s_add_u32 m0, s62, 92160\n"
  "v_mfma_f32_16x16x32_bf16 a[172:175], v[224:227], v[212:215], a[172:175]\n"
  "buffer_load_dwordx4 v239, s[36:39], s66 offen lds\n"
  "v_mfma_f32_16x16x32_bf16 a[204:207], v[224:227], v[216:219], a[204:207]\n"
  "v_mfma_f32_16x16x32_bf16 a[16:19], v[220:223], v[192:195], a[16:19]\n"
  "v_mfma_f32_16x16x32_bf16 a[48:51], v[220:223], v[196:199], a[48:51]\n"
  "v_perm_b32 v224, v102, v98, s71\n"
  "v_perm_b32 v225, v110, v106, s71\n"
  "v_mfma_f32_16x16x32_bf16 a[80:83], v[220:223], v[200:203], a[80:83]\n"
  "s_add_u32 m0, s63, 92160\n"
  "v_perm_b32 v226, v118, v114, s71\n"
  "v_perm_b32 v227, v126, v122, s71\n"
  "v_mfma_f32_16x16x32_bf16 a[112:115], v[220:223], v[204:207], a[112:115]\n"
  "buffer_load_dwordx4 v239, s[36:39], s67 offen lds\n"
  "v_mfma_f32_16x16x32_bf16 a[144:147], v[220:223], v[208:211], a[144:147]\n"
  "v_mfma_f32_16x16x32_bf16 a[176:179], v[220:223], v[212:215], a[176:179]\n"
  "s_add_u32 m0, s64, 92160\n"
  "v_mfma_f32_16x16x32_bf16 a[208:211], v[220:223], v[216:219], a[208:211]\n"
  "buffer_load_dwordx4 v239, s[36:39], s68 offen lds\n"
  "v_mfma_f32_16x16x32_bf16 a[20:23], v[224:227], v[192:195], a[20:23]\n"
  "v_mfma_f32_16x16x32_bf16 a[52:55], v[224:227], v[196:199], a[52:55]\n"
  "v_perm_b32 v220, v103, v99, s70\n"
  "v_perm_b32 v221, v111, v107, s70\n"
  "v_mfma_f32_16x16x32_bf16 a[84:87], v[224:227], v[200:203], a[84:87]\n"
  "v_perm_b32 v222, v119, v115, s70\n"
  "v_perm_b32 v223, v127, v123, s70\n"
  "v_mfma_f32_16x16x32_bf16 a[116:119], v[224:227], v[204:207], a[116:119]\n"
  "s_add_u32 m0, s65, 92160\n"
  "v_mfma_f32_16x16x32_bf16 a[148:151], v[224:227], v[208:211], a[148:151]\n"
  "buffer_load_dwordx4 v239, s[36:39], s69 offen lds\n"
  "v_mfma_f32_16x16x32_bf16 a[180:183], v[224:227], v[212:215], a[180:183]\n"
  "v_mfma_f32_16x16x32_bf16 a[212:215], v[224:227], v[216:219], a[212:215]\n"
  "s_add_u32 s61, s61, 0x40000\n"
  "v_mfma_f32_16x16x32_bf16 a[24:27], v[220:223], v[192:195], a[24:27]\n"
  "buffer_load_dwordx4 v[32:35], v228, s[40:43], s61 offen\n"
  "v_mfma_f32_16x16x32_bf16 a[56:59], v[220:223], v[196:199], a[56:59]\n"
  "v_perm_b32 v224, v103, v99, s71\n"
  "v_perm_b32 v225, v111, v107, s71\n"
  "v_mfma_f32_16x16x32_bf16 a[88:91], v[220:223], v[200:203], a[88:91]\n"
  "v_perm_b32 v226, v119, v115, s71\n"
  "v_perm_b32 v227, v127, v123, s71\n"
  "v_mfma_f32_16x16x32_bf16 a[120:123], v[220:223], v[204:207], a[120:123]\n"
  "v_mfma_f32_16x16x32_bf16 a[152:155], v[220:223], v[208:211], a[152:155]\n"
  "buffer_load_dwordx4 v[36:39], v229, s[40:43], s61 offen\n"
  "v_mfma_f32_16x16x32_bf16 a[184:187], v[220:223], v[212:215], a[184:187]\n"
  "v_mfma_f32_16x16x32_bf16 a[216:219], v[220:223], v[216:219], a[216:219]\n"
  "v_mfma_f32_16x16x32_bf16 a[28:31], v[224:227], v[192:195], a[28:31]\n"
  "buffer_load_dwordx4 v[40:43], v230, s[40:43], s61 offen\n"
  "v_mfma_f32_16x16x32_bf16 a[60:63], v[224:227], v[196:199], a[60:63]\n"
  "s_waitcnt vmcnt(28)\n"
  "v_mfma_f32_16x16x32_bf16 a[92:95], v[224:227], v[200:203], a[92:95]\n"
  "v_perm_b32 v220, v132, v128, s70\n"
  "v_perm_b32 v221, v140, v136, s70\n"
  "v_mfma_f32_16x16x32_bf16 a[124:127], v[224:227], v[204:207], a[124:127]\n"
  "v_perm_b32 v222, v148, v144, s70\n"
  "v_perm_b32 v223, v156, v152, s70\n"
  "v_mfma_f32_16x16x32_bf16 a[156:159], v[224:227], v[208:211], a[156:159]\n"
  "v_mfma_f32_16x16x32_bf16 a[188:191], v[224:227], v[212:215], a[188:191]\n"
  "buffer_load_dwordx4 v[44:47], v231, s[40:43], s61 offen\n"
  "v_mfma_f32_16x16x32_bf16 a[220:223], v[224:227], v[216:219], a[220:223]\n"
  "s_waitcnt lgkmcnt(0)\n"
  "v_mfma_f32_16x16x32_bf16 a[0:3], v[220:223], a[224:227], a[0:3]\n"
  "ds_read_b128 v[192:195], v238 offset:28672\n"
  "ds_read_b128 v[196:199], v238 offset:30720\n"
  "v_mfma_f32_16x16x32_bf16 a[32:35], v[220:223], a[228:231], a[32:35]\n"
  "v_perm_b32 v224, v132, v128, s71\n"
  "v_perm_b32 v225, v140, v136, s71\n"
  "v_mfma_f32_16x16x32_bf16 a[64:67], v[220:223], a[232:235], a[64:67]\n"
  "v_perm_b32 v226, v148, v144, s71\n"
  "v_perm_b32 v227, v156, v152, s71\n"
  "v_mfma_f32_16x16x32_bf16 a[96:99], v[220:223], a[236:239], a[96:99]\n"
  "buffer_load_dwordx4 v[48:51], v232, s[40:43], s61 offen\n"
  "v_mfma_f32_16x16x32_bf16 a[128:131], v[220:223], a[240:243], a[128:131]\n"
  "ds_read_b128 v[200:203], v238 offset:32768\n"
  "ds_read_b128 v[204:207], v238 offset:34816\n"
  "v_mfma_f32_16x16x32_bf16 a[160:163], v[220:223], a[244:247], a[160:163]\n"
  "ds_read_b128 v[208:211], v238 offset:36864\n"
  "ds_read_b128 v[212:215], v238 offset:38912\n"
  "v_mfma_f32_16x16x32_bf16 a[192:195], v[220:223], a[248:251], a[192:195]\n"
  "buffer_load_dwordx4 v[52:55], v233, s[40:43], s61 offen\n"
  "ds_read_b128 v[216:219], v238 offset:40960\n"
  "v_mfma_f32_16x16x32_bf16 a[4:7], v[224:227], a[224:227], a[4:7]\n"
  "v_mfma_f32_16x16x32_bf16 a[36:39], v[224:227], a[228:231], a[36:39]\n"
  "v_perm_b32 v220, v133, v129, s70\n"
  "v_perm_b32 v221, v141, v137, s70\n"
  "v_mfma_f32_16x16x32_bf16 a[68:71], v[224:227], a[232:235], a[68:71]\n"
  "v_perm_b32 v222, v149, v145, s70\n"
  "v_perm_b32 v223, v157, v153, s70\n"
  "v_mfma_f32_16x16x32_bf16 a[100:103], v[224:227], a[236:239], a[100:103]\n"
  "v_mfma_f32_16x16x32_bf16 a[132:135], v[224:227], a[240:243], a[132:135]\n"
  "buffer_load_dwordx4 v[56:59], v234, s[40:43], s61 offen\n"
  "v_mfma_f32_16x16x32_bf16 a[164:167], v[224:227], a[244:247], a[164:167]\n"
  "v_mfma_f32_16x16x32_bf16 a[196:199], v[224:227], a[248:251], a[196:199]\n"
  "v_mfma_f32_16x16x32_bf16 a[8:11], v[220:223], a[224:227], a[8:11]\n"
  "buffer_load_dwordx4 v[60:63], v235, s[40:43], s61 offen\n"
  "v_mfma_f32_16x16x32_bf16 a[40:43], v[220:223], a[228:231], a[40:43]\n"
  "v_perm_b32 v224, v133, v129, s71\n"
  "v_perm_b32 v225, v141, v137, s71\n"
  "v_mfma_f32_16x16x32_bf16 a[72:75], v[220:223], a[232:235], a[72:75]\n"
  "v_perm_b32 v226, v149, v145, s71\n"
  "v_perm_b32 v227, v157, v153, s71\n"
  "s_add_u32 s73, s73, 128\n"
  "s_cmp_eq_u32 s73, 1152\n"
  "s_cselect_b32 s73, 0, s73\n"
  "v_add_u32 v239, s73, v236\n"
  "v_mfma_f32_16x16x32_bf16 a[104:107], v[220:223], a[236:239], a[104:107]\n"
  "s_add_u32 m0, s62, 106496\n"
  "v_mfma_f32_16x16x32_bf16 a[136:139], v[220:223], a[240:243], a[136:139]\n"
  "buffer_load_dwordx4 v239, s[36:39], s66 offen lds\n"
  "v_mfma_f32_16x16x32_bf16 a[168:171], v[220:223], a[244:247], a[168:171]\n"
  "v_mfma_f32_16x16x32_bf16 a[200:203], v[220:223], a[248:251], a[200:203]\n"
  "s_add_u32 m0, s63, 106496\n"
  "v_mfma_f32_16x16x32_bf16 a[12:15], v[224:227], a[224:227], a[12:15]\n"
  "buffer_load_dwordx4 v239, s[36:39], s67 offen lds\n"
  "v_mfma_f32_16x16x32_bf16 a[44:47], v[224:227], a[228:231], a[44:47]\n"
  "v_perm_b32 v220, v134, v130, s70\n"
  "v_perm_b32 v221, v142, v138, s70\n"
  "v_mfma_f32_16x16x32_bf16 a[76:79], v[224:227], a[232:235], a[76:79]\n"
  "v_perm_b32 v222, v150, v146, s70\n"
  "v_perm_b32 v223, v158, v154, s70\n"
  "v_mfma_f32_16x16x32_bf16 a[108:111], v[224:227], a[236:239], a[108:111]\n"
  "v_mfma_f32_16x16x32_bf16 a[140:143], v[224:227], a[240:243], a[140:143]\n"
  "s_add_u32 m0, s64, 106496\n"
  "v_mfma_f32_16x16x32_bf16 a[172:175], v[224:227], a[244:247], a[172:175]\n"
  "buffer_load_dwordx4 v239, s[36:39], s68 offen lds\n"
  "v_mfma_f32_16x16x32_bf16 a[204:207], v[224:227], a[248:251], a[204:207]\n"
  "v_mfma_f32_16x16x32_bf16 a[16:19], v[220:223], a[224:227], a[16:19]\n"
  "v_mfma_f32_16x16x32_bf16 a[48:51], v[220:223], a[228:231], a[48:51]\n"
  "v_perm_b32 v224, v134, v130, s71\n"
  "v_perm_b32 v225, v142, v138, s71\n"
  "v_mfma_f32_16x16x32_bf16 a[80:83], v[220:223], a[232:235], a[80:83]\n"
  "s_add_u32 m0, s65, 106496\n"
  "v_perm_b32 v226, v150, v146, s71\n"
  "v_perm_b32 v227, v158, v154, s71\n"
  "v_mfma_f32_16x16x32_bf16 a[112:115], v[220:223], a[236:239], a[112:115]\n"
  "buffer_load_dwordx4 v239, s[36:39], s69 offen lds\n"
  "s_add_u32 s61, s61, 0x40000\n"
  "v_mfma_f32_16x16x32_bf16 a[144:147], v[220:223], a[240:243], a[144:147]\n"
  "s_cmp_eq_u32 s61, 0x480000\n"
  "v_mfma_f32_16x16x32_bf16 a[176:179], v[220:223], a[244:247], a[176:179]\n"
  "s_cselect_b32 s61, 0, s61\n"
  "v_mfma_f32_16x16x32_bf16 a[208:211], v[220:223], a[248:251], a[208:211]\n"
  "buffer_load_dwordx4 v[64:67], v228, s[40:43], s61 offen\n"
  "v_mfma_f32_16x16x32_bf16 a[20:23], v[224:227], a[224:227], a[20:23]\n"
  "v_mfma_f32_16x16x32_bf16 a[52:55], v[224:227], a[228:231], a[52:55]\n"
  "v_perm_b32 v220, v135, v131, s70\n"
  "v_perm_b32 v221, v143, v139, s70\n"
  "v_mfma_f32_16x16x32_bf16 a[84:87], v[224:227], a[232:235], a[84:87]\n"
  "v_perm_b32 v222, v151, v147, s70\n"
  "v_perm_b32 v223, v159, v155, s70\n"
  "v_mfma_f32_16x16x32_bf16 a[116:119], v[224:227], a[236:239], a[116:119]\n"
  "v_mfma_f32_16x16x32_bf16 a[148:151], v[224:227], a[240:243], a[148:151]\n"
  "buffer_load_dwordx4 v[68:71], v229, s[40:43], s61 offen\n"
  "v_mfma_f32_16x16x32_bf16 a[180:183], v[224:227], a[244:247], a[180:183]\n"
  "v_mfma_f32_16x16x32_bf16 a[212:215], v[224:227], a[248:251], a[212:215]\n"
  "v_mfma_f32_16x16x32_bf16 a[24:27], v[220:223], a[224:227], a[24:27]\n"
  "buffer_load_dwordx4 v[72:75], v230, s[40:43], s61 offen\n"
  "v_mfma_f32_16x16x32_bf16 a[56:59], v[220:223], a[228:231], a[56:59]\n"
  "v_perm_b32 v224, v135, v131, s71\n"
  "v_perm_b32 v225, v143, v139, s71\n"
  "v_mfma_f32_16x16x32_bf16 a[88:91], v[220:223], a[232:235], a[88:91]\n"
  "v_perm_b32 v226, v151, v147, s71\n"
  "v_perm_b32 v227, v159, v155, s71\n"
  "v_mfma_f32_16x16x32_bf16 a[120:123], v[220:223], a[236:239], a[120:123]\n"
  "v_mfma_f32_16x16x32_bf16 a[152:155], v[220:223], a[240:243], a[152:155]\n"
  "buffer_load_dwordx4 v[76:79], v231, s[40:43], s61 offen\n"
  "v_mfma_f32_16x16x32_bf16 a[184:187], v[220:223], a[244:247], a[184:187]\n"
  "v_mfma_f32_16x16x32_bf16 a[216:219], v[220:223], a[248:251], a[216:219]\n"
  "v_mfma_f32_16x16x32_bf16 a[28:31], v[224:227], a[224:227], a[28:31]\n"
  "buffer_load_dwordx4 v[80:83], v232, s[40:43], s61 offen\n"
  "v_mfma_f32_16x16x32_bf16 a[60:63], v[224:227], a[228:231], a[60:63]\n"
  "s_waitcnt vmcnt(33)\n"
  "v_mfma_f32_16x16x32_bf16 a[92:95], v[224:227], a[232:235], a[92:95]\n"
  "v_perm_b32 v220, v164, v160, s70\n"
  "v_perm_b32 v221, v172, v168, s70\n"
  "v_mfma_f32_16x16x32_bf16 a[124:127], v[224:227], a[236:239], a[124:127]\n"
  "v_perm_b32 v222, v180, v176, s70\n"
  "v_perm_b32 v223, v188, v184, s70\n"
  "v_mfma_f32_16x16x32_bf16 a[156:159], v[224:227], a[240:243], a[156:159]\n"
  "v_mfma_f32_16x16x32_bf16 a[188:191], v[224:227], a[244:247], a[188:191]\n"
  "buffer_load_dwordx4 v[84:87], v233, s[40:43], s61 offen\n"
  "v_mfma_f32_16x16x32_bf16 a[220:223], v[224:227], a[248:251], a[220:223]\n"
  "s_waitcnt lgkmcnt(0)\n"
  "s_waitcnt vmcnt(30)\n"
  "s_barrier\n"
  "v_add_u32 v237, 43008, v237\n"
  "v_mfma_f32_16x16x32_bf16 a[0:3], v[220:223], v[192:195], a[0:3]\n"
  "ds_read_b128 a[224:227], v237 offset:0\n"
  "ds_read_b128 a[228:231], v237 offset:2048\n"
  "v_mfma_f32_16x16x32_bf16 a[32:35], v[220:223], v[196:199], a[32:35]\n"
  "v_perm_b32 v224, v164, v160, s71\n"
  "v_perm_b32 v225, v172, v168, s71\n"
  "v_mfma_f32_16x16x32_bf16 a[64:67], v[220:223], v[200:203], a[64:67]\n"
  "v_perm_b32 v226, v180, v176, s71\n"
  "v_perm_b32 v227, v188, v184, s71\n"
  "v_mfma_f32_16x16x32_bf16 a[96:99], v[220:223], v[204:207], a[96:99]\n"
  "buffer_load_dwordx4 v[88:91], v234, s[40:43], s61 offen\n"
  "v_mfma_f32_16x16x32_bf16 a[128:131], v[220:223], v[208:211], a[128:131]\n"
  "ds_read_b128 a[232:235], v237 offset:4096\n"
  "ds_read_b128 a[236:239], v237 offset:6144\n"
  "v_mfma_f32_16x16x32_bf16 a[160:163], v[220:223], v[212:215], a[160:163]\n"
  "ds_read_b128 a[240:243], v237 offset:8192\n"
  "ds_read_b128 a[244:247], v237 offset:10240\n"
  "v_mfma_f32_16x16x32_bf16 a[192:195], v[220:223], v[216:219], a[192:195]\n"
  "buffer_load_dwordx4 v[92:95], v235, s[40:43], s61 offen\n"
  "ds_read_b128 a[248:251], v237 offset:12288\n"
  "v_mfma_f32_16x16x32_bf16 a[4:7], v[224:227], v[192:195], a[4:7]\n"
  "v_mfma_f32_16x16x32_bf16 a[36:39], v[224:227], v[196:199], a[36:39]\n"
  "v_perm_b32 v220, v165, v161, s70\n"
  "v_perm_b32 v221, v173, v169, s70\n"
  "v_mfma_f32_16x16x32_bf16 a[68:71], v[224:227], v[200:203], a[68:71]\n"
  "v_perm_b32 v222, v181, v177, s70\n"
  "v_perm_b32 v223, v189, v185, s70\n"
  "s_add_u32 s73, s73, 128\n"
  "s_cmp_eq_u32 s73, 1152\n"
  "s_cselect_b32 s73, 0, s73\n"
  "v_add_u32 v239, s73, v236\n"
  "v_mfma_f32_16x16x32_bf16 a[100:103], v[224:227], v[204:207], a[100:103]\n"
  "s_add_u32 m0, s62, 120832\n"
  "v_mfma_f32_16x16x32_bf16 a[132:135], v[224:227], v[208:211], a[132:135]\n"
  "buffer_load_dwordx4 v239, s[36:39], s66 offen lds\n"
  "v_mfma_f32_16x16x32_bf16 a[164:167], v[224:227], v[212:215], a[164:167]\n"
  "v_mfma_f32_16x16x32_bf16 a[196:199], v[224:227], v[216:219], a[196:199]\n"
  "s_add_u32 m0, s63, 120832\n"
  "v_mfma_f32_16x16x32_bf16 a[8:11], v[220:223], v[192:195], a[8:11]\n"
  "buffer_load_dwordx4 v239, s[36:39], s67 offen lds\n"
  "v_mfma_f32_16x16x32_bf16 a[40:43], v[220:223], v[196:199], a[40:43]\n"
  "v_perm_b32 v224, v165, v161, s71\n"
  "v_perm_b32 v225, v173, v169, s71\n"
  "v_mfma_f32_16x16x32_bf16 a[72:75], v[220:223], v[200:203], a[72:75]\n"
  "v_perm_b32 v226, v181, v177, s71\n"
  "v_perm_b32 v227, v189, v185, s71\n"
  "v_mfma_f32_16x16x32_bf16 a[104:107], v[220:223], v[204:207], a[104:107]\n"
  "s_add_u32 m0, s64, 120832\n"
  "v_mfma_f32_16x16x32_bf16 a[136:139], v[220:223], v[208:211], a[136:139]\n"
  "buffer_load_dwordx4 v239, s[36:39], s68 offen lds\n"
  "v_mfma_f32_16x16x32_bf16 a[168:171], v[220:223], v[212:215], a[168:171]\n"
  "v_mfma_f32_16x16x32_bf16 a[200:203], v[220:223], v[216:219], a[200:203]\n"
  "s_add_u32 m0, s65, 120832\n"
  "v_mfma_f32_16x16x32_bf16 a[12:15], v[224:227], v[192:195], a[12:15]\n"
  "buffer_load_dwordx4 v239, s[36:39], s69 offen lds\n"
  "v_mfma_f32_16x16x32_bf16 a[44:47], v[224:227], v[196:199], a[44:47]\n"
  "v_perm_b32 v220, v166, v162, s70\n"
  "v_perm_b32 v221, v174, v170, s70\n"
  "v_mfma_f32_16x16x32_bf16 a[76:79], v[224:227], v[200:203], a[76:79]\n"
  "v_perm_b32 v222, v182, v178, s70\n"
  "v_perm_b32 v223, v190, v186, s70\n"
  "v_mfma_f32_16x16x32_bf16 a[108:111], v[224:227], v[204:207], a[108:111]\n"
  "v_mfma_f32_16x16x32_bf16 a[140:143], v[224:227], v[208:211], a[140:143]\n"
  "s_add_u32 s61, s61, 0x40000\n"
  "v_mfma_f32_16x16x32_bf16 a[172:175], v[224:227], v[212:215], a[172:175]\n"
  "buffer_load_dwordx4 v[96:99], v228, s[40:43], s61 offen\n"
  "v_mfma_f32_16x16x32_bf16 a[204:207], v[224:227], v[216:219], a[204:207]\n"
  "v_mfma_f32_16x16x32_bf16 a[16:19], v[220:223], v[192:195], a[16:19]\n"
  "v_mfma_f32_16x16x32_bf16 a[48:51], v[220:223], v[196:199], a[48:51]\n"
  "v_perm_b32 v224, v166, v162, s71\n"
  "v_perm_b32 v225, v174, v170, s71\n"
  "v_mfma_f32_16x16x32_bf16 a[80:83], v[220:223], v[200:203], a[80:83]\n"
  "v_perm_b32 v226, v182, v178, s71\n"
  "v_perm_b32 v227, v190, v186, s71\n"
  "v_mfma_f32_16x16x32_bf16 a[112:115], v[220:223], v[204:207], a[112:115]\n"
  "buffer_load_dwordx4 v[100:103], v229, s[40:43], s61 offen\n"
  "v_mfma_f32_16x16x32_bf16 a[144:147], v[220:223], v[208:211], a[144:147]\n"
  "v_mfma_f32_16x16x32_bf16 a[176:179], v[220:223], v[212:215], a[176:179]\n"
  "v_mfma_f32_16x16x32_bf16 a[208:211], v[220:223], v[216:219], a[208:211]\n"
  "buffer_load_dwordx4 v[104:107], v230, s[40:43], s61 offen\n"
  "v_mfma_f32_16x16x32_bf16 a[20:23], v[224:227], v[192:195], a[20:23]\n"
  "v_mfma_f32_16x16x32_bf16 a[52:55], v[224:227], v[196:199], a[52:55]\n"
  "v_perm_b32 v220, v167, v163, s70\n"
  "v_perm_b32 v221, v175, v171, s70\n"
  "v_mfma_f32_16x16x32_bf16 a[84:87], v[224:227], v[200:203], a[84:87]\n"
  "v_perm_b32 v222, v183, v179, s70\n"
  "v_perm_b32 v223, v191, v187, s70\n"
  "v_mfma_f32_16x16x32_bf16 a[116:119], v[224:227], v[204:207], a[116:119]\n"
  "v_mfma_f32_16x16x32_bf16 a[148:151], v[224:227], v[208:211], a[148:151]\n"
  "buffer_load_dwordx4 v[108:111], v231, s[40:43], s61 offen\n"
  "v_mfma_f32_16x16x32_bf16 a[180:183], v[224:227], v[212:215], a[180:183]\n"
  "v_mfma_f32_16x16x32_bf16 a[212:215], v[224:227], v[216:219], a[212:215]\n"
  "v_mfma_f32_16x16x32_bf16 a[24:27], v[220:223], v[192:195], a[24:27]\n"
  "buffer_load_dwordx4 v[112:115], v232, s[40:43], s61 offen\n"
  "v_mfma_f32_16x16x32_bf16 a[56:59], v[220:223], v[196:199], a[56:59]\n"
  "v_perm_b32 v224, v167, v163, s71\n"
  "v_perm_b32 v225, v175, v171, s71\n"
  "v_mfma_f32_16x16x32_bf16 a[88:91], v[220:223], v[200:203], a[88:91]\n"
  "v_perm_b32 v226, v183, v179, s71\n"
  "v_perm_b32 v227, v191, v187, s71\n"
  "v_mfma_f32_16x16x32_bf16 a[120:123], v[220:223], v[204:207], a[120:123]\n"
  "v_mfma_f32_16x16x32_bf16 a[152:155], v[220:223], v[208:211], a[152:155]\n"
  "buffer_load_dwordx4 v[116:119], v233, s[40:43], s61 offen\n"
  "v_mfma_f32_16x16x32_bf16 a[184:187], v[220:223], v[212:215], a[184:187]\n"
  "v_mfma_f32_16x16x32_bf16 a[216:219], v[220:223], v[216:219], a[216:219]\n"
  "v_mfma_f32_16x16x32_bf16 a[28:31], v[224:227], v[192:195], a[28:31]\n"
  "buffer_load_dwordx4 v[120:123], v234, s[40:43], s61 offen\n"
  "v_mfma_f32_16x16x32_bf16 a[60:63], v[224:227], v[196:199], a[60:63]\n"
  "s_waitcnt vmcnt(35)\n"
  "v_mfma_f32_16x16x32_bf16 a[92:95], v[224:227], v[200:203], a[92:95]\n"
  "v_perm_b32 v220, v4, v0, s70\n"
  "v_perm_b32 v221, v12, v8, s70\n"
  "v_mfma_f32_16x16x32_bf16 a[124:127], v[224:227], v[204:207], a[124:127]\n"
  "v_perm_b32 v222, v20, v16, s70\n"
  "v_perm_b32 v223, v28, v24, s70\n"
  "v_mfma_f32_16x16x32_bf16 a[156:159], v[224:227], v[208:211], a[156:159]\n"
  "v_mfma_f32_16x16x32_bf16 a[188:191], v[224:227], v[212:215], a[188:191]\n"
  "buffer_load_dwordx4 v[124:127], v235, s[40:43], s61 offen\n"
  "v_mfma_f32_16x16x32_bf16 a[220:223], v[224:227], v[216:219], a[220:223]\n"
  "s_waitcnt lgkmcnt(0)\n"
  "v_add_u32 v238, 43008, v238\n"
  "v_mfma_f32_16x16x32_bf16 a[0:3], v[220:223], a[224:227], a[0:3]\n"
  "ds_read_b128 v[192:195], v238 offset:0\n"
  "ds_read_b128 v[196:199], v238 offset:2048\n"
  "v_mfma_f32_16x16x32_bf16 a[32:35], v[220:223], a[228:231], a[32:35]\n"
  "v_perm_b32 v224, v4, v0, s71\n"
  "v_perm_b32 v225, v12, v8, s71\n"
  "s_add_u32 s73, s73, 128\n"
  "s_cmp_eq_u32 s73, 1152\n"
  "s_cselect_b32 s73, 0, s73\n"
  "v_add_u32 v239, s73, v236\n"
  "v_mfma_f32_16x16x32_bf16 a[64:67], v[220:223], a[232:235], a[64:67]\n"
  "s_add_u32 m0, s62, 135168\n"
  "v_perm_b32 v226, v20, v16, s71\n"
  "v_perm_b32 v227, v28, v24, s71\n"
  "v_mfma_f32_16x16x32_bf16 a[96:99], v[220:223], a[236:239], a[96:99]\n"
  "buffer_load_dwordx4 v239, s[36:39], s66 offen lds\n"
  "v_mfma_f32_16x16x32_bf16 a[128:131], v[220:223], a[240:243], a[128:131]\n"
  "ds_read_b128 v[200:203], v238 offset:4096\n"
  "ds_read_b128 v[204:207], v238 offset:6144\n"
  "v_mfma_f32_16x16x32_bf16 a[160:163], v[220:223], a[244:247], a[160:163]\n"
  "ds_read_b128 v[208:211], v238 offset:8192\n"
  "ds_read_b128 v[212:215], v238 offset:10240\n"
  "v_mfma_f32_16x16x32_bf16 a[192:195], v[220:223], a[248:251], a[192:195]\n"
  "s_add_u32 m0, s63, 135168\n"
  "ds_read_b128 v[216:219], v238 offset:12288\n"
  "v_mfma_f32_16x16x32_bf16 a[4:7], v[224:227], a[224:227], a[4:7]\n"
  "buffer_load_dwordx4 v239, s[36:39], s67 offen lds\n"
  "v_mfma_f32_16x16x32_bf16 a[36:39], v[224:227], a[228:231], a[36:39]\n"
  "v_perm_b32 v220, v5, v1, s70\n"
  "v_perm_b32 v221, v13, v9, s70\n"
  "v_mfma_f32_16x16x32_bf16 a[68:71], v[224:227], a[232:235], a[68:71]\n"
  "v_perm_b32 v222, v21, v17, s70\n"
  "v_perm_b32 v223, v29, v25, s70\n"
  "v_mfma_f32_16x16x32_bf16 a[100:103], v[224:227], a[236:239], a[100:103]\n"
  "v_mfma_f32_16x16x32_bf16 a[132:135], v[224:227], a[240:243], a[132:135]\n"
  "s_add_u32 m0, s64, 135168\n"
  "v_mfma_f32_16x16x32_bf16 a[164:167], v[224:227], a[244:247], a[164:167]\n"
  "buffer_load_dwordx4 v239, s[36:39], s68 offen lds\n"
  "v_mfma_f32_16x16x32_bf16 a[196:199], v[224:227], a[248:251], a[196:199]\n"
  "v_mfma_f32_16x16x32_bf16 a[8:11], v[220:223], a[224:227], a[8:11]\n"
  "v_mfma_f32_16x16x32_bf16 a[40:43], v[220:223], a[228:231], a[40:43]\n"
  "v_perm_b32 v224, v5, v1, s71\n"
  "v_perm_b32 v225, v13, v9, s71\n"
  "v_mfma_f32_16x16x32_bf16 a[72:75], v[220:223], a[232:235], a[72:75]\n"
  "s_add_u32 m0, s65, 135168\n"
  "v_perm_b32 v226, v21, v17, s71\n"
  "v_perm_b32 v227, v29, v25, s71\n"
  "v_mfma_f32_16x16x32_bf16 a[104:107], v[220:223], a[236:239], a[104:107]\n"
  "buffer_load_dwordx4 v239, s[36:39], s69 offen lds\n"
  "v_mfma_f32_16x16x32_bf16 a[136:139], v[220:223], a[240:243], a[136:139]\n"
  "s_add_u32 s61, s61, 0x40000\n"
  "v_mfma_f32_16x16x32_bf16 a[168:171], v[220:223], a[244:247], a[168:171]\n"
  "s_cmp_eq_u32 s61, 0x480000\n"
  "v_mfma_f32_16x16x32_bf16 a[200:203], v[220:223], a[248:251], a[200:203]\n"
  "s_cselect_b32 s61, 0, s61\n"
  "v_mfma_f32_16x16x32_bf16 a[12:15], v[224:227], a[224:227], a[12:15]\n"
  "buffer_load_dwordx4 v[128:131], v228, s[40:43], s61 offen\n"
  "v_mfma_f32_16x16x32_bf16 a[44:47], v[224:227], a[228:231], a[44:47]\n"
  "v_perm_b32 v220, v6, v2, s70\n"
  "v_perm_b32 v221, v14, v10, s70\n"
  "v_mfma_f32_16x16x32_bf16 a[76:79], v[224:227], a[232:235], a[76:79]\n"
  "v_perm_b32 v222, v22, v18, s70\n"
  "v_perm_b32 v223, v30, v26, s70\n"
  "v_mfma_f32_16x16x32_bf16 a[108:111], v[224:227], a[236:239], a[108:111]\n"
  "v_mfma_f32_16x16x32_bf16 a[140:143], v[224:227], a[240:243], a[140:143]\n"
  "v_mfma_f32_16x16x32_bf16 a[172:175], v[224:227], a[244:247], a[172:175]\n"
  "buffer_load_dwordx4 v[132:135], v229, s[40:43], s61 offen\n"
  "v_mfma_f32_16x16x32_bf16 a[204:207], v[224:227], a[248:251], a[204:207]\n"
  "v_mfma_f32_16x16x32_bf16 a[16:19], v[220:223], a[224:227], a[16:19]\n"
  "v_mfma_f32_16x16x32_bf16 a[48:51], v[220:223], a[228:231], a[48:51]\n"
  "v_perm_b32 v224, v6, v2, s71\n"
  "v_perm_b32 v225, v14, v10, s71\n"
  "v_mfma_f32_16x16x32_bf16 a[80:83], v[220:223], a[232:235], a[80:83]\n"
  "v_perm_b32 v226, v22, v18, s71\n"
  "v_perm_b32 v227, v30, v26, s71\n"
  "v_mfma_f32_16x16x32_bf16 a[112:115], v[220:223], a[236:239], a[112:115]\n"
  "buffer_load_dwordx4 v[136:139], v230, s[40:43], s61 offen\n"
  "v_mfma_f32_16x16x32_bf16 a[144:147], v[220:223], a[240:243], a[144:147]\n"
  "v_mfma_f32_16x16x32_bf16 a[176:179], v[220:223], a[244:247], a[176:179]\n"
  "v_mfma_f32_16x16x32_bf16 a[208:211], v[220:223], a[248:251], a[208:211]\n"
  "v_mfma_f32_16x16x32_bf16 a[20:23], v[224:227], a[224:227], a[20:23]\n"
  "buffer_load_dwordx4 v[140:143], v231, s[40:43], s61 offen\n"
  "v_mfma_f32_16x16x32_bf16 a[52:55], v[224:227], a[228:231], a[52:55]\n"
  "v_perm_b32 v220, v7, v3, s70\n"
  "v_perm_b32 v221, v15, v11, s70\n"
  "v_mfma_f32_16x16x32_bf16 a[84:87], v[224:227], a[232:235], a[84:87]\n"
  "v_perm_b32 v222, v23, v19, s70\n"
  "v_perm_b32 v223, v31, v27, s70\n"
  "v_mfma_f32_16x16x32_bf16 a[116:119], v[224:227], a[236:239], a[116:119]\n"
  "v_mfma_f32_16x16x32_bf16 a[148:151], v[224:227], a[240:243], a[148:151]\n"
  "v_mfma_f32_16x16x32_bf16 a[180:183], v[224:227], a[244:247], a[180:183]\n"
  "buffer_load_dwordx4 v[144:147], v232, s[40:43], s61 offen\n"
  "v_mfma_f32_16x16x32_bf16 a[212:215], v[224:227], a[248:251], a[212:215]\n"
  "v_mfma_f32_16x16x32_bf16 a[24:27], v[220:223], a[224:227], a[24:27]\n"
  "v_mfma_f32_16x16x32_bf16 a[56:59], v[220:223], a[228:231], a[56:59]\n"
  "v_perm_b32 v224, v7, v3, s71\n"
  "v_perm_b32 v225, v15, v11, s71\n"
  "v_mfma_f32_16x16x32_bf16 a[88:91], v[220:223], a[232:235], a[88:91]\n"
  "v_perm_b32 v226, v23, v19, s71\n"
  "v_perm_b32 v227, v31, v27, s71\n"
  "v_mfma_f32_16x16x32_bf16 a[120:123], v[220:223], a[236:239], a[120:123]\n"
  "buffer_load_dwordx4 v[148:151], v233, s[40:43], s61 offen\n"
  "v_mfma_f32_16x16x32_bf16 a[152:155], v[220:223], a[240:243], a[152:155]\n"
  "v_mfma_f32_16x16x32_bf16 a[184:187], v[220:223], a[244:247], a[184:187]\n"
  "v_mfma_f32_16x16x32_bf16 a[216:219], v[220:223], a[248:251], a[216:219]\n"
  "v_mfma_f32_16x16x32_bf16 a[28:31], v[224:227], a[224:227], a[28:31]\n"
  "buffer_load_dwordx4 v[152:155], v234, s[40:43], s61 offen\n"
  "v_mfma_f32_16x16x32_bf16 a[60:63], v[224:227], a[228:231], a[60:63]\n"
  "s_waitcnt vmcnt(35)\n"
  "v_mfma_f32_16x16x32_bf16 a[92:95], v[224:227], a[232:235], a[92:95]\n"
  "v_perm_b32 v220, v36, v32, s70\n"
  "v_perm_b32 v221, v44, v40, s70\n"
  "v_mfma_f32_16x16x32_bf16 a[124:127], v[224:227], a[236:239], a[124:127]\n"
  "v_perm_b32 v222, v52, v48, s70\n"
  "v_perm_b32 v223, v60, v56, s70\n"
  "v_mfma_f32_16x16x32_bf16 a[156:159], v[224:227], a[240:243], a[156:159]\n"
  "v_mfma_f32_16x16x32_bf16 a[188:191], v[224:227], a[244:247], a[188:191]\n"
  "buffer_load_dwordx4 v[156:159], v235, s[40:43], s61 offen\n"
  "v_mfma_f32_16x16x32_bf16 a[220:223], v[224:227], a[248:251], a[220:223]\n"
  "s_waitcnt lgkmcnt(0)\n"
  "s_waitcnt vmcnt(32)\n"
  "s_barrier\n"
  "v_mfma_f32_16x16x32_bf16 a[0:3], v[220:223], v[192:195], a[0:3]\n"
  "ds_read_b128 a[224:227], v237 offset:14336\n"
  "ds_read_b128 a[228:231], v237 offset:16384\n"
  "v_mfma_f32_16x16x32_bf16 a[32:35], v[220:223], v[196:199], a[32:35]\n"
  "v_perm_b32 v224, v36, v32, s71\n"
  "v_perm_b32 v225, v44, v40, s71\n"
  "s_add_u32 s73, s73, 128\n"
  "s_cmp_eq_u32 s73, 1152\n"
  "s_cselect_b32 s73, 0, s73\n"
  "v_add_u32 v239, s73, v236\n"
  "v_mfma_f32_16x16x32_bf16 a[64:67], v[220:223], v[200:203], a[64:67]\n"
  "s_add_u32 m0, s62, 149504\n"
  "v_perm_b32 v226, v52, v48, s71\n"
  "v_perm_b32 v227, v60, v56, s71\n"
  "v_mfma_f32_16x16x32_bf16 a[96:99], v[220:223], v[204:207], a[96:99]\n"
  "buffer_load_dwordx4 v239, s[36:39], s66 offen lds\n"
  "v_mfma_f32_16x16x32_bf16 a[128:131], v[220:223], v[208:211], a[128:131]\n"
  "ds_read_b128 a[232:235], v237 offset:18432\n"
  "ds_read_b128 a[236:239], v237 offset:20480\n"
  "v_mfma_f32_16x16x32_bf16 a[160:163], v[220:223], v[212:215], a[160:163]\n"
  "ds_read_b128 a[240:243], v237 offset:22528\n"
  "ds_read_b128 a[244:247], v237 offset:24576\n"
  "v_mfma_f32_16x16x32_bf16 a[192:195], v[220:223], v[216:219], a[192:195]\n"
  "s_add_u32 m0, s63, 149504\n"
  "ds_read_b128 a[248:251], v237 offset:26624\n"
  "v_mfma_f32_16x16x32_bf16 a[4:7], v[224:227], v[192:195], a[4:7]\n"
  "buffer_load_dwordx4 v239, s[36:39], s67 offen lds\n"
  "v_mfma_f32_16x16x32_bf16 a[36:39], v[224:227], v[196:199], a[36:39]\n"
  "v_perm_b32 v220, v37, v33, s70\n"
  "v_perm_b32 v221, v45, v41, s70\n"
  "v_mfma_f32_16x16x32_bf16 a[68:71], v[224:227], v[200:203], a[68:71]\n"
  "v_perm_b32 v222, v53, v49, s70\n"
  "v_perm_b32 v223, v61, v57, s70\n"
  "v_mfma_f32_16x16x32_bf16 a[100:103], v[224:227], v[204:207], a[100:103]\n"
  "v_mfma_f32_16x16x32_bf16 a[132:135], v[224:227], v[208:211], a[132:135]\n"
  "s_add_u32 m0, s64, 149504\n"
  "v_mfma_f32_16x16x32_bf16 a[164:167], v[224:227], v[212:215], a[164:167]\n"
  "buffer_load_dwordx4 v239, s[36:39], s68 offen lds\n"
  "v_mfma_f32_16x16x32_bf16 a[196:199], v[224:227], v[216:219], a[196:199]\n"
  "v_mfma_f32_16x16x32_bf16 a[8:11], v[220:223], v[192:195], a[8:11]\n"
  "v_mfma_f32_16x16x32_bf16 a[40:43], v[220:223], v[196:199], a[40:43]\n"
  "v_perm_b32 v224, v37, v33, s71\n"
  "v_perm_b32 v225, v45, v41, s71\n"
  "v_mfma_f32_16x16x32_bf16 a[72:75], v[220:223], v[200:203], a[72:75]\n"
  "s_add_u32 m0, s65, 149504\n"
  "v_perm_b32 v226, v53, v49, s71\n"
  "v_perm_b32 v227, v61, v57, s71\n"
  "v_mfma_f32_16x16x32_bf16 a[104:107], v[220:223], v[204:207], a[104:107]\n"
  "buffer_load_dwordx4 v239, s[36:39], s69 offen lds\n"
  "v_mfma_f32_16x16x32_bf16 a[136:139], v[220:223], v[208:211], a[136:139]\n"
  "v_mfma_f32_16x16x32_bf16 a[168:171], v[220:223], v[212:215], a[168:171]\n"
  "v_mfma_f32_16x16x32_bf16 a[200:203], v[220:223], v[216:219], a[200:203]\n"
  "s_add_u32 s61, s61, 0x40000\n"
  "v_mfma_f32_16x16x32_bf16 a[12:15], v[224:227], v[192:195], a[12:15]\n"
  "buffer_load_dwordx4 v[160:163], v228, s[40:43], s61 offen\n"
  "v_mfma_f32_16x16x32_bf16 a[44:47], v[224:227], v[196:199], a[44:47]\n"
  "v_perm_b32 v220, v38, v34, s70\n"
  "v_perm_b32 v221, v46, v42, s70\n"
  "v_mfma_f32_16x16x32_bf16 a[76:79], v[224:227], v[200:203], a[76:79]\n"
  "v_perm_b32 v222, v54, v50, s70\n"
  "v_perm_b32 v223, v62, v58, s70\n"
  "v_mfma_f32_16x16x32_bf16 a[108:111], v[224:227], v[204:207], a[108:111]\n"
  "v_mfma_f32_16x16x32_bf16 a[140:143], v[224:227], v[208:211], a[140:143]\n"
  "v_mfma_f32_16x16x32_bf16 a[172:175], v[224:227], v[212:215], a[172:175]\n"
  "buffer_load_dwordx4 v[164:167], v229, s[40:43], s61 offen\n"
  "v_mfma_f32_16x16x32_bf16 a[204:207], v[224:227], v[216:219], a[204:207]\n"
  "v_mfma_f32_16x16x32_bf16 a[16:19], v[220:223], v[192:195], a[16:19]\n"
  "v_mfma_f32_16x16x32_bf16 a[48:51], v[220:223], v[196:199], a[48:51]\n"
  "v_perm_b32 v224, v38, v34, s71\n"
  "v_perm_b32 v225, v46, v42, s71\n"
  "v_mfma_f32_16x16x32_bf16 a[80:83], v[220:223], v[200:203], a[80:83]\n"
  "v_perm_b32 v226, v54, v50, s71\n"
  "v_perm_b32 v227, v62, v58, s71\n"
  "v_mfma_f32_16x16x32_bf16 a[112:115], v[220:223], v[204:207], a[112:115]\n"
  "buffer_load_dwordx4 v[168:171], v230, s[40:43], s61 offen\n"
  "v_mfma_f32_16x16x32_bf16 a[144:147], v[220:223], v[208:211], a[144:147]\n"
  "v_mfma_f32_16x16x32_bf16 a[176:179], v[220:223], v[212:215], a[176:179]\n"
  "v_mfma_f32_16x16x32_bf16 a[208:211], v[220:223], v[216:219], a[208:211]\n"
  "v_mfma_f32_16x16x32_bf16 a[20:23], v[224:227], v[192:195], a[20:23]\n"
  "buffer_load_dwordx4 v[172:175], v231, s[40:43], s61 offen\n"
  "v_mfma_f32_16x16x32_bf16 a[52:55], v[224:227], v[196:199], a[52:55]\n"
  "v_perm_b32 v220, v39, v35, s70\n"
  "v_perm_b32 v221, v47, v43, s70\n"
  "v_mfma_f32_16x16x32_bf16 a[84:87], v[224:227], v[200:203], a[84:87]\n"
  "v_perm_b32 v222, v55, v51, s70\n"
  "v_perm_b32 v223, v63, v59, s70\n"
  "v_mfma_f32_16x16x32_bf16 a[116:119], v[224:227], v[204:207], a[116:119]\n"
  "v_mfma_f32_16x16x32_bf16 a[148:151], v[224:227], v[208:211], a[148:151]\n"
  "v_mfma_f32_16x16x32_bf16 a[180:183], v[224:227], v[212:215], a[180:183]\n"
  "buffer_load_dwordx4 v[176:179], v232, s[40:43], s61 offen\n"
  "v_mfma_f32_16x16x32_bf16 a[212:215], v[224:227], v[216:219], a[212:215]\n"
  "v_mfma_f32_16x16x32_bf16 a[24:27], v[220:223], v[192:195], a[24:27]\n"
  "v_mfma_f32_16x16x32_bf16 a[56:59], v[220:223], v[196:199], a[56:59]\n"
  "v_perm_b32 v224, v39, v35, s71\n"
  "v_perm_b32 v225, v47, v43, s71\n"
  "v_mfma_f32_16x16x32_bf16 a[88:91], v[220:223], v[200:203], a[88:91]\n"
  "v_perm_b32 v226, v55, v51, s71\n"
  "v_perm_b32 v227, v63, v59, s71\n"
  "v_mfma_f32_16x16x32_bf16 a[120:123], v[220:223], v[204:207], a[120:123]\n"
  "buffer_load_dwordx4 v[180:183], v233, s[40:43], s61 offen\n"
  "v_mfma_f32_16x16x32_bf16 a[152:155], v[220:223], v[208:211], a[152:155]\n"
  "v_mfma_f32_16x16x32_bf16 a[184:187], v[220:223], v[212:215], a[184:187]\n"
  "v_mfma_f32_16x16x32_bf16 a[216:219], v[220:223], v[216:219], a[216:219]\n"
  "v_mfma_f32_16x16x32_bf16 a[28:31], v[224:227], v[192:195], a[28:31]\n"
  "buffer_load_dwordx4 v[184:187], v234, s[40:43], s61 offen\n"
  "v_mfma_f32_16x16x32_bf16 a[60:63], v[224:227], v[196:199], a[60:63]\n"
  "s_waitcnt vmcnt(35)\n"
  "v_mfma_f32_16x16x32_bf16 a[92:95], v[224:227], v[200:203], a[92:95]\n"
  "v_perm_b32 v220, v68, v64, s70\n"
  "v_perm_b32 v221, v76, v72, s70\n"
  "v_mfma_f32_16x16x32_bf16 a[124:127], v[224:227], v[204:207], a[124:127]\n"
  "v_perm_b32 v222, v84, v80, s70\n"
  "v_perm_b32 v223, v92, v88, s70\n"
  "v_mfma_f32_16x16x32_bf16 a[156:159], v[224:227], v[208:211], a[156:159]\n"
  "v_mfma_f32_16x16x32_bf16 a[188:191], v[224:227], v[212:215], a[188:191]\n"
  "buffer_load_dwordx4 v[188:191], v235, s[40:43], s61 offen\n"
  "v_mfma_f32_16x16x32_bf16 a[220:223], v[224:227], v[216:219], a[220:223]\n"
  "s_waitcnt lgkmcnt(0)\n"
  "v_mfma_f32_16x16x32_bf16 a[0:3], v[220:223], a[224:227], a[0:3]\n"
  "s_add_u32 s61, s61, 0x40000\n"
  "ds_read_b128 v[192:195], v238 offset:14336\n"
  "ds_read_b128 v[196:199], v238 offset:16384\n"
  "v_mfma_f32_16x16x32_bf16 a[32:35], v[220:223], a[228:231], a[32:35]\n"
  "s_cmp_eq_u32 s61, 0x480000\n"
  "v_perm_b32 v224, v68, v64, s71\n"
  "v_perm_b32 v225, v76, v72, s71\n"
  "v_mfma_f32_16x16x32_bf16 a[64:67], v[220:223], a[232:235], a[64:67]\n"
  "s_cselect_b32 s61, 0, s61\n"
  "v_perm_b32 v226, v84, v80, s71\n"
  "v_perm_b32 v227, v92, v88, s71\n"
  "v_mfma_f32_16x16x32_bf16 a[96:99], v[220:223], a[236:239], a[96:99]\n"
  "buffer_load_dwordx4 v[0:3], v228, s[40:43], s61 offen\n"
  "v_mfma_f32_16x16x32_bf16 a[128:131], v[220:223], a[240:243], a[128:131]\n"
  "ds_read_b128 v[200:203], v238 offset:18432\n"
  "ds_read_b128 v[204:207], v238 offset:20480\n"
  "v_mfma_f32_16x16x32_bf16 a[160:163], v[220:223], a[244:247], a[160:163]\n"
  "ds_read_b128 v[208:211], v238 offset:22528\n"
  "ds_read_b128 v[212:215], v238 offset:24576\n"
  "v_mfma_f32_16x16x32_bf16 a[192:195], v[220:223], a[248:251], a[192:195]\n"
  "ds_read_b128 v[216:219], v238 offset:26624\n"
  "v_mfma_f32_16x16x32_bf16 a[4:7], v[224:227], a[224:227], a[4:7]\n"
  "buffer_load_dwordx4 v[4:7], v229, s[40:43], s61 offen\n"
  "v_mfma_f32_16x16x32_bf16 a[36:39], v[224:227], a[228:231], a[36:39]\n"
  "v_perm_b32 v220, v69, v65, s70\n"
  "v_perm_b32 v221, v77, v73, s70\n"
  "v_mfma_f32_16x16x32_bf16 a[68:71], v[224:227], a[232:235], a[68:71]\n"
  "v_perm_b32 v222, v85, v81, s70\n"
  "v_perm_b32 v223, v93, v89, s70\n"
  "v_mfma_f32_16x16x32_bf16 a[100:103], v[224:227], a[236:239], a[100:103]\n"
  "v_mfma_f32_16x16x32_bf16 a[132:135], v[224:227], a[240:243], a[132:135]\n"
  "v_mfma_f32_16x16x32_bf16 a[164:167], v[224:227], a[244:247], a[164:167]\n"
  "buffer_load_dwordx4 v[8:11], v230, s[40:43], s61 offen\n"
  "v_mfma_f32_16x16x32_bf16 a[196:199], v[224:227], a[248:251], a[196:199]\n"
  "v_mfma_f32_16x16x32_bf16 a[8:11], v[220:223], a[224:227], a[8:11]\n"
  "v_mfma_f32_16x16x32_bf16 a[40:43], v[220:223], a[228:231], a[40:43]\n"
  "v_perm_b32 v224, v69, v65, s71\n"
  "v_perm_b32 v225, v77, v73, s71\n"
  "v_mfma_f32_16x16x32_bf16 a[72:75], v[220:223], a[232:235], a[72:75]\n"
  "v_perm_b32 v226, v85, v81, s71\n"
  "v_perm_b32 v227, v93, v89, s71\n"
  "v_mfma_f32_16x16x32_bf16 a[104:107], v[220:223], a[236:239], a[104:107]\n"
  "buffer_load_dwordx4 v[12:15], v231, s[40:43], s61 offen\n"
  "v_mfma_f32_16x16x32_bf16 a[136:139], v[220:223], a[240:243], a[136:139]\n"
  "v_mfma_f32_16x16x32_bf16 a[168:171], v[220:223], a[244:247], a[168:171]\n"
  "v_mfma_f32_16x16x32_bf16 a[200:203], v[220:223], a[248:251], a[200:203]\n"
  "v_mfma_f32_16x16x32_bf16 a[12:15], v[224:227], a[224:227], a[12:15]\n"
  "buffer_load_dwordx4 v[16:19], v232, s[40:43], s61 offen\n"
  "v_mfma_f32_16x16x32_bf16 a[44:47], v[224:227], a[228:231], a[44:47]\n"
  "v_perm_b32 v220, v70, v66, s70\n"
  "v_perm_b32 v221, v78, v74, s70\n"
  "v_mfma_f32_16x16x32_bf16 a[76:79], v[224:227], a[232:235], a[76:79]\n"
  "v_perm_b32 v222, v86, v82, s70\n"
  "v_perm_b32 v223, v94, v90, s70\n"
  "v_mfma_f32_16x16x32_bf16 a[108:111], v[224:227], a[236:239], a[108:111]\n"
  "v_mfma_f32_16x16x32_bf16 a[140:143], v[224:227], a[240:243], a[140:143]\n"
  "v_mfma_f32_16x16x32_bf16 a[172:175], v[224:227], a[244:247], a[172:175]\n"
  "buffer_load_dwordx4 v[20:23], v233, s[40:43], s61 offen\n"
  "v_mfma_f32_16x16x32_bf16 a[204:207], v[224:227], a[248:251], a[204:207]\n"
  "v_mfma_f32_16x16x32_bf16 a[16:19], v[220:223], a[224:227], a[16:19]\n"
  "v_mfma_f32_16x16x32_bf16 a[48:51], v[220:223], a[228:231], a[48:51]\n"
  "v_perm_b32 v224, v70, v66, s71\n"
  "v_perm_b32 v225, v78, v74, s71\n"
  "v_mfma_f32_16x16x32_bf16 a[80:83], v[220:223], a[232:235], a[80:83]\n"
  "v_perm_b32 v226, v86, v82, s71\n"
  "v_perm_b32 v227, v94, v90, s71\n"
  "v_mfma_f32_16x16x32_bf16 a[112:115], v[220:223], a[236:239], a[112:115]\n"
  "buffer_load_dwordx4 v[24:27], v234, s[40:43], s61 offen\n"
  "v_mfma_f32_16x16x32_bf16 a[144:147], v[220:223], a[240:243], a[144:147]\n"
  "v_mfma_f32_16x16x32_bf16 a[176:179], v[220:223], a[244:247], a[176:179]\n"
  "v_mfma_f32_16x16x32_bf16 a[208:211], v[220:223], a[248:251], a[208:211]\n"
  "v_mfma_f32_16x16x32_bf16 a[20:23], v[224:227], a[224:227], a[20:23]\n"
  "buffer_load_dwordx4 v[28:31], v235, s[40:43], s61 offen\n"
  "v_mfma_f32_16x16x32_bf16 a[52:55], v[224:227], a[228:231], a[52:55]\n"
  "v_perm_b32 v220, v71, v67, s70\n"
  "v_perm_b32 v221, v79, v75, s70\n"
  "v_mfma_f32_16x16x32_bf16 a[84:87], v[224:227], a[232:235], a[84:87]\n"
  "v_perm_b32 v222, v87, v83, s70\n"
  "v_perm_b32 v223, v95, v91, s70\n"
  "v_mfma_f32_16x16x32_bf16 a[116:119], v[224:227], a[236:239], a[116:119]\n"
  "v_mfma_f32_16x16x32_bf16 a[148:151], v[224:227], a[240:243], a[148:151]\n"
  "s_add_u32 s61, s61, 0x40000\n"
  "v_mfma_f32_16x16x32_bf16 a[180:183], v[224:227], a[244:247], a[180:183]\n"
  "buffer_load_dwordx4 v[32:35], v228, s[40:43], s61 offen\n"
  "v_mfma_f32_16x16x32_bf16 a[212:215], v[224:227], a[248:251], a[212:215]\n"
  "v_mfma_f32_16x16x32_bf16 a[24:27], v[220:223], a[224:227], a[24:27]\n"
  "v_mfma_f32_16x16x32_bf16 a[56:59], v[220:223], a[228:231], a[56:59]\n"
  "v_perm_b32 v224, v71, v67, s71\n"
  "v_perm_b32 v225, v79, v75, s71\n"
  "v_mfma_f32_16x16x32_bf16 a[88:91], v[220:223], a[232:235], a[88:91]\n"
  "v_perm_b32 v226, v87, v83, s71\n"
  "v_perm_b32 v227, v95, v91, s71\n"
  "v_mfma_f32_16x16x32_bf16 a[120:123], v[220:223], a[236:239], a[120:123]\n"
  "buffer_load_dwordx4 v[36:39], v229, s[40:43], s61 offen\n"
  "v_mfma_f32_16x16x32_bf16 a[152:155], v[220:223], a[240:243], a[152:155]\n"
  "v_mfma_f32_16x16x32_bf16 a[184:187], v[220:223], a[244:247], a[184:187]\n"
  "v_mfma_f32_16x16x32_bf16 a[216:219], v[220:223], a[248:251], a[216:219]\n"
  "v_mfma_f32_16x16x32_bf16 a[28:31], v[224:227], a[224:227], a[28:31]\n"
  "buffer_load_dwordx4 v[40:43], v230, s[40:43], s61 offen\n"
  "v_mfma_f32_16x16x32_bf16 a[60:63], v[224:227], a[228:231], a[60:63]\n"
  "s_waitcnt vmcnt(35)\n"
  "v_mfma_f32_16x16x32_bf16 a[92:95], v[224:227], a[232:235], a[92:95]\n"
  "v_perm_b32 v220, v100, v96, s70\n"
  "v_perm_b32 v221, v108, v104, s70\n"
  "v_mfma_f32_16x16x32_bf16 a[124:127], v[224:227], a[236:239], a[124:127]\n"
  "v_perm_b32 v222, v116, v112, s70\n"
  "v_perm_b32 v223, v124, v120, s70\n"
  "v_mfma_f32_16x16x32_bf16 a[156:159], v[224:227], a[240:243], a[156:159]\n"
  "v_mfma_f32_16x16x32_bf16 a[188:191], v[224:227], a[244:247], a[188:191]\n"
  "buffer_load_dwordx4 v[44:47], v231, s[40:43], s61 offen\n"
  "v_mfma_f32_16x16x32_bf16 a[220:223], v[224:227], a[248:251], a[220:223]\n"
  "s_waitcnt lgkmcnt(0)\n"
  "v_mfma_f32_16x16x32_bf16 a[0:3], v[220:223], v[192:195], a[0:3]\n"
  "ds_read_b128 a[224:227], v237 offset:28672\n"
  "ds_read_b128 a[228:231], v237 offset:30720\n"
  "v_mfma_f32_16x16x32_bf16 a[32:35], v[220:223], v[196:199], a[32:35]\n"
  "v_perm_b32 v224, v100, v96, s71\n"
  "v_perm_b32 v225, v108, v104, s71\n"
  "v_mfma_f32_16x16x32_bf16 a[64:67], v[220:223], v[200:203], a[64:67]\n"
  "v_perm_b32 v226, v116, v112, s71\n"
  "v_perm_b32 v227, v124, v120, s71\n"
  "v_mfma_f32_16x16x32_bf16 a[96:99], v[220:223], v[204:207], a[96:99]\n"
  "v_mfma_f32_16x16x32_bf16 a[128:131], v[220:223], v[208:211], a[128:131]\n"
  "ds_read_b128 a[232:235], v237 offset:32768\n"
  "ds_read_b128 a[236:239], v237 offset:34816\n"
  "v_mfma_f32_16x16x32_bf16 a[160:163], v[220:223], v[212:215], a[160:163]\n"
  "ds_read_b128 a[240:243], v237 offset:36864\n"
  "ds_read_b128 a[244:247], v237 offset:38912\n"
  "v_mfma_f32_16x16x32_bf16 a[192:195], v[220:223], v[216:219], a[192:195]\n"
  "buffer_load_dwordx4 v[48:51], v232, s[40:43], s61 offen\n"
  "ds_read_b128 a[248:251], v237 offset:40960\n"
  "v_mfma_f32_16x16x32_bf16 a[4:7], v[224:227], v[192:195], a[4:7]\n"
  "v_mfma_f32_16x16x32_bf16 a[36:39], v[224:227], v[196:199], a[36:39]\n"
  "v_perm_b32 v220, v101, v97, s70\n"
  "v_perm_b32 v221, v109, v105, s70\n"
  "v_mfma_f32_16x16x32_bf16 a[68:71], v[224:227], v[200:203], a[68:71]\n"
  "v_perm_b32 v222, v117, v113, s70\n"
  "v_perm_b32 v223, v125, v121, s70\n"
  "v_mfma_f32_16x16x32_bf16 a[100:103], v[224:227], v[204:207], a[100:103]\n"
  "v_mfma_f32_16x16x32_bf16 a[132:135], v[224:227], v[208:211], a[132:135]\n"
  "v_mfma_f32_16x16x32_bf16 a[164:167], v[224:227], v[212:215], a[164:167]\n"
  "buffer_load_dwordx4 v[52:55], v233, s[40:43], s61 offen\n"
  "v_mfma_f32_16x16x32_bf16 a[196:199], v[224:227], v[216:219], a[196:199]\n"
  "s_add_u32 s76, s76, 0x2000\n"
  "v_mfma_f32_16x16x32_bf16 a[8:11], v[220:223], v[192:195], a[8:11]\n"
  "s_and_b32 s76, s76, 0x1ffff\n"
  "v_mfma_f32_16x16x32_bf16 a[40:43], v[220:223], v[196:199], a[40:43]\n"
  "s_add_u32 s77, s76, 4096\n"
  "v_perm_b32 v224, v101, v97, s71\n"
  "v_perm_b32 v225, v109, v105, s71\n"
  "v_mfma_f32_16x16x32_bf16 a[72:75], v[220:223], v[200:203], a[72:75]\n"
  "s_add_u32 m0, s72, 0\n"
  "v_perm_b32 v226, v117, v113, s71\n"
  "v_perm_b32 v227, v125, v121, s71\n"
  "v_mfma_f32_16x16x32_bf16 a[104:107], v[220:223], v[204:207], a[104:107]\n"
  "buffer_load_dwordx4 v244, s[44:47], s76 offen nt lds\n"
  "v_mfma_f32_16x16x32_bf16 a[136:139], v[220:223], v[208:211], a[136:139]\n"
  "v_mfma_f32_16x16x32_bf16 a[168:171], v[220:223], v[212:215], a[168:171]\n"
  "v_mfma_f32_16x16x32_bf16 a[200:203], v[220:223], v[216:219], a[200:203]\n"
  "v_mfma_f32_16x16x32_bf16 a[12:15], v[224:227], v[192:195], a[12:15]\n"
  "v_mfma_f32_16x16x32_bf16 a[44:47], v[224:227], v[196:199], a[44:47]\n"
  "v_perm_b32 v220, v102, v98, s70\n"
  "v_perm_b32 v221, v110, v106, s70\n"
  "v_mfma_f32_16x16x32_bf16 a[76:79], v[224:227], v[200:203], a[76:79]\n"
  "v_perm_b32 v222, v118, v114, s70\n"
  "v_perm_b32 v223, v126, v122, s70\n"
  "v_mfma_f32_16x16x32_bf16 a[108:111], v[224:227], v[204:207], a[108:111]\n"
  "buffer_load_dwordx4 v[56:59], v234, s[40:43], s61 offen\n"
  "v_mfma_f32_16x16x32_bf16 a[140:143], v[224:227], v[208:211], a[140:143]\n"
  "v_mfma_f32_16x16x32_bf16 a[172:175], v[224:227], v[212:215], a[172:175]\n"
  "v_mfma_f32_16x16x32_bf16 a[204:207], v[224:227], v[216:219], a[204:207]\n"
  "v_mfma_f32_16x16x32_bf16 a[16:19], v[220:223], v[192:195], a[16:19]\n"
  "buffer_load_dwordx4 v[60:63], v235, s[40:43], s61 offen\n"
  "v_mfma_f32_16x16x32_bf16 a[48:51], v[220:223], v[196:199], a[48:51]\n"
  "v_perm_b32 v224, v102, v98, s71\n"
  "v_perm_b32 v225, v110, v106, s71\n"
  "v_mfma_f32_16x16x32_bf16 a[80:83], v[220:223], v[200:203], a[80:83]\n"
  "v_perm_b32 v226, v118, v114, s71\n"
  "v_perm_b32 v227, v126, v122, s71\n"
  "v_mfma_f32_16x16x32_bf16 a[112:115], v[220:223], v[204:207], a[112:115]\n"
  "v_mfma_f32_16x16x32_bf16 a[144:147], v[220:223], v[208:211], a[144:147]\n"
  "v_mfma_f32_16x16x32_bf16 a[176:179], v[220:223], v[212:215], a[176:179]\n"
  "v_mfma_f32_16x16x32_bf16 a[208:211], v[220:223], v[216:219], a[208:211]\n"
  "v_mfma_f32_16x16x32_bf16 a[20:23], v[224:227], v[192:195], a[20:23]\n"
  "buffer_load_dwordx4 v244, s[44:47], s76 offen offset:1024 nt lds\n"
  "v_mfma_f32_16x16x32_bf16 a[52:55], v[224:227], v[196:199], a[52:55]\n"
  "v_perm_b32 v220, v103, v99, s70\n"
  "v_perm_b32 v221, v111, v107, s70\n"
  "v_mfma_f32_16x16x32_bf16 a[84:87], v[224:227], v[200:203], a[84:87]\n"
  "v_perm_b32 v222, v119, v115, s70\n"
  "v_perm_b32 v223, v127, v123, s70\n"
  "v_mfma_f32_16x16x32_bf16 a[116:119], v[224:227], v[204:207], a[116:119]\n"
  "s_add_u32 s61, s61, 0x40000\n"
  "v_mfma_f32_16x16x32_bf16 a[148:151], v[224:227], v[208:211], a[148:151]\n"
  "s_cmp_eq_u32 s61, 0x480000\n"
  "v_mfma_f32_16x16x32_bf16 a[180:183], v[224:227], v[212:215], a[180:183]\n"
  "s_cselect_b32 s61, 0, s61\n"
  "v_mfma_f32_16x16x32_bf16 a[212:215], v[224:227], v[216:219], a[212:215]\n"
  "buffer_load_dwordx4 v[64:67], v228, s[40:43], s61 offen\n"
  "v_mfma_f32_16x16x32_bf16 a[24:27], v[220:223], v[192:195], a[24:27]\n"
  "v_mfma_f32_16x16x32_bf16 a[56:59], v[220:223], v[196:199], a[56:59]\n"
  "v_perm_b32 v224, v103, v99, s71\n"
  "v_perm_b32 v225, v111, v107, s71\n"
  "v_mfma_f32_16x16x32_bf16 a[88:91], v[220:223], v[200:203], a[88:91]\n"
  "v_perm_b32 v226, v119, v115, s71\n"
  "v_perm_b32 v227, v127, v123, s71\n"
  "v_mfma_f32_16x16x32_bf16 a[120:123], v[220:223], v[204:207], a[120:123]\n"
  "v_mfma_f32_16x16x32_bf16 a[152:155], v[220:223], v[208:211], a[152:155]\n"
  "buffer_load_dwordx4 v[68:71], v229, s[40:43], s61 offen\n"
  "v_mfma_f32_16x16x32_bf16 a[184:187], v[220:223], v[212:215], a[184:187]\n"
  "v_mfma_f32_16x16x32_bf16 a[216:219], v[220:223], v[216:219], a[216:219]\n"
  "v_mfma_f32_16x16x32_bf16 a[28:31], v[224:227], v[192:195], a[28:31]\n"
  "v_mfma_f32_16x16x32_bf16 a[60:63], v[224:227], v[196:199], a[60:63]\n"
  "s_waitcnt vmcnt(32)\n"
  "v_mfma_f32_16x16x32_bf16 a[92:95], v[224:227], v[200:203], a[92:95]\n"
  "v_perm_b32 v220, v132, v128, s70\n"
  "v_perm_b32 v221, v140, v136, s70\n"
  "v_mfma_f32_16x16x32_bf16 a[124:127], v[224:227], v[204:207], a[124:127]\n"
  "v_perm_b32 v222, v148, v144, s70\n"
  "v_perm_b32 v223, v156, v152, s70\n"
  "v_mfma_f32_16x16x32_bf16 a[156:159], v[224:227], v[208:211], a[156:159]\n"
  "buffer_load_dwordx4 v244, s[44:47], s76 offen offset:2048 nt lds\n"
  "v_mfma_f32_16x16x32_bf16 a[188:191], v[224:227], v[212:215], a[188:191]\n"
  "v_mfma_f32_16x16x32_bf16 a[220:223], v[224:227], v[216:219], a[220:223]\n"
  "s_waitcnt lgkmcnt(0)\n"
  "v_mfma_f32_16x16x32_bf16 a[0:3], v[220:223], a[224:227], a[0:3]\n"
  "ds_read_b128 v[192:195], v238 offset:28672\n"
  "ds_read_b128 v[196:199], v238 offset:30720\n"
  "v_mfma_f32_16x16x32_bf16 a[32:35], v[220:223], a[228:231], a[32:35]\n"
  "v_perm_b32 v224, v132, v128, s71\n"
  "v_perm_b32 v225, v140, v136, s71\n"
  "v_mfma_f32_16x16x32_bf16 a[64:67], v[220:223], a[232:235], a[64:67]\n"
  "v_perm_b32 v226, v148, v144, s71\n"
  "v_perm_b32 v227, v156, v152, s71\n"
  "v_mfma_f32_16x16x32_bf16 a[96:99], v[220:223], a[236:239], a[96:99]\n"
  "v_mfma_f32_16x16x32_bf16 a[128:131], v[220:223], a[240:243], a[128:131]\n"
  "ds_read_b128 v[200:203], v238 offset:32768\n"
  "ds_read_b128 v[204:207], v238 offset:34816\n"
  "v_mfma_f32_16x16x32_bf16 a[160:163], v[220:223], a[244:247], a[160:163]\n"
  "ds_read_b128 v[208:211], v238 offset:36864\n"
  "ds_read_b128 v[212:215], v238 offset:38912\n"
  "v_mfma_f32_16x16x32_bf16 a[192:195], v[220:223], a[248:251], a[192:195]\n"
  "buffer_load_dwordx4 v[72:75], v230, s[40:43], s61 offen\n"
  "ds_read_b128 v[216:219], v238 offset:40960\n"
  "v_mfma_f32_16x16x32_bf16 a[4:7], v[224:227], a[224:227], a[4:7]\n"
  "v_mfma_f32_16x16x32_bf16 a[36:39], v[224:227], a[228:231], a[36:39]\n"
  "v_perm_b32 v220, v133, v129, s70\n"
  "v_perm_b32 v221, v141, v137, s70\n"
  "v_mfma_f32_16x16x32_bf16 a[68:71], v[224:227], a[232:235], a[68:71]\n"
  "v_perm_b32 v222, v149, v145, s70\n"
  "v_perm_b32 v223, v157, v153, s70\n"
  "v_mfma_f32_16x16x32_bf16 a[100:103], v[224:227], a[236:239], a[100:103]\n"
  "v_mfma_f32_16x16x32_bf16 a[132:135], v[224:227], a[240:243], a[132:135]\n"
  "v_mfma_f32_16x16x32_bf16 a[164:167], v[224:227], a[244:247], a[164:167]\n"
  "buffer_load_dwordx4 v[76:79], v231, s[40:43], s61 offen\n"
  "v_mfma_f32_16x16x32_bf16 a[196:199], v[224:227], a[248:251], a[196:199]\n"
  "v_mfma_f32_16x16x32_bf16 a[8:11], v[220:223], a[224:227], a[8:11]\n"
  "v_mfma_f32_16x16x32_bf16 a[40:43], v[220:223], a[228:231], a[40:43]\n"
  "v_perm_b32 v224, v133, v129, s71\n"
  "v_perm_b32 v225, v141, v137, s71\n"
  "v_mfma_f32_16x16x32_bf16 a[72:75], v[220:223], a[232:235], a[72:75]\n"
  "v_perm_b32 v226, v149, v145, s71\n"
  "v_perm_b32 v227, v157, v153, s71\n"
  "v_mfma_f32_16x16x32_bf16 a[104:107], v[220:223], a[236:239], a[104:107]\n"
  "buffer_load_dwordx4 v244, s[44:47], s76 offen offset:3072 nt lds\n"
  "v_mfma_f32_16x16x32_bf16 a[136:139], v[220:223], a[240:243], a[136:139]\n"
  "v_mfma_f32_16x16x32_bf16 a[168:171], v[220:223], a[244:247], a[168:171]\n"
  "v_mfma_f32_16x16x32_bf16 a[200:203], v[220:223], a[248:251], a[200:203]\n"
  "v_mfma_f32_16x16x32_bf16 a[12:15], v[224:227], a[224:227], a[12:15]\n"
  "v_mfma_f32_16x16x32_bf16 a[44:47], v[224:227], a[228:231], a[44:47]\n"
  "v_perm_b32 v220, v134, v130, s70\n"
  "v_perm_b32 v221, v142, v138, s70\n"
  "v_mfma_f32_16x16x32_bf16 a[76:79], v[224:227], a[232:235], a[76:79]\n"
  "v_perm_b32 v222, v150, v146, s70\n"
  "v_perm_b32 v223, v158, v154, s70\n"
  "v_mfma_f32_16x16x32_bf16 a[108:111], v[224:227], a[236:239], a[108:111]\n"
  "buffer_load_dwordx4 v[80:83], v232, s[40:43], s61 offen\n"
  "v_mfma_f32_16x16x32_bf16 a[140:143], v[224:227], a[240:243], a[140:143]\n"
  "v_mfma_f32_16x16x32_bf16 a[172:175], v[224:227], a[244:247], a[172:175]\n"
  "v_mfma_f32_16x16x32_bf16 a[204:207], v[224:227], a[248:251], a[204:207]\n"
  "v_mfma_f32_16x16x32_bf16 a[16:19], v[220:223], a[224:227], a[16:19]\n"
  "buffer_load_dwordx4 v[84:87], v233, s[40:43], s61 offen\n"
  "v_mfma_f32_16x16x32_bf16 a[48:51], v[220:223], a[228:231], a[48:51]\n"
  "v_perm_b32 v224, v134, v130, s71\n"
  "v_perm_b32 v225, v142, v138, s71\n"
  "v_mfma_f32_16x16x32_bf16 a[80:83], v[220:223], a[232:235], a[80:83]\n"
  "v_perm_b32 v226, v150, v146, s71\n"
  "v_perm_b32 v227, v158, v154, s71\n"
  "v_mfma_f32_16x16x32_bf16 a[112:115], v[220:223], a[236:239], a[112:115]\n"
  "v_mfma_f32_16x16x32_bf16 a[144:147], v[220:223], a[240:243], a[144:147]\n"
  "v_mfma_f32_16x16x32_bf16 a[176:179], v[220:223], a[244:247], a[176:179]\n"
  "v_mfma_f32_16x16x32_bf16 a[208:211], v[220:223], a[248:251], a[208:211]\n"
  "s_add_u32 m0, s72, 4128\n"
  "v_mfma_f32_16x16x32_bf16 a[20:23], v[224:227], a[224:227], a[20:23]\n"
  "buffer_load_dwordx4 v244, s[44:47], s77 offen nt lds\n"
  "v_mfma_f32_16x16x32_bf16 a[52:55], v[224:227], a[228:231], a[52:55]\n"
  "v_perm_b32 v220, v135, v131, s70\n"
  "v_perm_b32 v221, v143, v139, s70\n"
  "v_mfma_f32_16x16x32_bf16 a[84:87], v[224:227], a[232:235], a[84:87]\n"
  "v_perm_b32 v222, v151, v147, s70\n"
  "v_perm_b32 v223, v159, v155, s70\n"
  "v_mfma_f32_16x16x32_bf16 a[116:119], v[224:227], a[236:239], a[116:119]\n"
  "v_mfma_f32_16x16x32_bf16 a[148:151], v[224:227], a[240:243], a[148:151]\n"
  "v_mfma_f32_16x16x32_bf16 a[180:183], v[224:227], a[244:247], a[180:183]\n"
  "v_mfma_f32_16x16x32_bf16 a[212:215], v[224:227], a[248:251], a[212:215]\n"
  "buffer_load_dwordx4 v[88:91], v234, s[40:43], s61 offen\n"
  "v_mfma_f32_16x16x32_bf16 a[24:27], v[220:223], a[224:227], a[24:27]\n"
  "v_mfma_f32_16x16x32_bf16 a[56:59], v[220:223], a[228:231], a[56:59]\n"
  "v_perm_b32 v224, v135, v131, s71\n"
  "v_perm_b32 v225, v143, v139, s71\n"
  "v_mfma_f32_16x16x32_bf16 a[88:91], v[220:223], a[232:235], a[88:91]\n"
  "v_perm_b32 v226, v151, v147, s71\n"
  "v_perm_b32 v227, v159, v155, s71\n"
  "v_mfma_f32_16x16x32_bf16 a[120:123], v[220:223], a[236:239], a[120:123]\n"
  "v_mfma_f32_16x16x32_bf16 a[152:155], v[220:223], a[240:243], a[152:155]\n"
  "buffer_load_dwordx4 v[92:95], v235, s[40:43], s61 offen\n"
  "v_mfma_f32_16x16x32_bf16 a[184:187], v[220:223], a[244:247], a[184:187]\n"
  "v_mfma_f32_16x16x32_bf16 a[216:219], v[220:223], a[248:251], a[216:219]\n"
  "v_mfma_f32_16x16x32_bf16 a[28:31], v[224:227], a[224:227], a[28:31]\n"
  "v_mfma_f32_16x16x32_bf16 a[60:63], v[224:227], a[228:231], a[60:63]\n"
  "s_waitcnt vmcnt(29)\n"
  "v_mfma_f32_16x16x32_bf16 a[92:95], v[224:227], a[232:235], a[92:95]\n"
  "v_perm_b32 v220, v164, v160, s70\n"
  "v_perm_b32 v221, v172, v168, s70\n"
  "v_mfma_f32_16x16x32_bf16 a[124:127], v[224:227], a[236:239], a[124:127]\n"
  "v_perm_b32 v222, v180, v176, s70\n"
  "v_perm_b32 v223, v188, v184, s70\n"
  "v_mfma_f32_16x16x32_bf16 a[156:159], v[224:227], a[240:243], a[156:159]\n"
  "buffer_load_dwordx4 v244, s[44:47], s77 offen offset:1024 nt lds\n"
  "v_mfma_f32_16x16x32_bf16 a[188:191], v[224:227], a[244:247], a[188:191]\n"
  "v_mfma_f32_16x16x32_bf16 a[220:223], v[224:227], a[248:251], a[220:223]\n"
  "s_waitcnt lgkmcnt(0)\n"
  "s_waitcnt vmcnt(38)\n"
  "s_barrier\n"
  "v_add_u32 v237, 43008, v237\n"
  "v_mfma_f32_16x16x32_bf16 a[0:3], v[220:223], v[192:195], a[0:3]\n"
  "ds_read_b128 a[224:227], v237 offset:0\n"
  "ds_read_b128 a[228:231], v237 offset:2048\n"
  "v_mfma_f32_16x16x32_bf16 a[32:35], v[220:223], v[196:199], a[32:35]\n"
  "v_perm_b32 v224, v164, v160, s71\n"
  "v_perm_b32 v225, v172, v168, s71\n"
  "v_mfma_f32_16x16x32_bf16 a[64:67], v[220:223], v[200:203], a[64:67]\n"
  "v_perm_b32 v226, v180, v176, s71\n"
  "v_perm_b32 v227, v188, v184, s71\n"
  "v_mfma_f32_16x16x32_bf16 a[96:99], v[220:223], v[204:207], a[96:99]\n"
  "v_mfma_f32_16x16x32_bf16 a[128:131], v[220:223], v[208:211], a[128:131]\n"
  "ds_read_b128 a[232:235], v237 offset:4096\n"
  "ds_read_b128 a[236:239], v237 offset:6144\n"
  "v_mfma_f32_16x16x32_bf16 a[160:163], v[220:223], v[212:215], a[160:163]\n"
  "s_add_u32 s61, s61, 0x40000\n"
  "ds_read_b128 a[240:243], v237 offset:8192\n"
  "ds_read_b128 a[244:247], v237 offset:10240\n"
  "v_mfma_f32_16x16x32_bf16 a[192:195], v[220:223], v[216:219], a[192:195]\n"
  "buffer_load_dwordx4 v[96:99], v228, s[40:43], s61 offen\n"
  "ds_read_b128 a[248:251], v237 offset:12288\n"
  "v_mfma_f32_16x16x32_bf16 a[4:7], v[224:227], v[192:195], a[4:7]\n"
  "v_mfma_f32_16x16x32_bf16 a[36:39], v[224:227], v[196:199], a[36:39]\n"
  "v_perm_b32 v220, v165, v161, s70\n"
  "v_perm_b32 v221, v173, v169, s70\n"
  "v_mfma_f32_16x16x32_bf16 a[68:71], v[224:227], v[200:203], a[68:71]\n"
  "v_perm_b32 v222, v181, v177, s70\n"
  "v_perm_b32 v223, v189, v185, s70\n"
  "v_mfma_f32_16x16x32_bf16 a[100:103], v[224:227], v[204:207], a[100:103]\n"
  "v_mfma_f32_16x16x32_bf16 a[132:135], v[224:227], v[208:211], a[132:135]\n"
  "v_mfma_f32_16x16x32_bf16 a[164:167], v[224:227], v[212:215], a[164:167]\n"
  "buffer_load_dwordx4 v[100:103], v229, s[40:43], s61 offen\n"
  "v_mfma_f32_16x16x32_bf16 a[196:199], v[224:227], v[216:219], a[196:199]\n"
  "v_mfma_f32_16x16x32_bf16 a[8:11], v[220:223], v[192:195], a[8:11]\n"
  "v_mfma_f32_16x16x32_bf16 a[40:43], v[220:223], v[196:199], a[40:43]\n"
  "v_perm_b32 v224, v165, v161, s71\n"
  "v_perm_b32 v225, v173, v169, s71\n"
  "v_mfma_f32_16x16x32_bf16 a[72:75], v[220:223], v[200:203], a[72:75]\n"
  "v_perm_b32 v226, v181, v177, s71\n"
  "v_perm_b32 v227, v189, v185, s71\n"
  "v_mfma_f32_16x16x32_bf16 a[104:107], v[220:223], v[204:207], a[104:107]\n"
  "buffer_load_dwordx4 v244, s[44:47], s77 offen offset:2048 nt lds\n"
  "v_mfma_f32_16x16x32_bf16 a[136:139], v[220:223], v[208:211], a[136:139]\n"
  "v_mfma_f32_16x16x32_bf16 a[168:171], v[220:223], v[212:215], a[168:171]\n"
  "v_mfma_f32_16x16x32_bf16 a[200:203], v[220:223], v[216:219], a[200:203]\n"
  "v_mfma_f32_16x16x32_bf16 a[12:15], v[224:227], v[192:195], a[12:15]\n"
  "v_mfma_f32_16x16x32_bf16 a[44:47], v[224:227], v[196:199], a[44:47]\n"
  "v_perm_b32 v220, v166, v162, s70\n"
  "v_perm_b32 v221, v174, v170, s70\n"
  "v_mfma_f32_16x16x32_bf16 a[76:79], v[224:227], v[200:203], a[76:79]\n"
  "v_perm_b32 v222, v182, v178, s70\n"
  "v_perm_b32 v223, v190, v186, s70\n"
  "v_mfma_f32_16x16x32_bf16 a[108:111], v[224:227], v[204:207], a[108:111]\n"
  "buffer_load_dwordx4 v[104:107], v230, s[40:43], s61 offen\n"
  "v_mfma_f32_16x16x32_bf16 a[140:143], v[224:227], v[208:211], a[140:143]\n"
  "v_mfma_f32_16x16x32_bf16 a[172:175], v[224:227], v[212:215], a[172:175]\n"
  "v_mfma_f32_16x16x32_bf16 a[204:207], v[224:227], v[216:219], a[204:207]\n"
  "v_mfma_f32_16x16x32_bf16 a[16:19], v[220:223], v[192:195], a[16:19]\n"
  "buffer_load_dwordx4 v[108:111], v231, s[40:43], s61 offen\n"
  "v_mfma_f32_16x16x32_bf16 a[48:51], v[220:223], v[196:199], a[48:51]\n"
  "v_perm_b32 v224, v166, v162, s71\n"
  "v_perm_b32 v225, v174, v170, s71\n"
  "v_mfma_f32_16x16x32_bf16 a[80:83], v[220:223], v[200:203], a[80:83]\n"
  "v_perm_b32 v226, v182, v178, s71\n"
  "v_perm_b32 v227, v190, v186, s71\n"
  "v_mfma_f32_16x16x32_bf16 a[112:115], v[220:223], v[204:207], a[112:115]\n"
  "v_mfma_f32_16x16x32_bf16 a[144:147], v[220:223], v[208:211], a[144:147]\n"
  "v_mfma_f32_16x16x32_bf16 a[176:179], v[220:223], v[212:215], a[176:179]\n"
  "v_mfma_f32_16x16x32_bf16 a[208:211], v[220:223], v[216:219], a[208:211]\n"
  "v_mfma_f32_16x16x32_bf16 a[20:23], v[224:227], v[192:195], a[20:23]\n"
  "buffer_load_dwordx4 v244, s[44:47], s77 offen offset:3072 nt lds\n"
  "v_mfma_f32_16x16x32_bf16 a[52:55], v[224:227], v[196:199], a[52:55]\n"
  "v_perm_b32 v220, v167, v163, s70\n"
  "v_perm_b32 v221, v175, v171, s70\n"
  "v_mfma_f32_16x16x32_bf16 a[84:87], v[224:227], v[200:203], a[84:87]\n"
  "v_perm_b32 v222, v183, v179, s70\n"
  "v_perm_b32 v223, v191, v187, s70\n"
  "v_mfma_f32_16x16x32_bf16 a[116:119], v[224:227], v[204:207], a[116:119]\n"
  "v_mfma_f32_16x16x32_bf16 a[148:151], v[224:227], v[208:211], a[148:151]\n"
  "v_mfma_f32_16x16x32_bf16 a[180:183], v[224:227], v[212:215], a[180:183]\n"
  "v_mfma_f32_16x16x32_bf16 a[212:215], v[224:227], v[216:219], a[212:215]\n"
  "buffer_load_dwordx4 v[112:115], v232, s[40:43], s61 offen\n"
  "v_mfma_f32_16x16x32_bf16 a[24:27], v[220:223], v[192:195], a[24:27]\n"
  "v_mfma_f32_16x16x32_bf16 a[56:59], v[220:223], v[196:199], a[56:59]\n"
  "v_perm_b32 v224, v167, v163, s71\n"
  "v_perm_b32 v225, v175, v171, s71\n"
  "v_mfma_f32_16x16x32_bf16 a[88:91], v[220:223], v[200:203], a[88:91]\n"
  "v_perm_b32 v226, v183, v179, s71\n"
  "v_perm_b32 v227, v191, v187, s71\n"
  "v_mfma_f32_16x16x32_bf16 a[120:123], v[220:223], v[204:207], a[120:123]\n"
  "v_mfma_f32_16x16x32_bf16 a[152:155], v[220:223], v[208:211], a[152:155]\n"
  "buffer_load_dwordx4 v[116:119], v233, s[40:43], s61 offen\n"
  "v_mfma_f32_16x16x32_bf16 a[184:187], v[220:223], v[212:215], a[184:187]\n"
  "v_mfma_f32_16x16x32_bf16 a[216:219], v[220:223], v[216:219], a[216:219]\n"
  "v_mfma_f32_16x16x32_bf16 a[28:31], v[224:227], v[192:195], a[28:31]\n"
  "v_mfma_f32_16x16x32_bf16 a[60:63], v[224:227], v[196:199], a[60:63]\n"
  "s_waitcnt vmcnt(30)\n"
  "s_add_u32 s76, s76, 0x2000\n"
  "s_and_b32 s76, s76, 0x1ffff\n"
  "v_mfma_f32_16x16x32_bf16 a[92:95], v[224:227], v[200:203], a[92:95]\n"
  "s_add_u32 s77, s76, 4096\n"
  "v_perm_b32 v220, v4, v0, s70\n"
  "v_perm_b32 v221, v12, v8, s70\n"
  "v_mfma_f32_16x16x32_bf16 a[124:127], v[224:227], v[204:207], a[124:127]\n"
  "s_add_u32 m0, s72, 33024\n"
  "v_perm_b32 v222, v20, v16, s70\n"
  "v_perm_b32 v223, v28, v24, s70\n"
  "v_mfma_f32_16x16x32_bf16 a[156:159], v[224:227], v[208:211], a[156:159]\n"
  "buffer_load_dwordx4 v244, s[44:47], s76 offen nt lds\n"
  "v_mfma_f32_16x16x32_bf16 a[188:191], v[224:227], v[212:215], a[188:191]\n"
  "v_mfma_f32_16x16x32_bf16 a[220:223], v[224:227], v[216:219], a[220:223]\n"
  "s_waitcnt lgkmcnt(0)\n"
  "v_add_u32 v238, 43008, v238\n"
  "v_mfma_f32_16x16x32_bf16 a[0:3], v[220:223], a[224:227], a[0:3]\n"
  "ds_read_b128 v[192:195], v238 offset:0\n"
  "ds_read_b128 v[196:199], v238 offset:2048\n"
  "v_mfma_f32_16x16x32_bf16 a[32:35], v[220:223], a[228:231], a[32:35]\n"
  "v_perm_b32 v224, v4, v0, s71\n"
  "v_perm_b32 v225, v12, v8, s71\n"
  "v_mfma_f32_16x16x32_bf16 a[64:67], v[220:223], a[232:235], a[64:67]\n"
  "v_perm_b32 v226, v20, v16, s71\n"
  "v_perm_b32 v227, v28, v24, s71\n"
  "v_mfma_f32_16x16x32_bf16 a[96:99], v[220:223], a[236:239], a[96:99]\n"
  "v_mfma_f32_16x16x32_bf16 a[128:131], v[220:223], a[240:243], a[128:131]\n"
  "ds_read_b128 v[200:203], v238 offset:4096\n"
  "ds_read_b128 v[204:207], v238 offset:6144\n"
  "v_mfma_f32_16x16x32_bf16 a[160:163], v[220:223], a[244:247], a[160:163]\n"
  "ds_read_b128 v[208:211], v238 offset:8192\n"
  "ds_read_b128 v[212:215], v238 offset:10240\n"
  "v_mfma_f32_16x16x32_bf16 a[192:195], v[220:223], a[248:251], a[192:195]\n"
  "buffer_load_dwordx4 v[120:123], v234, s[40:43], s61 offen\n"
  "ds_read_b128 v[216:219], v238 offset:12288\n"
  "v_mfma_f32_16x16x32_bf16 a[4:7], v[224:227], a[224:227], a[4:7]\n"
  "v_mfma_f32_16x16x32_bf16 a[36:39], v[224:227], a[228:231], a[36:39]\n"
  "v_perm_b32 v220, v5, v1, s70\n"
  "v_perm_b32 v221, v13, v9, s70\n"
  "v_mfma_f32_16x16x32_bf16 a[68:71], v[224:227], a[232:235], a[68:71]\n"
  "v_perm_b32 v222, v21, v17, s70\n"
  "v_perm_b32 v223, v29, v25, s70\n"
  "v_mfma_f32_16x16x32_bf16 a[100:103], v[224:227], a[236:239], a[100:103]\n"
  "v_mfma_f32_16x16x32_bf16 a[132:135], v[224:227], a[240:243], a[132:135]\n"
  "v_mfma_f32_16x16x32_bf16 a[164:167], v[224:227], a[244:247], a[164:167]\n"
  "buffer_load_dwordx4 v[124:127], v235, s[40:43], s61 offen\n"
  "v_mfma_f32_16x16x32_bf16 a[196:199], v[224:227], a[248:251], a[196:199]\n"
  "v_mfma_f32_16x16x32_bf16 a[8:11], v[220:223], a[224:227], a[8:11]\n"
  "v_mfma_f32_16x16x32_bf16 a[40:43], v[220:223], a[228:231], a[40:43]\n"
  "v_perm_b32 v224, v5, v1, s71\n"
  "v_perm_b32 v225, v13, v9, s71\n"
  "v_mfma_f32_16x16x32_bf16 a[72:75], v[220:223], a[232:235], a[72:75]\n"
  "v_perm_b32 v226, v21, v17, s71\n"
  "v_perm_b32 v227, v29, v25, s71\n"
  "v_mfma_f32_16x16x32_bf16 a[104:107], v[220:223], a[236:239], a[104:107]\n"
  "buffer_load_dwordx4 v244, s[44:47], s76 offen offset:1024 nt lds\n"
  "v_mfma_f32_16x16x32_bf16 a[136:139], v[220:223], a[240:243], a[136:139]\n"
  "v_mfma_f32_16x16x32_bf16 a[168:171], v[220:223], a[244:247], a[168:171]\n"
  "v_mfma_f32_16x16x32_bf16 a[200:203], v[220:223], a[248:251], a[200:203]\n"
  "v_mfma_f32_16x16x32_bf16 a[12:15], v[224:227], a[224:227], a[12:15]\n"
  "s_add_u32 s61, s61, 0x40000\n"
  "v_mfma_f32_16x16x32_bf16 a[44:47], v[224:227], a[228:231], a[44:47]\n"
  "s_cmp_eq_u32 s61, 0x480000\n"
  "v_perm_b32 v220, v6, v2, s70\n"
  "v_perm_b32 v221, v14, v10, s70\n"
  "v_mfma_f32_16x16x32_bf16 a[76:79], v[224:227], a[232:235], a[76:79]\n"
  "s_cselect_b32 s61, 0, s61\n"
  "v_perm_b32 v222, v22, v18, s70\n"
  "v_perm_b32 v223, v30, v26, s70\n"
  "v_mfma_f32_16x16x32_bf16 a[108:111], v[224:227], a[236:239], a[108:111]\n"
  "buffer_load_dwordx4 v[128:131], v228, s[40:43], s61 offen\n"
  "v_mfma_f32_16x16x32_bf16 a[140:143], v[224:227], a[240:243], a[140:143]\n"
  "v_mfma_f32_16x16x32_bf16 a[172:175], v[224:227], a[244:247], a[172:175]\n"
  "v_mfma_f32_16x16x32_bf16 a[204:207], v[224:227], a[248:251], a[204:207]\n"
  "v_mfma_f32_16x16x32_bf16 a[16:19], v[220:223], a[224:227], a[16:19]\n"
  "buffer_load_dwordx4 v[132:135], v229, s[40:43], s61 offen\n"
  "v_mfma_f32_16x16x32_bf16 a[48:51], v[220:223], a[228:231], a[48:51]\n"
  "v_perm_b32 v224, v6, v2, s71\n"
  "v_perm_b32 v225, v14, v10, s71\n"
  "v_mfma_f32_16x16x32_bf16 a[80:83], v[220:223], a[232:235], a[80:83]\n"
  "v_perm_b32 v226, v22, v18, s71\n"
  "v_perm_b32 v227, v30, v26, s71\n"
  "v_mfma_f32_16x16x32_bf16 a[112:115], v[220:223], a[236:239], a[112:115]\n"
  "v_mfma_f32_16x16x32_bf16 a[144:147], v[220:223], a[240:243], a[144:147]\n"
  "v_mfma_f32_16x16x32_bf16 a[176:179], v[220:223], a[244:247], a[176:179]\n"
  "v_mfma_f32_16x16x32_bf16 a[208:211], v[220:223], a[248:251], a[208:211]\n"
  "v_mfma_f32_16x16x32_bf16 a[20:23], v[224:227], a[224:227], a[20:23]\n"
  "buffer_load_dwordx4 v244, s[44:47], s76 offen offset:2048 nt lds\n"
  "v_mfma_f32_16x16x32_bf16 a[52:55], v[224:227], a[228:231], a[52:55]\n"
  "v_perm_b32 v220, v7, v3, s70\n"
  "v_perm_b32 v221, v15, v11, s70\n"
  "v_mfma_f32_16x16x32_bf16 a[84:87], v[224:227], a[232:235], a[84:87]\n"
  "v_perm_b32 v222, v23, v19, s70\n"
  "v_perm_b32 v223, v31, v27, s70\n"
  "v_mfma_f32_16x16x32_bf16 a[116:119], v[224:227], a[236:239], a[116:119]\n"
  "v_mfma_f32_16x16x32_bf16 a[148:151], v[224:227], a[240:243], a[148:151]\n"
  "v_mfma_f32_16x16x32_bf16 a[180:183], v[224:227], a[244:247], a[180:183]\n"
  "v_mfma_f32_16x16x32_bf16 a[212:215], v[224:227], a[248:251], a[212:215]\n"
  "buffer_load_dwordx4 v[136:139], v230, s[40:43], s61 offen\n"
  "v_mfma_f32_16x16x32_bf16 a[24:27], v[220:223], a[224:227], a[24:27]\n"
  "v_mfma_f32_16x16x32_bf16 a[56:59], v[220:223], a[228:231], a[56:59]\n"
  "v_perm_b32 v224, v7, v3, s71\n"
  "v_perm_b32 v225, v15, v11, s71\n"
  "v_mfma_f32_16x16x32_bf16 a[88:91], v[220:223], a[232:235], a[88:91]\n"
  "v_perm_b32 v226, v23, v19, s71\n"
  "v_perm_b32 v227, v31, v27, s71\n"
  "v_mfma_f32_16x16x32_bf16 a[120:123], v[220:223], a[236:239], a[120:123]\n"
  "v_mfma_f32_16x16x32_bf16 a[152:155], v[220:223], a[240:243], a[152:155]\n"
  "buffer_load_dwordx4 v[140:143], v231, s[40:43], s61 offen\n"
  "v_mfma_f32_16x16x32_bf16 a[184:187], v[220:223], a[244:247], a[184:187]\n"
  "v_mfma_f32_16x16x32_bf16 a[216:219], v[220:223], a[248:251], a[216:219]\n"
  "v_mfma_f32_16x16x32_bf16 a[28:31], v[224:227], a[224:227], a[28:31]\n"
  "v_mfma_f32_16x16x32_bf16 a[60:63], v[224:227], a[228:231], a[60:63]\n"
  "s_waitcnt vmcnt(30)\n"
  "v_mfma_f32_16x16x32_bf16 a[92:95], v[224:227], a[232:235], a[92:95]\n"
  "v_perm_b32 v220, v36, v32, s70\n"
  "v_perm_b32 v221, v44, v40, s70\n"
  "v_mfma_f32_16x16x32_bf16 a[124:127], v[224:227], a[236:239], a[124:127]\n"
  "v_perm_b32 v222, v52, v48, s70\n"
  "v_perm_b32 v223, v60, v56, s70\n"
  "v_mfma_f32_16x16x32_bf16 a[156:159], v[224:227], a[240:243], a[156:159]\n"
  "buffer_load_dwordx4 v244, s[44:47], s76 offen offset:3072 nt lds\n"
  "v_mfma_f32_16x16x32_bf16 a[188:191], v[224:227], a[244:247], a[188:191]\n"
  "v_mfma_f32_16x16x32_bf16 a[220:223], v[224:227], a[248:251], a[220:223]\n"
  "s_waitcnt lgkmcnt(0)\n"
  "s_barrier\n"
  "v_mfma_f32_16x16x32_bf16 a[0:3], v[220:223], v[192:195], a[0:3]\n"
  "ds_read_b128 a[224:227], v237 offset:14336\n"
  "ds_read_b128 a[228:231], v237 offset:16384\n"
  "v_mfma_f32_16x16x32_bf16 a[32:35], v[220:223], v[196:199], a[32:35]\n"
  "v_perm_b32 v224, v36, v32, s71\n"
  "v_perm_b32 v225, v44, v40, s71\n"
  "v_mfma_f32_16x16x32_bf16 a[64:67], v[220:223], v[200:203], a[64:67]\n"
  "v_perm_b32 v226, v52, v48, s71\n"
  "v_perm_b32 v227, v60, v56, s71\n"
  "v_mfma_f32_16x16x32_bf16 a[96:99], v[220:223], v[204:207], a[96:99]\n"
  "v_mfma_f32_16x16x32_bf16 a[128:131], v[220:223], v[208:211], a[128:131]\n"
  "ds_read_b128 a[232:235], v237 offset:18432\n"
  "ds_read_b128 a[236:239], v237 offset:20480\n"
  "v_mfma_f32_16x16x32_bf16 a[160:163], v[220:223], v[212:215], a[160:163]\n"
  "ds_read_b128 a[240:243], v237 offset:22528\n"
  "ds_read_b128 a[244:247], v237 offset:24576\n"
  "v_mfma_f32_16x16x32_bf16 a[192:195], v[220:223], v[216:219], a[192:195]\n"
  "buffer_load_dwordx4 v[144:147], v232, s[40:43], s61 offen\n"
  "ds_read_b128 a[248:251], v237 offset:26624\n"
  "v_mfma_f32_16x16x32_bf16 a[4:7], v[224:227], v[192:195], a[4:7]\n"
  "v_mfma_f32_16x16x32_bf16 a[36:39], v[224:227], v[196:199], a[36:39]\n"
  "v_perm_b32 v220, v37, v33, s70\n"
  "v_perm_b32 v221, v45, v41, s70\n"
  "v_mfma_f32_16x16x32_bf16 a[68:71], v[224:227], v[200:203], a[68:71]\n"
  "v_perm_b32 v222, v53, v49, s70\n"
  "v_perm_b32 v223, v61, v57, s70\n"
  "v_mfma_f32_16x16x32_bf16 a[100:103], v[224:227], v[204:207], a[100:103]\n"
  "v_mfma_f32_16x16x32_bf16 a[132:135], v[224:227], v[208:211], a[132:135]\n"
  "v_mfma_f32_16x16x32_bf16 a[164:167], v[224:227], v[212:215], a[164:167]\n"
  "buffer_load_dwordx4 v[148:151], v233, s[40:43], s61 offen\n"
  "v_mfma_f32_16x16x32_bf16 a[196:199], v[224:227], v[216:219], a[196:199]\n"
  "v_mfma_f32_16x16x32_bf16 a[8:11], v[220:223], v[192:195], a[8:11]\n"
  "v_mfma_f32_16x16x32_bf16 a[40:43], v[220:223], v[196:199], a[40:43]\n"
  "v_perm_b32 v224, v37, v33, s71\n"
  "v_perm_b32 v225, v45, v41, s71\n"
  "v_mfma_f32_16x16x32_bf16 a[72:75], v[220:223], v[200:203], a[72:75]\n"
  "s_add_u32 m0, s72, 37152\n"
  "v_perm_b32 v226, v53, v49, s71\n"
  "v_perm_b32 v227, v61, v57, s71\n"
  "v_mfma_f32_16x16x32_bf16 a[104:107], v[220:223], v[204:207], a[104:107]\n"
  "buffer_load_dwordx4 v244, s[44:47], s77 offen nt lds\n"
  "v_mfma_f32_16x16x32_bf16 a[136:139], v[220:223], v[208:211], a[136:139]\n"
  "v_mfma_f32_16x16x32_bf16 a[168:171], v[220:223], v[212:215], a[168:171]\n"
  "v_mfma_f32_16x16x32_bf16 a[200:203], v[220:223], v[216:219], a[200:203]\n"
  "v_mfma_f32_16x16x32_bf16 a[12:15], v[224:227], v[192:195], a[12:15]\n"
  "v_mfma_f32_16x16x32_bf16 a[44:47], v[224:227], v[196:199], a[44:47]\n"
  "v_perm_b32 v220, v38, v34, s70\n"
  "v_perm_b32 v221, v46, v42, s70\n"
  "v_mfma_f32_16x16x32_bf16 a[76:79], v[224:227], v[200:203], a[76:79]\n"
  "v_perm_b32 v222, v54, v50, s70\n"
  "v_perm_b32 v223, v62, v58, s70\n"
  "v_mfma_f32_16x16x32_bf16 a[108:111], v[224:227], v[204:207], a[108:111]\n"
  "buffer_load_dwordx4 v[152:155], v234, s[40:43], s61 offen\n"
  "v_mfma_f32_16x16x32_bf16 a[140:143], v[224:227], v[208:211], a[140:143]\n"
  "v_mfma_f32_16x16x32_bf16 a[172:175], v[224:227], v[212:215], a[172:175]\n"
  "v_mfma_f32_16x16x32_bf16 a[204:207], v[224:227], v[216:219], a[204:207]\n"
  "v_mfma_f32_16x16x32_bf16 a[16:19], v[220:223], v[192:195], a[16:19]\n"
  "buffer_load_dwordx4 v[156:159], v235, s[40:43], s61 offen\n"
  "v_mfma_f32_16x16x32_bf16 a[48:51], v[220:223], v[196:199], a[48:51]\n"
  "v_perm_b32 v224, v38, v34, s71\n"
  "v_perm_b32 v225, v46, v42, s71\n"
  "v_mfma_f32_16x16x32_bf16 a[80:83], v[220:223], v[200:203], a[80:83]\n"
  "v_perm_b32 v226, v54, v50, s71\n"
  "v_perm_b32 v227, v62, v58, s71\n"
  "v_mfma_f32_16x16x32_bf16 a[112:115], v[220:223], v[204:207], a[112:115]\n"
  "v_mfma_f32_16x16x32_bf16 a[144:147], v[220:223], v[208:211], a[144:147]\n"
  "v_mfma_f32_16x16x32_bf16 a[176:179], v[220:223], v[212:215], a[176:179]\n"
  "v_mfma_f32_16x16x32_bf16 a[208:211], v[220:223], v[216:219], a[208:211]\n"
  "v_mfma_f32_16x16x32_bf16 a[20:23], v[224:227], v[192:195], a[20:23]\n"
  "buffer_load_dwordx4 v244, s[44:47], s77 offen offset:1024 nt lds\n"
  "v_mfma_f32_16x16x32_bf16 a[52:55], v[224:227], v[196:199], a[52:55]\n"
  "v_perm_b32 v220, v39, v35, s70\n"
  "v_perm_b32 v221, v47, v43, s70\n"
  "v_mfma_f32_16x16x32_bf16 a[84:87], v[224:227], v[200:203], a[84:87]\n"
  "v_perm_b32 v222, v55, v51, s70\n"
  "v_perm_b32 v223, v63, v59, s70\n"
  "v_mfma_f32_16x16x32_bf16 a[116:119], v[224:227], v[204:207], a[116:119]\n"
  "v_mfma_f32_16x16x32_bf16 a[148:151], v[224:227], v[208:211], a[148:151]\n"
  "v_mfma_f32_16x16x32_bf16 a[180:183], v[224:227], v[212:215], a[180:183]\n"
  "s_add_u32 s61, s61, 0x40000\n"
  "v_mfma_f32_16x16x32_bf16 a[212:215], v[224:227], v[216:219], a[212:215]\n"
  "buffer_load_dwordx4 v[160:163], v228, s[40:43], s61 offen\n"
  "v_mfma_f32_16x16x32_bf16 a[24:27], v[220:223], v[192:195], a[24:27]\n"
  "v_mfma_f32_16x16x32_bf16 a[56:59], v[220:223], v[196:199], a[56:59]\n"
  "v_perm_b32 v224, v39, v35, s71\n"
  "v_perm_b32 v225, v47, v43, s71\n"
  "v_mfma_f32_16x16x32_bf16 a[88:91], v[220:223], v[200:203], a[88:91]\n"
  "v_perm_b32 v226, v55, v51, s71\n"
  "v_perm_b32 v227, v63, v59, s71\n"
  "v_mfma_f32_16x16x32_bf16 a[120:123], v[220:223], v[204:207], a[120:123]\n"
  "v_mfma_f32_16x16x32_bf16 a[152:155], v[220:223], v[208:211], a[152:155]\n"
  "buffer_load_dwordx4 v[164:167], v229, s[40:43], s61 offen\n"
  "v_mfma_f32_16x16x32_bf16 a[184:187], v[220:223], v[212:215], a[184:187]\n"
  "v_mfma_f32_16x16x32_bf16 a[216:219], v[220:223], v[216:219], a[216:219]\n"
  "v_mfma_f32_16x16x32_bf16 a[28:31], v[224:227], v[192:195], a[28:31]\n"
  "v_mfma_f32_16x16x32_bf16 a[60:63], v[224:227], v[196:199], a[60:63]\n"
  "s_waitcnt vmcnt(27)\n"
  "v_mfma_f32_16x16x32_bf16 a[92:95], v[224:227], v[200:203], a[92:95]\n"
  "v_perm_b32 v220, v68, v64, s70\n"
  "v_perm_b32 v221, v76, v72, s70\n"
  "v_mfma_f32_16x16x32_bf16 a[124:127], v[224:227], v[204:207], a[124:127]\n"
  "v_perm_b32 v222, v84, v80, s70\n"
  "v_perm_b32 v223, v92, v88, s70\n"
  "v_mfma_f32_16x16x32_bf16 a[156:159], v[224:227], v[208:211], a[156:159]\n"
  "buffer_load_dwordx4 v244, s[44:47], s77 offen offset:2048 nt lds\n"
  "v_mfma_f32_16x16x32_bf16 a[188:191], v[224:227], v[212:215], a[188:191]\n"
  "v_mfma_f32_16x16x32_bf16 a[220:223], v[224:227], v[216:219], a[220:223]\n"
  "s_waitcnt lgkmcnt(0)\n"
  "v_mfma_f32_16x16x32_bf16 a[0:3], v[220:223], a[224:227], a[0:3]\n"
  "ds_read_b128 v[192:195], v238 offset:14336\n"
  "ds_read_b128 v[196:199], v238 offset:16384\n"
  "v_mfma_f32_16x16x32_bf16 a[32:35], v[220:223], a[228:231], a[32:35]\n"
  "v_perm_b32 v224, v68, v64, s71\n"
  "v_perm_b32 v225, v76, v72, s71\n"
  "v_mfma_f32_16x16x32_bf16 a[64:67], v[220:223], a[232:235], a[64:67]\n"
  "v_perm_b32 v226, v84, v80, s71\n"
  "v_perm_b32 v227, v92, v88, s71\n"
  "v_mfma_f32_16x16x32_bf16 a[96:99], v[220:223], a[236:239], a[96:99]\n"
  "v_mfma_f32_16x16x32_bf16 a[128:131], v[220:223], a[240:243], a[128:131]\n"
  "ds_read_b128 v[200:203], v238 offset:18432\n"
  "ds_read_b128 v[204:207], v238 offset:20480\n"
  "v_mfma_f32_16x16x32_bf16 a[160:163], v[220:223], a[244:247], a[160:163]\n"
  "ds_read_b128 v[208:211], v238 offset:22528\n"
  "ds_read_b128 v[212:215], v238 offset:24576\n"
  "v_mfma_f32_16x16x32_bf16 a[192:195], v[220:223], a[248:251], a[192:195]\n"
  "buffer_load_dwordx4 v[168:171], v230, s[40:43], s61 offen\n"
  "ds_read_b128 v[216:219], v238 offset:26624\n"
  "v_mfma_f32_16x16x32_bf16 a[4:7], v[224:227], a[224:227], a[4:7]\n"
  "v_mfma_f32_16x16x32_bf16 a[36:39], v[224:227], a[228:231], a[36:39]\n"
  "v_perm_b32 v220, v69, v65, s70\n"
  "v_perm_b32 v221, v77, v73, s70\n"
  "v_mfma_f32_16x16x32_bf16 a[68:71], v[224:227], a[232:235], a[68:71]\n"
  "v_perm_b32 v222, v85, v81, s70\n"
  "v_perm_b32 v223, v93, v89, s70\n"
  "v_mfma_f32_16x16x32_bf16 a[100:103], v[224:227], a[236:239], a[100:103]\n"
  "v_mfma_f32_16x16x32_bf16 a[132:135], v[224:227], a[240:243], a[132:135]\n"
  "v_mfma_f32_16x16x32_bf16 a[164:167], v[224:227], a[244:247], a[164:167]\n"
  "buffer_load_dwordx4 v[172:175], v231, s[40:43], s61 offen\n"
  "v_mfma_f32_16x16x32_bf16 a[196:199], v[224:227], a[248:251], a[196:199]\n"
  "v_mfma_f32_16x16x32_bf16 a[8:11], v[220:223], a[224:227], a[8:11]\n"
  "v_mfma_f32_16x16x32_bf16 a[40:43], v[220:223], a[228:231], a[40:43]\n"
  "v_perm_b32 v224, v69, v65, s71\n"
  "v_perm_b32 v225, v77, v73, s71\n"
  "v_mfma_f32_16x16x32_bf16 a[72:75], v[220:223], a[232:235], a[72:75]\n"
  "v_perm_b32 v226, v85, v81, s71\n"
  "v_perm_b32 v227, v93, v89, s71\n"
  "v_mfma_f32_16x16x32_bf16 a[104:107], v[220:223], a[236:239], a[104:107]\n"
  "buffer_load_dwordx4 v244, s[44:47], s77 offen offset:3072 nt lds\n"
  "v_mfma_f32_16x16x32_bf16 a[136:139], v[220:223], a[240:243], a[136:139]\n"
  "v_mfma_f32_16x16x32_bf16 a[168:171], v[220:223], a[244:247], a[168:171]\n"
  "v_mfma_f32_16x16x32_bf16 a[200:203], v[220:223], a[248:251], a[200:203]\n"
  "v_mfma_f32_16x16x32_bf16 a[12:15], v[224:227], a[224:227], a[12:15]\n"
  "v_mfma_f32_16x16x32_bf16 a[44:47], v[224:227], a[228:231], a[44:47]\n"
  "v_perm_b32 v220, v70, v66, s70\n"
  "v_perm_b32 v221, v78, v74, s70\n"
  "v_mfma_f32_16x16x32_bf16 a[76:79], v[224:227], a[232:235], a[76:79]\n"
  "v_perm_b32 v222, v86, v82, s70\n"
  "v_perm_b32 v223, v94, v90, s70\n"
  "v_mfma_f32_16x16x32_bf16 a[108:111], v[224:227], a[236:239], a[108:111]\n"
  "buffer_load_dwordx4 v[176:179], v232, s[40:43], s61 offen\n"
  "v_mfma_f32_16x16x32_bf16 a[140:143], v[224:227], a[240:243], a[140:143]\n"
  "v_mfma_f32_16x16x32_bf16 a[172:175], v[224:227], a[244:247], a[172:175]\n"
  "v_mfma_f32_16x16x32_bf16 a[204:207], v[224:227], a[248:251], a[204:207]\n"
  "v_mfma_f32_16x16x32_bf16 a[16:19], v[220:223], a[224:227], a[16:19]\n"
  "buffer_load_dwordx4 v[180:183], v233, s[40:43], s61 offen\n"
  "v_mfma_f32_16x16x32_bf16 a[48:51], v[220:223], a[228:231], a[48:51]\n"
  "v_perm_b32 v224, v70, v66, s71\n"
  "v_perm_b32 v225, v78, v74, s71\n"
  "v_mfma_f32_16x16x32_bf16 a[80:83], v[220:223], a[232:235], a[80:83]\n"
  "v_perm_b32 v226, v86, v82, s71\n"
  "v_perm_b32 v227, v94, v90, s71\n"
  "v_mfma_f32_16x16x32_bf16 a[112:115], v[220:223], a[236:239], a[112:115]\n"
  "s_add_u32 s78, s78, 0x2000\n"
  "v_mfma_f32_16x16x32_bf16 a[144:147], v[220:223], a[240:243], a[144:147]\n"
  "s_and_b32 s78, s78, 0x1ffff\n"
  "v_mfma_f32_16x16x32_bf16 a[176:179], v[220:223], a[244:247], a[176:179]\n"
  "s_add_u32 s79, s78, 4096\n"
  "v_mfma_f32_16x16x32_bf16 a[208:211], v[220:223], a[248:251], a[208:211]\n"
  "s_add_u32 m0, s72, 66048\n"
  "v_mfma_f32_16x16x32_bf16 a[20:23], v[224:227], a[224:227], a[20:23]\n"
  "buffer_load_dwordx4 v245, s[48:51], s78 offen nt lds\n"
  "v_mfma_f32_16x16x32_bf16 a[52:55], v[224:227], a[228:231], a[52:55]\n"
  "v_perm_b32 v220, v71, v67, s70\n"
  "v_perm_b32 v221, v79, v75, s70\n"
  "v_mfma_f32_16x16x32_bf16 a[84:87], v[224:227], a[232:235], a[84:87]\n"
  "v_perm_b32 v222, v87, v83, s70\n"
  "v_perm_b32 v223, v95, v91, s70\n"
  "v_mfma_f32_16x16x32_bf16 a[116:119], v[224:227], a[236:239], a[116:119]\n"
  "v_mfma_f32_16x16x32_bf16 a[148:151], v[224:227], a[240:243], a[148:151]\n"
  "v_mfma_f32_16x16x32_bf16 a[180:183], v[224:227], a[244:247], a[180:183]\n"
  "v_mfma_f32_16x16x32_bf16 a[212:215], v[224:227], a[248:251], a[212:215]\n"
  "buffer_load_dwordx4 v[184:187], v234, s[40:43], s61 offen\n"
  "v_mfma_f32_16x16x32_bf16 a[24:27], v[220:223], a[224:227], a[24:27]\n"
  "v_mfma_f32_16x16x32_bf16 a[56:59], v[220:223], a[228:231], a[56:59]\n"
  "v_perm_b32 v224, v71, v67, s71\n"
  "v_perm_b32 v225, v79, v75, s71\n"
  "v_mfma_f32_16x16x32_bf16 a[88:91], v[220:223], a[232:235], a[88:91]\n"
  "v_perm_b32 v226, v87, v83, s71\n"
  "v_perm_b32 v227, v95, v91, s71\n"
  "v_mfma_f32_16x16x32_bf16 a[120:123], v[220:223], a[236:239], a[120:123]\n"
  "v_mfma_f32_16x16x32_bf16 a[152:155], v[220:223], a[240:243], a[152:155]\n"
  "buffer_load_dwordx4 v[188:191], v235, s[40:43], s61 offen\n"
  "v_mfma_f32_16x16x32_bf16 a[184:187], v[220:223], a[244:247], a[184:187]\n"
  "v_mfma_f32_16x16x32_bf16 a[216:219], v[220:223], a[248:251], a[216:219]\n"
  "v_mfma_f32_16x16x32_bf16 a[28:31], v[224:227], a[224:227], a[28:31]\n"
  "v_mfma_f32_16x16x32_bf16 a[60:63], v[224:227], a[228:231], a[60:63]\n"
  "s_waitcnt vmcnt(24)\n"
  "v_mfma_f32_16x16x32_bf16 a[92:95], v[224:227], a[232:235], a[92:95]\n"
  "v_perm_b32 v220, v100, v96, s70\n"
  "v_perm_b32 v221, v108, v104, s70\n"
  "v_mfma_f32_16x16x32_bf16 a[124:127], v[224:227], a[236:239], a[124:127]\n"
  "v_perm_b32 v222, v116, v112, s70\n"
  "v_perm_b32 v223, v124, v120, s70\n"
  "v_mfma_f32_16x16x32_bf16 a[156:159], v[224:227], a[240:243], a[156:159]\n"
  "buffer_load_dwordx4 v245, s[48:51], s78 offen offset:1024 nt lds\n"
  "v_mfma_f32_16x16x32_bf16 a[188:191], v[224:227], a[244:247], a[188:191]\n"
  "v_mfma_f32_16x16x32_bf16 a[220:223], v[224:227], a[248:251], a[220:223]\n"
  "s_waitcnt lgkmcnt(0)\n"
  "v_mfma_f32_16x16x32_bf16 a[0:3], v[220:223], v[192:195], a[0:3]\n"
  "ds_read_b128 a[224:227], v237 offset:28672\n"
  "ds_read_b128 a[228:231], v237 offset:30720\n"
  "v_mfma_f32_16x16x32_bf16 a[32:35], v[220:223], v[196:199], a[32:35]\n"
  "v_perm_b32 v224, v100, v96, s71\n"
  "v_perm_b32 v225, v108, v104, s71\n"
  "v_mfma_f32_16x16x32_bf16 a[64:67], v[220:223], v[200:203], a[64:67]\n"
  "v_perm_b32 v226, v116, v112, s71\n"
  "v_perm_b32 v227, v124, v120, s71\n"
  "v_mfma_f32_16x16x32_bf16 a[96:99], v[220:223], v[204:207], a[96:99]\n"
  "v_mfma_f32_16x16x32_bf16 a[128:131], v[220:223], v[208:211], a[128:131]\n"
  "ds_read_b128 a[232:235], v237 offset:32768\n"
  "ds_read_b128 a[236:239], v237 offset:34816\n"
  "v_mfma_f32_16x16x32_bf16 a[160:163], v[220:223], v[212:215], a[160:163]\n"
  "ds_read_b128 a[240:243], v237 offset:36864\n"
  "ds_read_b128 a[244:247], v237 offset:38912\n"
  "v_mfma_f32_16x16x32_bf16 a[192:195], v[220:223], v[216:219], a[192:195]\n"
  "ds_read_b128 a[248:251], v237 offset:40960\n"
  "v_mfma_f32_16x16x32_bf16 a[4:7], v[224:227], v[192:195], a[4:7]\n"
  "v_mfma_f32_16x16x32_bf16 a[36:39], v[224:227], v[196:199], a[36:39]\n"
  "v_perm_b32 v220, v101, v97, s70\n"
  "v_perm_b32 v221, v109, v105, s70\n"
  "v_mfma_f32_16x16x32_bf16 a[68:71], v[224:227], v[200:203], a[68:71]\n"
  "v_perm_b32 v222, v117, v113, s70\n"
  "v_perm_b32 v223, v125, v121, s70\n"
  "v_mfma_f32_16x16x32_bf16 a[100:103], v[224:227], v[204:207], a[100:103]\n"
  "v_mfma_f32_16x16x32_bf16 a[132:135], v[224:227], v[208:211], a[132:135]\n"
  "v_mfma_f32_16x16x32_bf16 a[164:167], v[224:227], v[212:215], a[164:167]\n"
  "buffer_load_dwordx4 v245, s[48:51], s78 offen offset:2048 nt lds\n"
  "v_mfma_f32_16x16x32_bf16 a[196:199], v[224:227], v[216:219], a[196:199]\n"
  "v_mfma_f32_16x16x32_bf16 a[8:11], v[220:223], v[192:195], a[8:11]\n"
  "v_mfma_f32_16x16x32_bf16 a[40:43], v[220:223], v[196:199], a[40:43]\n"
  "v_perm_b32 v224, v101, v97, s71\n"
  "v_perm_b32 v225, v109, v105, s71\n"
  "v_mfma_f32_16x16x32_bf16 a[72:75], v[220:223], v[200:203], a[72:75]\n"
  "v_perm_b32 v226, v117, v113, s71\n"
  "v_perm_b32 v227, v125, v121, s71\n"
  "v_mfma_f32_16x16x32_bf16 a[104:107], v[220:223], v[204:207], a[104:107]\n"
  "v_mfma_f32_16x16x32_bf16 a[136:139], v[220:223], v[208:211], a[136:139]\n"
  "v_mfma_f32_16x16x32_bf16 a[168:171], v[220:223], v[212:215], a[168:171]\n"
  "v_mfma_f32_16x16x32_bf16 a[200:203], v[220:223], v[216:219], a[200:203]\n"
  "v_mfma_f32_16x16x32_bf16 a[12:15], v[224:227], v[192:195], a[12:15]\n"
  "v_mfma_f32_16x16x32_bf16 a[44:47], v[224:227], v[196:199], a[44:47]\n"
  "v_perm_b32 v220, v102, v98, s70\n"
  "v_perm_b32 v221, v110, v106, s70\n"
  "v_mfma_f32_16x16x32_bf16 a[76:79], v[224:227], v[200:203], a[76:79]\n"
  "v_perm_b32 v222, v118, v114, s70\n"
  "v_perm_b32 v223, v126, v122, s70\n"
  "v_mfma_f32_16x16x32_bf16 a[108:111], v[224:227], v[204:207], a[108:111]\n"
  "v_mfma_f32_16x16x32_bf16 a[140:143], v[224:227], v[208:211], a[140:143]\n"
  "v_mfma_f32_16x16x32_bf16 a[172:175], v[224:227], v[212:215], a[172:175]\n"
  "v_mfma_f32_16x16x32_bf16 a[204:207], v[224:227], v[216:219], a[204:207]\n"
  "v_mfma_f32_16x16x32_bf16 a[16:19], v[220:223], v[192:195], a[16:19]\n"
  "buffer_load_dwordx4 v245, s[48:51], s78 offen offset:3072 nt lds\n"
  "v_mfma_f32_16x16x32_bf16 a[48:51], v[220:223], v[196:199], a[48:51]\n"
  "v_perm_b32 v224, v102, v98, s71\n"
  "v_perm_b32 v225, v110, v106, s71\n"
  "v_mfma_f32_16x16x32_bf16 a[80:83], v[220:223], v[200:203], a[80:83]\n"
  "v_perm_b32 v226, v118, v114, s71\n"
  "v_perm_b32 v227, v126, v122, s71\n"
  "v_mfma_f32_16x16x32_bf16 a[112:115], v[220:223], v[204:207], a[112:115]\n"
  "v_mfma_f32_16x16x32_bf16 a[144:147], v[220:223], v[208:211], a[144:147]\n"
  "v_mfma_f32_16x16x32_bf16 a[176:179], v[220:223], v[212:215], a[176:179]\n"
  "v_mfma_f32_16x16x32_bf16 a[208:211], v[220:223], v[216:219], a[208:211]\n"
  "v_mfma_f32_16x16x32_bf16 a[20:23], v[224:227], v[192:195], a[20:23]\n"
  "v_mfma_f32_16x16x32_bf16 a[52:55], v[224:227], v[196:199], a[52:55]\n"
  "v_perm_b32 v220, v103, v99, s70\n"
  "v_perm_b32 v221, v111, v107, s70\n"
  "v_mfma_f32_16x16x32_bf16 a[84:87], v[224:227], v[200:203], a[84:87]\n"
  "v_perm_b32 v222, v119, v115, s70\n"
  "v_perm_b32 v223, v127, v123, s70\n"
  "v_mfma_f32_16x16x32_bf16 a[116:119], v[224:227], v[204:207], a[116:119]\n"
  "v_mfma_f32_16x16x32_bf16 a[148:151], v[224:227], v[208:211], a[148:151]\n"
  "v_mfma_f32_16x16x32_bf16 a[180:183], v[224:227], v[212:215], a[180:183]\n"
  "v_mfma_f32_16x16x32_bf16 a[212:215], v[224:227], v[216:219], a[212:215]\n"
  "v_mfma_f32_16x16x32_bf16 a[24:27], v[220:223], v[192:195], a[24:27]\n"
  "v_mfma_f32_16x16x32_bf16 a[56:59], v[220:223], v[196:199], a[56:59]\n"
  "v_perm_b32 v224, v103, v99, s71\n"
  "v_perm_b32 v225, v111, v107, s71\n"
  "v_mfma_f32_16x16x32_bf16 a[88:91], v[220:223], v[200:203], a[88:91]\n"
  "v_perm_b32 v226, v119, v115, s71\n"
  "v_perm_b32 v227, v127, v123, s71\n"
  "v_mfma_f32_16x16x32_bf16 a[120:123], v[220:223], v[204:207], a[120:123]\n"
  "s_add_u32 m0, s72, 70176\n"
  "v_mfma_f32_16x16x32_bf16 a[152:155], v[220:223], v[208:211], a[152:155]\n"
  "buffer_load_dwordx4 v245, s[48:51], s79 offen nt lds\n"
  "v_mfma_f32_16x16x32_bf16 a[184:187], v[220:223], v[212:215], a[184:187]\n"
  "v_mfma_f32_16x16x32_bf16 a[216:219], v[220:223], v[216:219], a[216:219]\n"
  "v_mfma_f32_16x16x32_bf16 a[28:31], v[224:227], v[192:195], a[28:31]\n"
  "v_mfma_f32_16x16x32_bf16 a[60:63], v[224:227], v[196:199], a[60:63]\n"
  "s_waitcnt vmcnt(16)\n"
  "v_mfma_f32_16x16x32_bf16 a[92:95], v[224:227], v[200:203], a[92:95]\n"
  "v_perm_b32 v220, v132, v128, s70\n"
  "v_perm_b32 v221, v140, v136, s70\n"
  "v_mfma_f32_16x16x32_bf16 a[124:127], v[224:227], v[204:207], a[124:127]\n"
  "v_perm_b32 v222, v148, v144, s70\n"
  "v_perm_b32 v223, v156, v152, s70\n"
  "v_mfma_f32_16x16x32_bf16 a[156:159], v[224:227], v[208:211], a[156:159]\n"
  "v_mfma_f32_16x16x32_bf16 a[188:191], v[224:227], v[212:215], a[188:191]\n"
  "v_mfma_f32_16x16x32_bf16 a[220:223], v[224:227], v[216:219], a[220:223]\n"
  "s_waitcnt lgkmcnt(0)\n"
  "v_mfma_f32_16x16x32_bf16 a[0:3], v[220:223], a[224:227], a[0:3]\n"
  "ds_read_b128 v[192:195], v238 offset:28672\n"
  "ds_read_b128 v[196:199], v238 offset:30720\n"
  "v_mfma_f32_16x16x32_bf16 a[32:35], v[220:223], a[228:231], a[32:35]\n"
  "v_perm_b32 v224, v132, v128, s71\n"
  "v_perm_b32 v225, v140, v136, s71\n"
  "v_mfma_f32_16x16x32_bf16 a[64:67], v[220:223], a[232:235], a[64:67]\n"
  "v_perm_b32 v226, v148, v144, s71\n"
  "v_perm_b32 v227, v156, v152, s71\n"
  "v_mfma_f32_16x16x32_bf16 a[96:99], v[220:223], a[236:239], a[96:99]\n"
  "v_mfma_f32_16x16x32_bf16 a[128:131], v[220:223], a[240:243], a[128:131]\n"
  "ds_read_b128 v[200:203], v238 offset:32768\n"
  "ds_read_b128 v[204:207], v238 offset:34816\n"
  "v_mfma_f32_16x16x32_bf16 a[160:163], v[220:223], a[244:247], a[160:163]\n"
  "ds_read_b128 v[208:211], v238 offset:36864\n"
  "ds_read_b128 v[212:215], v238 offset:38912\n"
  "v_mfma_f32_16x16x32_bf16 a[192:195], v[220:223], a[248:251], a[192:195]\n"
  "ds_read_b128 v[216:219], v238 offset:40960\n"
  "v_mfma_f32_16x16x32_bf16 a[4:7], v[224:227], a[224:227], a[4:7]\n"
  "v_mfma_f32_16x16x32_bf16 a[36:39], v[224:227], a[228:231], a[36:39]\n"
  "v_perm_b32 v220, v133, v129, s70\n"
  "v_perm_b32 v221, v141, v137, s70\n"
  "v_mfma_f32_16x16x32_bf16 a[68:71], v[224:227], a[232:235], a[68:71]\n"
  "v_perm_b32 v222, v149, v145, s70\n"
  "v_perm_b32 v223, v157, v153, s70\n"
  "v_mfma_f32_16x16x32_bf16 a[100:103], v[224:227], a[236:239], a[100:103]\n"
  "v_mfma_f32_16x16x32_bf16 a[132:135], v[224:227], a[240:243], a[132:135]\n"
  "v_mfma_f32_16x16x32_bf16 a[164:167], v[224:227], a[244:247], a[164:167]\n"
  "buffer_load_dwordx4 v245, s[48:51], s79 offen offset:1024 nt lds\n"
  "v_mfma_f32_16x16x32_bf16 a[196:199], v[224:227], a[248:251], a[196:199]\n"
  "v_mfma_f32_16x16x32_bf16 a[8:11], v[220:223], a[224:227], a[8:11]\n"
  "v_mfma_f32_16x16x32_bf16 a[40:43], v[220:223], a[228:231], a[40:43]\n"
  "v_perm_b32 v224, v133, v129, s71\n"
  "v_perm_b32 v225, v141, v137, s71\n"
  "v_mfma_f32_16x16x32_bf16 a[72:75], v[220:223], a[232:235], a[72:75]\n"
  "v_perm_b32 v226, v149, v145, s71\n"
  "v_perm_b32 v227, v157, v153, s71\n"
  "v_mfma_f32_16x16x32_bf16 a[104:107], v[220:223], a[236:239], a[104:107]\n"
  "v_mfma_f32_16x16x32_bf16 a[136:139], v[220:223], a[240:243], a[136:139]\n"
  "v_mfma_f32_16x16x32_bf16 a[168:171], v[220:223], a[244:247], a[168:171]\n"
  "v_mfma_f32_16x16x32_bf16 a[200:203], v[220:223], a[248:251], a[200:203]\n"
  "v_mfma_f32_16x16x32_bf16 a[12:15], v[224:227], a[224:227], a[12:15]\n"
  "v_mfma_f32_16x16x32_bf16 a[44:47], v[224:227], a[228:231], a[44:47]\n"
  "v_perm_b32 v220, v134, v130, s70\n"
  "v_perm_b32 v221, v142, v138, s70\n"
  "v_mfma_f32_16x16x32_bf16 a[76:79], v[224:227], a[232:235], a[76:79]\n"
  "v_perm_b32 v222, v150, v146, s70\n"
  "v_perm_b32 v223, v158, v154, s70\n"
  "v_mfma_f32_16x16x32_bf16 a[108:111], v[224:227], a[236:239], a[108:111]\n"
  "v_mfma_f32_16x16x32_bf16 a[140:143], v[224:227], a[240:243], a[140:143]\n"
  "v_mfma_f32_16x16x32_bf16 a[172:175], v[224:227], a[244:247], a[172:175]\n"
  "v_mfma_f32_16x16x32_bf16 a[204:207], v[224:227], a[248:251], a[204:207]\n"
  "v_mfma_f32_16x16x32_bf16 a[16:19], v[220:223], a[224:227], a[16:19]\n"
  "buffer_load_dwordx4 v245, s[48:51], s79 offen offset:2048 nt lds\n"
  "v_mfma_f32_16x16x32_bf16 a[48:51], v[220:223], a[228:231], a[48:51]\n"
  "v_perm_b32 v224, v134, v130, s71\n"
  "v_perm_b32 v225, v142, v138, s71\n"
  "v_mfma_f32_16x16x32_bf16 a[80:83], v[220:223], a[232:235], a[80:83]\n"
  "v_perm_b32 v226, v150, v146, s71\n"
  "v_perm_b32 v227, v158, v154, s71\n"
  "v_mfma_f32_16x16x32_bf16 a[112:115], v[220:223], a[236:239], a[112:115]\n"
  "v_mfma_f32_16x16x32_bf16 a[144:147], v[220:223], a[240:243], a[144:147]\n"
  "v_mfma_f32_16x16x32_bf16 a[176:179], v[220:223], a[244:247], a[176:179]\n"
  "v_mfma_f32_16x16x32_bf16 a[208:211], v[220:223], a[248:251], a[208:211]\n"
  "v_mfma_f32_16x16x32_bf16 a[20:23], v[224:227], a[224:227], a[20:23]\n"
  "v_mfma_f32_16x16x32_bf16 a[52:55], v[224:227], a[228:231], a[52:55]\n"
  "v_perm_b32 v220, v135, v131, s70\n"
  "v_perm_b32 v221, v143, v139, s70\n"
  "v_mfma_f32_16x16x32_bf16 a[84:87], v[224:227], a[232:235], a[84:87]\n"
  "v_perm_b32 v222, v151, v147, s70\n"
  "v_perm_b32 v223, v159, v155, s70\n"
  "v_mfma_f32_16x16x32_bf16 a[116:119], v[224:227], a[236:239], a[116:119]\n"
  "v_mfma_f32_16x16x32_bf16 a[148:151], v[224:227], a[240:243], a[148:151]\n"
  "v_mfma_f32_16x16x32_bf16 a[180:183], v[224:227], a[244:247], a[180:183]\n"
  "v_mfma_f32_16x16x32_bf16 a[212:215], v[224:227], a[248:251], a[212:215]\n"
  "v_mfma_f32_16x16x32_bf16 a[24:27], v[220:223], a[224:227], a[24:27]\n"
  "v_mfma_f32_16x16x32_bf16 a[56:59], v[220:223], a[228:231], a[56:59]\n"
  "v_perm_b32 v224, v135, v131, s71\n"
  "v_perm_b32 v225, v143, v139, s71\n"
  "v_mfma_f32_16x16x32_bf16 a[88:91], v[220:223], a[232:235], a[88:91]\n"
  "v_perm_b32 v226, v151, v147, s71\n"
  "v_perm_b32 v227, v159, v155, s71\n"
  "v_mfma_f32_16x16x32_bf16 a[120:123], v[220:223], a[236:239], a[120:123]\n"
  "v_mfma_f32_16x16x32_bf16 a[152:155], v[220:223], a[240:243], a[152:155]\n"
  "buffer_load_dwordx4 v245, s[48:51], s79 offen offset:3072 nt lds\n"
  "v_mfma_f32_16x16x32_bf16 a[184:187], v[220:223], a[244:247], a[184:187]\n"
  "v_mfma_f32_16x16x32_bf16 a[216:219], v[220:223], a[248:251], a[216:219]\n"
  "v_mfma_f32_16x16x32_bf16 a[28:31], v[224:227], a[224:227], a[28:31]\n"
  "v_mfma_f32_16x16x32_bf16 a[60:63], v[224:227], a[228:231], a[60:63]\n"
  "s_waitcnt vmcnt(7)\n"
  "v_mfma_f32_16x16x32_bf16 a[92:95], v[224:227], a[232:235], a[92:95]\n"
  "v_perm_b32 v220, v164, v160, s70\n"
  "v_perm_b32 v221, v172, v168, s70\n"
  "v_mfma_f32_16x16x32_bf16 a[124:127], v[224:227], a[236:239], a[124:127]\n"
  "v_perm_b32 v222, v180, v176, s70\n"
  "v_perm_b32 v223, v188, v184, s70\n"
  "v_mfma_f32_16x16x32_bf16 a[156:159], v[224:227], a[240:243], a[156:159]\n"
  "v_mfma_f32_16x16x32_bf16 a[188:191], v[224:227], a[244:247], a[188:191]\n"
  "v_mfma_f32_16x16x32_bf16 a[220:223], v[224:227], a[248:251], a[220:223]\n"
  "s_waitcnt lgkmcnt(0)\n"
  "v_mfma_f32_16x16x32_bf16 a[0:3], v[220:223], v[192:195], a[0:3]\n"
  "v_mfma_f32_16x16x32_bf16 a[32:35], v[220:223], v[196:199], a[32:35]\n"
  "v_perm_b32 v224, v164, v160, s71\n"
  "v_perm_b32 v225, v172, v168, s71\n"
  "v_mfma_f32_16x16x32_bf16 a[64:67], v[220:223], v[200:203], a[64:67]\n"
  "v_perm_b32 v226, v180, v176, s71\n"
  "v_perm_b32 v227, v188, v184, s71\n"
  "v_mfma_f32_16x16x32_bf16 a[96:99], v[220:223], v[204:207], a[96:99]\n"
  "v_mfma_f32_16x16x32_bf16 a[128:131], v[220:223], v[208:211], a[128:131]\n"
  "v_mfma_f32_16x16x32_bf16 a[160:163], v[220:223], v[212:215], a[160:163]\n"
  "v_mfma_f32_16x16x32_bf16 a[192:195], v[220:223], v[216:219], a[192:195]\n"
  "v_mfma_f32_16x16x32_bf16 a[4:7], v[224:227], v[192:195], a[4:7]\n"
  "v_mfma_f32_16x16x32_bf16 a[36:39], v[224:227], v[196:199], a[36:39]\n"
  "s_add_u32 s78, s78, 0x2000\n"
  "v_perm_b32 v220, v165, v161, s70\n"
  "v_perm_b32 v221, v173, v169, s70\n"
  "v_mfma_f32_16x16x32_bf16 a[68:71], v[224:227], v[200:203], a[68:71]\n"
  "s_and_b32 s78, s78, 0x1ffff\n"
  "v_perm_b32 v222, v181, v177, s70\n"
  "v_perm_b32 v223, v189, v185, s70\n"
  "v_mfma_f32_16x16x32_bf16 a[100:103], v[224:227], v[204:207], a[100:103]\n"
  "s_add_u32 s79, s78, 4096\n"
  "v_mfma_f32_16x16x32_bf16 a[132:135], v[224:227], v[208:211], a[132:135]\n"
  "s_add_u32 m0, s72, 99072\n"
  "v_mfma_f32_16x16x32_bf16 a[164:167], v[224:227], v[212:215], a[164:167]\n"
  "buffer_load_dwordx4 v245, s[48:51], s78 offen nt lds\n"
  "v_mfma_f32_16x16x32_bf16 a[196:199], v[224:227], v[216:219], a[196:199]\n"
  "v_mfma_f32_16x16x32_bf16 a[8:11], v[220:223], v[192:195], a[8:11]\n"
  "v_mfma_f32_16x16x32_bf16 a[40:43], v[220:223], v[196:199], a[40:43]\n"
  "v_perm_b32 v224, v165, v161, s71\n"
  "v_perm_b32 v225, v173, v169, s71\n"
  "v_mfma_f32_16x16x32_bf16 a[72:75], v[220:223], v[200:203], a[72:75]\n"
  "v_perm_b32 v226, v181, v177, s71\n"
  "v_perm_b32 v227, v189, v185, s71\n"
  "v_mfma_f32_16x16x32_bf16 a[104:107], v[220:223], v[204:207], a[104:107]\n"
  "v_mfma_f32_16x16x32_bf16 a[136:139], v[220:223], v[208:211], a[136:139]\n"
  "v_mfma_f32_16x16x32_bf16 a[168:171], v[220:223], v[212:215], a[168:171]\n"
  "v_mfma_f32_16x16x32_bf16 a[200:203], v[220:223], v[216:219], a[200:203]\n"
  "v_mfma_f32_16x16x32_bf16 a[12:15], v[224:227], v[192:195], a[12:15]\n"
  "v_mfma_f32_16x16x32_bf16 a[44:47], v[224:227], v[196:199], a[44:47]\n"
  "v_perm_b32 v220, v166, v162, s70\n"
  "v_perm_b32 v221, v174, v170, s70\n"
  "v_mfma_f32_16x16x32_bf16 a[76:79], v[224:227], v[200:203], a[76:79]\n"
  "v_perm_b32 v222, v182, v178, s70\n"
  "v_perm_b32 v223, v190, v186, s70\n"
  "v_mfma_f32_16x16x32_bf16 a[108:111], v[224:227], v[204:207], a[108:111]\n"
  "v_mfma_f32_16x16x32_bf16 a[140:143], v[224:227], v[208:211], a[140:143]\n"
  "v_mfma_f32_16x16x32_bf16 a[172:175], v[224:227], v[212:215], a[172:175]\n"
  "v_mfma_f32_16x16x32_bf16 a[204:207], v[224:227], v[216:219], a[204:207]\n"
  "v_mfma_f32_16x16x32_bf16 a[16:19], v[220:223], v[192:195], a[16:19]\n"
  "buffer_load_dwordx4 v245, s[48:51], s78 offen offset:1024 nt lds\n"
  "v_mfma_f32_16x16x32_bf16 a[48:51], v[220:223], v[196:199], a[48:51]\n"
  "v_perm_b32 v224, v166, v162, s71\n"
  "v_perm_b32 v225, v174, v170, s71\n"
  "v_mfma_f32_16x16x32_bf16 a[80:83], v[220:223], v[200:203], a[80:83]\n"
  "v_perm_b32 v226, v182, v178, s71\n"
  "v_perm_b32 v227, v190, v186, s71\n"
  "v_mfma_f32_16x16x32_bf16 a[112:115], v[220:223], v[204:207], a[112:115]\n"
  "v_mfma_f32_16x16x32_bf16 a[144:147], v[220:223], v[208:211], a[144:147]\n"
  "v_mfma_f32_16x16x32_bf16 a[176:179], v[220:223], v[212:215], a[176:179]\n"
  "v_mfma_f32_16x16x32_bf16 a[208:211], v[220:223], v[216:219], a[208:211]\n"
  "v_mfma_f32_16x16x32_bf16 a[20:23], v[224:227], v[192:195], a[20:23]\n"
  "v_mfma_f32_16x16x32_bf16 a[52:55], v[224:227], v[196:199], a[52:55]\n"
  "v_perm_b32 v220, v167, v163, s70\n"
  "v_perm_b32 v221, v175, v171, s70\n"
  "v_mfma_f32_16x16x32_bf16 a[84:87], v[224:227], v[200:203], a[84:87]\n"
  "v_perm_b32 v222, v183, v179, s70\n"
  "v_perm_b32 v223, v191, v187, s70\n"
  "v_mfma_f32_16x16x32_bf16 a[116:119], v[224:227], v[204:207], a[116:119]\n"
  "v_mfma_f32_16x16x32_bf16 a[148:151], v[224:227], v[208:211], a[148:151]\n"
  "v_mfma_f32_16x16x32_bf16 a[180:183], v[224:227], v[212:215], a[180:183]\n"
  "v_mfma_f32_16x16x32_bf16 a[212:215], v[224:227], v[216:219], a[212:215]\n"
  "v_mfma_f32_16x16x32_bf16 a[24:27], v[220:223], v[192:195], a[24:27]\n"
  "v_mfma_f32_16x16x32_bf16 a[56:59], v[220:223], v[196:199], a[56:59]\n"
  "v_perm_b32 v224, v167, v163, s71\n"
  "v_perm_b32 v225, v175, v171, s71\n"
  "v_mfma_f32_16x16x32_bf16 a[88:91], v[220:223], v[200:203], a[88:91]\n"
  "v_perm_b32 v226, v183, v179, s71\n"
  "v_perm_b32 v227, v191, v187, s71\n"
  "v_mfma_f32_16x16x32_bf16 a[120:123], v[220:223], v[204:207], a[120:123]\n"
  "v_mfma_f32_16x16x32_bf16 a[152:155], v[220:223], v[208:211], a[152:155]\n"
  "buffer_load_dwordx4 v245, s[48:51], s78 offen offset:2048 nt lds\n"
  "v_mfma_f32_16x16x32_bf16 a[184:187], v[220:223], v[212:215], a[184:187]\n"
  "v_mfma_f32_16x16x32_bf16 a[216:219], v[220:223], v[216:219], a[216:219]\n"
  "v_mfma_f32_16x16x32_bf16 a[28:31], v[224:227], v[192:195], a[28:31]\n"
  "v_mfma_f32_16x16x32_bf16 a[60:63], v[224:227], v[196:199], a[60:63]\n"
  "v_mfma_f32_16x16x32_bf16 a[92:95], v[224:227], v[200:203], a[92:95]\n"
  "v_mfma_f32_16x16x32_bf16 a[124:127], v[224:227], v[204:207], a[124:127]\n"
  "v_mfma_f32_16x16x32_bf16 a[156:159], v[224:227], v[208:211], a[156:159]\n"
  "v_mfma_f32_16x16x32_bf16 a[188:191], v[224:227], v[212:215], a[188:191]\n"
  "v_mfma_f32_16x16x32_bf16 a[220:223], v[224:227], v[216:219], a[220:223]\n"
  "s_barrier\n"
  "v_mov_b32 v240, 0\n"
  "v_mov_b32 v241, 0\n"
  "v_mov_b32 v242, 0\n"
  "v_mov_b32 v243, 0\n"
  "v_accvgpr_read_b32 v196, a0\n"
  "v_accvgpr_read_b32 v197, a4\n"
  "v_accvgpr_read_b32 v198, a8\n"
  "v_accvgpr_read_b32 v199, a12\n"
  "v_accvgpr_read_b32 v200, a16\n"
  "v_accvgpr_read_b32 v201, a20\n"
  "v_accvgpr_read_b32 v202, a24\n"
  "v_accvgpr_read_b32 v203, a28\n"
  "v_mfma_f32_32x32x16_bf16 a[224:239], v[240:243], v[240:243], 0\n"
  "v_accvgpr_read_b32 v204, a1\n"
  "v_accvgpr_read_b32 v205, a5\n"
  "v_accvgpr_read_b32 v206, a9\n"
  "v_accvgpr_read_b32 v207, a13\n"
  "v_accvgpr_read_b32 v208, a17\n"
  "v_accvgpr_read_b32 v209, a21\n"
  "v_accvgpr_read_b32 v210, a25\n"
  "v_accvgpr_read_b32 v211, a29\n"
  "v_mfma_f32_32x32x16_bf16 a[240:255], v[240:243], v[240:243], 0\n"
  "v_accvgpr_read_b32 v212, a2\n"
  "v_accvgpr_read_b32 v213, a6\n"
  "v_accvgpr_read_b32 v214, a10\n"
  "v_accvgpr_read_b32 v215, a14\n"
  "v_accvgpr_read_b32 v216, a18\n"
  "v_accvgpr_read_b32 v217, a22\n"
  "v_accvgpr_read_b32 v218, a26\n"
  "v_accvgpr_read_b32 v219, a30\n"
  "v_accvgpr_read_b32 v220, a3\n"
  "v_accvgpr_read_b32 v221, a7\n"
  "v_accvgpr_read_b32 v222, a11\n"
  "v_accvgpr_read_b32 v223, a15\n"
  "v_accvgpr_read_b32 v128, a19\n"
  "v_accvgpr_read_b32 v129, a23\n"
  "v_accvgpr_read_b32 v130, a27\n"
  "v_accvgpr_read_b32 v131, a31\n"
  "s_waitcnt vmcnt(39)\n"
  "ds_read_b128 v[224:227], v246 offset:0\n"
  "ds_read_b128 v[228:231], v247 offset:0\n"
  "ds_read_b128 v[232:235], v246 offset:32\n"
  "ds_read_b128 v[236:239], v247 offset:32\n"
  "ds_read_b128 v[112:115], v246 offset:1024\n"
  "ds_read_b128 v[116:119], v247 offset:1024\n"
  "ds_read_b128 v[120:123], v246 offset:1056\n"
  "ds_read_b128 v[124:127], v247 offset:1056\n"
  "v_pk_mul_f32 v[196:197], v[196:197], s[80:81]\n"
  "v_pk_mul_f32 v[198:199], v[198:199], s[80:81]\n"
  "v_pk_mul_f32 v[200:201], v[200:201], s[80:81]\n"
  "v_pk_mul_f32 v[202:203], v[202:203], s[80:81]\n"
  "v_pk_mul_f32 v[204:205], v[204:205], s[80:81]\n"
  "v_pk_mul_f32 v[206:207], v[206:207], s[80:81]\n"
  "v_pk_mul_f32 v[208:209], v[208:209], s[80:81]\n"
  "v_pk_mul_f32 v[210:211], v[210:211], s[80:81]\n"
  "v_pk_mul_f32 v[212:213], v[212:213], s[80:81]\n"
  "v_pk_mul_f32 v[214:215], v[214:215], s[80:81]\n"
  "v_pk_mul_f32 v[216:217], v[216:217], s[80:81]\n"
  "v_pk_mul_f32 v[218:219], v[218:219], s[80:81]\n"
  "v_pk_mul_f32 v[220:221], v[220:221], s[80:81]\n"
  "v_pk_mul_f32 v[222:223], v[222:223], s[80:81]\n"
  "v_pk_mul_f32 v[128:129], v[128:129], s[80:81]\n"
  "v_pk_mul_f32 v[130:131], v[130:131], s[80:81]\n"
  "v_cvt_pk_bf16_f32 v0, v196, v197\n"
  "v_cvt_pk_bf16_f32 v1, v198, v199\n"
  "v_cvt_pk_bf16_f32 v2, v200, v201\n"
  "v_cvt_pk_bf16_f32 v3, v202, v203\n"
  "v_cvt_pk_bf16_f32 v4, v204, v205\n"
  "v_cvt_pk_bf16_f32 v5, v206, v207\n"
  "v_mfma_f32_32x32x16_bf16 a[0:15], v[240:243], v[240:243], 0\n"
  "v_cvt_pk_bf16_f32 v6, v208, v209\n"
  "v_cvt_pk_bf16_f32 v7, v210, v211\n"
  "v_cvt_pk_bf16_f32 v8, v212, v213\n"
  "v_cvt_pk_bf16_f32 v9, v214, v215\n"
  "v_cvt_pk_bf16_f32 v10, v216, v217\n"
  "v_cvt_pk_bf16_f32 v11, v218, v219\n"
  "v_mfma_f32_32x32x16_bf16 a[16:31], v[240:243], v[240:243], 0\n"
  "v_cvt_pk_bf16_f32 v12, v220, v221\n"
  "v_cvt_pk_bf16_f32 v13, v222, v223\n"
  "v_cvt_pk_bf16_f32 v14, v128, v129\n"
  "v_cvt_pk_bf16_f32 v15, v130, v131\n"
  "s_waitcnt lgkmcnt(0)\n"
  "v_accvgpr_read_b32 v196, a32\n"
  "v_accvgpr_read_b32 v197, a36\n"
  "buffer_load_dwordx4 v245, s[48:51], s78 offen offset:3072 nt lds\n"
  "v_accvgpr_read_b32 v198, a40\n"
  "v_mfma_f32_16x16x32_bf16 v[168:171], v[224:227], v[0:3], 0\n"
  "v_accvgpr_read_b32 v199, a44\n"
  "v_accvgpr_read_b32 v200, a48\n"
  "v_accvgpr_read_b32 v201, a52\n"
  "v_mfma_f32_16x16x32_bf16 v[168:171], v[228:231], v[4:7], v[168:171]\n"
  "v_accvgpr_read_b32 v202, a56\n"
  "v_accvgpr_read_b32 v203, a60\n"
  "v_accvgpr_read_b32 v204, a33\n"
  "v_mfma_f32_16x16x32_bf16 v[168:171], v[232:235], v[8:11], v[168:171]\n"
  "v_accvgpr_read_b32 v205, a37\n"
  "v_accvgpr_read_b32 v206, a41\n"
  "v_accvgpr_read_b32 v207, a45\n"
  "v_mfma_f32_16x16x32_bf16 v[168:171], v[236:239], v[12:15], v[168:171]\n"
  "v_accvgpr_read_b32 v208, a49\n"
  "v_accvgpr_read_b32 v209, a53\n"
  "v_accvgpr_read_b32 v210, a57\n"
  "v_mfma_f32_16x16x32_bf16 v[140:143], v[112:115], v[0:3], 0\n"
  "v_accvgpr_read_b32 v211, a61\n"
  "v_accvgpr_read_b32 v212, a34\n"
  "v_accvgpr_read_b32 v213, a38\n"
  "v_mfma_f32_16x16x32_bf16 v[140:143], v[116:119], v[4:7], v[140:143]\n"
  "v_accvgpr_read_b32 v214, a42\n"
  "v_accvgpr_read_b32 v215, a46\n"
  "v_accvgpr_read_b32 v216, a50\n"
  "v_mfma_f32_16x16x32_bf16 v[140:143], v[120:123], v[8:11], v[140:143]\n"
  "v_accvgpr_read_b32 v217, a54\n"
  "v_accvgpr_read_b32 v218, a58\n"
  "v_accvgpr_read_b32 v219, a62\n"
  "v_mfma_f32_16x16x32_bf16 v[140:143], v[124:127], v[12:15], v[140:143]\n"
  "v_accvgpr_read_b32 v220, a35\n"
  "v_accvgpr_read_b32 v221, a39\n"
  "v_accvgpr_read_b32 v222, a43\n"
  "v_accvgpr_read_b32 v223, a47\n"
  "v_accvgpr_read_b32 v128, a51\n"
  "v_accvgpr_read_b32 v129, a55\n"
  "v_accvgpr_read_b32 v130, a59\n"
  "v_accvgpr_read_b32 v131, a63\n"
  "v_pk_mul_f32 v[196:197], v[196:197], s[80:81]\n"
  "v_pk_mul_f32 v[198:199], v[198:199], s[80:81]\n"
  "v_pk_mul_f32 v[200:201], v[200:201], s[80:81]\n"
  "v_pk_mul_f32 v[202:203], v[202:203], s[80:81]\n"
  "v_pk_mul_f32 v[204:205], v[204:205], s[80:81]\n"
  "s_add_u32 m0, s72, 103200\n"
  "v_pk_mul_f32 v[206:207], v[206:207], s[80:81]\n"
  "buffer_load_dwordx4 v245, s[48:51], s79 offen nt lds\n"
  "v_pk_mul_f32 v[208:209], v[208:209], s[80:81]\n"
  "v_pk_mul_f32 v[210:211], v[210:211], s[80:81]\n"
  "v_pk_mul_f32 v[212:213], v[212:213], s[80:81]\n"
  "v_pk_mul_f32 v[214:215], v[214:215], s[80:81]\n"
  "v_pk_mul_f32 v[216:217], v[216:217], s[80:81]\n"
  "v_pk_mul_f32 v[218:219], v[218:219], s[80:81]\n"
  "v_pk_mul_f32 v[220:221], v[220:221], s[80:81]\n"
  "v_pk_mul_f32 v[222:223], v[222:223], s[80:81]\n"
  "v_pk_mul_f32 v[128:129], v[128:129], s[80:81]\n"
  "v_pk_mul_f32 v[130:131], v[130:131], s[80:81]\n"
  "v_cvt_pk_bf16_f32 v16, v196, v197\n"
  "v_cvt_pk_bf16_f32 v17, v198, v199\n"
  "v_cvt_pk_bf16_f32 v18, v200, v201\n"
  "v_cvt_pk_bf16_f32 v19, v202, v203\n"
  "v_cvt_pk_bf16_f32 v20, v204, v205\n"
  "v_mfma_f32_32x32x16_bf16 a[32:47], v[240:243], v[240:243], 0\n"
  "v_cvt_pk_bf16_f32 v21, v206, v207\n"
  "v_cvt_pk_bf16_f32 v22, v208, v209\n"
  "v_cvt_pk_bf16_f32 v23, v210, v211\n"
  "v_cvt_pk_bf16_f32 v24, v212, v213\n"
  "v_cvt_pk_bf16_f32 v25, v214, v215\n"
  "v_mfma_f32_32x32x16_bf16 a[48:63], v[240:243], v[240:243], 0\n"
  "v_cvt_pk_bf16_f32 v26, v216, v217\n"
  "v_cvt_pk_bf16_f32 v27, v218, v219\n"
  "v_cvt_pk_bf16_f32 v28, v220, v221\n"
  "v_cvt_pk_bf16_f32 v29, v222, v223\n"
  "v_cvt_pk_bf16_f32 v30, v128, v129\n"
  "v_cvt_pk_bf16_f32 v31, v130, v131\n"
  "v_accvgpr_read_b32 v196, a64\n"
  "v_accvgpr_read_b32 v197, a68\n"
  "v_accvgpr_read_b32 v198, a72\n"
  "v_mfma_f32_16x16x32_bf16 v[172:175], v[224:227], v[16:19], 0\n"
  "v_accvgpr_read_b32 v199, a76\n"
  "v_accvgpr_read_b32 v200, a80\n"
  "v_accvgpr_read_b32 v201, a84\n"
  "v_mfma_f32_16x16x32_bf16 v[172:175], v[228:231], v[20:23], v[172:175]\n"
  "v_accvgpr_read_b32 v202, a88\n"
  "v_accvgpr_read_b32 v203, a92\n"
  "v_accvgpr_read_b32 v204, a65\n"
  "v_mfma_f32_16x16x32_bf16 v[172:175], v[232:235], v[24:27], v[172:175]\n"
  "v_accvgpr_read_b32 v205, a69\n"
  "v_accvgpr_read_b32 v206, a73\n"
  "v_accvgpr_read_b32 v207, a77\n"
  "v_mfma_f32_16x16x32_bf16 v[172:175], v[236:239], v[28:31], v[172:175]\n"
  "buffer_load_dwordx4 v245, s[48:51], s79 offen offset:1024 nt lds\n"
  "v_accvgpr_read_b32 v208, a81\n"
  "v_accvgpr_read_b32 v209, a85\n"
  "v_accvgpr_read_b32 v210, a89\n"
  "v_mfma_f32_16x16x32_bf16 v[144:147], v[112:115], v[16:19], 0\n"
  "v_accvgpr_read_b32 v211, a93\n"
  "v_accvgpr_read_b32 v212, a66\n"
  "v_accvgpr_read_b32 v213, a70\n"
  "v_mfma_f32_16x16x32_bf16 v[144:147], v[116:119], v[20:23], v[144:147]\n"
  "v_accvgpr_read_b32 v214, a74\n"
  "v_accvgpr_read_b32 v215, a78\n"
  "v_accvgpr_read_b32 v216, a82\n"
  "v_mfma_f32_16x16x32_bf16 v[144:147], v[120:123], v[24:27], v[144:147]\n"
  "v_accvgpr_read_b32 v217, a86\n"
  "v_accvgpr_read_b32 v218, a90\n"
  "v_accvgpr_read_b32 v219, a94\n"
  "v_mfma_f32_16x16x32_bf16 v[144:147], v[124:127], v[28:31], v[144:147]\n"
  "v_accvgpr_read_b32 v220, a67\n"
  "v_accvgpr_read_b32 v221, a71\n"
  "v_accvgpr_read_b32 v222, a75\n"
  "v_accvgpr_read_b32 v223, a79\n"
  "v_accvgpr_read_b32 v128, a83\n"
  "v_accvgpr_read_b32 v129, a87\n"
  "v_accvgpr_read_b32 v130, a91\n"
  "v_accvgpr_read_b32 v131, a95\n"
  "v_pk_mul_f32 v[196:197], v[196:197], s[80:81]\n"
  "v_pk_mul_f32 v[198:199], v[198:199], s[80:81]\n"
  "v_pk_mul_f32 v[200:201], v[200:201], s[80:81]\n"
  "v_pk_mul_f32 v[202:203], v[202:203], s[80:81]\n"
  "v_pk_mul_f32 v[204:205], v[204:205], s[80:81]\n"
  "v_pk_mul_f32 v[206:207], v[206:207], s[80:81]\n"
  "v_pk_mul_f32 v[208:209], v[208:209], s[80:81]\n"
  "v_pk_mul_f32 v[210:211], v[210:211], s[80:81]\n"
  "v_pk_mul_f32 v[212:213], v[212:213], s[80:81]\n"
  "v_pk_mul_f32 v[214:215], v[214:215], s[80:81]\n"
  "v_pk_mul_f32 v[216:217], v[216:217], s[80:81]\n"
  "v_pk_mul_f32 v[218:219], v[218:219], s[80:81]\n"
  "v_pk_mul_f32 v[220:221], v[220:221], s[80:81]\n"
  "v_pk_mul_f32 v[222:223], v[222:223], s[80:81]\n"
  "v_pk_mul_f32 v[128:129], v[128:129], s[80:81]\n"
  "v_pk_mul_f32 v[130:131], v[130:131], s[80:81]\n"
  "v_cvt_pk_bf16_f32 v32, v196, v197\n"
  "v_cvt_pk_bf16_f32 v33, v198, v199\n"
  "v_cvt_pk_bf16_f32 v34, v200, v201\n"
  "v_cvt_pk_bf16_f32 v35, v202, v203\n"
  "buffer_load_dwordx4 v245, s[48:51], s79 offen offset:2048 nt lds\n"
  "v_cvt_pk_bf16_f32 v36, v204, v205\n"
  "v_mfma_f32_32x32x16_bf16 a[64:79], v[240:243], v[240:243], 0\n"
  "v_cvt_pk_bf16_f32 v37, v206, v207\n"
  "v_cvt_pk_bf16_f32 v38, v208, v209\n"
  "v_cvt_pk_bf16_f32 v39, v210, v211\n"
  "v_cvt_pk_bf16_f32 v40, v212, v213\n"
  "v_cvt_pk_bf16_f32 v41, v214, v215\n"
  "v_mfma_f32_32x32x16_bf16 a[80:95], v[240:243], v[240:243], 0\n"
  "v_cvt_pk_bf16_f32 v42, v216, v217\n"
  "v_cvt_pk_bf16_f32 v43, v218, v219\n"
  "v_cvt_pk_bf16_f32 v44, v220, v221\n"
  "v_cvt_pk_bf16_f32 v45, v222, v223\n"
  "v_cvt_pk_bf16_f32 v46, v128, v129\n"
  "v_cvt_pk_bf16_f32 v47, v130, v131\n"
  "v_accvgpr_read_b32 v196, a96\n"
  "v_accvgpr_read_b32 v197, a100\n"
  "v_accvgpr_read_b32 v198, a104\n"
  "v_mfma_f32_16x16x32_bf16 v[176:179], v[224:227], v[32:35], 0\n"
  "v_accvgpr_read_b32 v199, a108\n"
  "v_accvgpr_read_b32 v200, a112\n"
  "v_accvgpr_read_b32 v201, a116\n"
  "v_mfma_f32_16x16x32_bf16 v[176:179], v[228:231], v[36:39], v[176:179]\n"
  "v_accvgpr_read_b32 v202, a120\n"
  "v_accvgpr_read_b32 v203, a124\n"
  "v_accvgpr_read_b32 v204, a97\n"
  "v_mfma_f32_16x16x32_bf16 v[176:179], v[232:235], v[40:43], v[176:179]\n"
  "v_accvgpr_read_b32 v205, a101\n"
  "v_accvgpr_read_b32 v206, a105\n"
  "v_accvgpr_read_b32 v207, a109\n"
  "v_mfma_f32_16x16x32_bf16 v[176:179], v[236:239], v[44:47], v[176:179]\n"
  "v_accvgpr_read_b32 v208, a113\n"
  "v_accvgpr_read_b32 v209, a117\n"
  "v_accvgpr_read_b32 v210, a121\n"
  "v_mfma_f32_16x16x32_bf16 v[148:151], v[112:115], v[32:35], 0\n"
  "v_accvgpr_read_b32 v211, a125\n"
  "v_accvgpr_read_b32 v212, a98\n"
  "v_accvgpr_read_b32 v213, a102\n"
  "v_mfma_f32_16x16x32_bf16 v[148:151], v[116:119], v[36:39], v[148:151]\n"
  "v_accvgpr_read_b32 v214, a106\n"
  "v_accvgpr_read_b32 v215, a110\n"
  "v_accvgpr_read_b32 v216, a114\n"
  "v_mfma_f32_16x16x32_bf16 v[148:151], v[120:123], v[40:43], v[148:151]\n"
  "v_accvgpr_read_b32 v217, a118\n"
  "v_accvgpr_read_b32 v218, a122\n"
  "buffer_load_dwordx4 v245, s[48:51], s79 offen offset:3072 nt lds\n"
  "v_accvgpr_read_b32 v219, a126\n"
  "v_mfma_f32_16x16x32_bf16 v[148:151], v[124:127], v[44:47], v[148:151]\n"
  "v_accvgpr_read_b32 v220, a99\n"
  "v_accvgpr_read_b32 v221, a103\n"
  "v_accvgpr_read_b32 v222, a107\n"
  "v_accvgpr_read_b32 v223, a111\n"
  "v_accvgpr_read_b32 v128, a115\n"
  "v_accvgpr_read_b32 v129, a119\n"
  "v_accvgpr_read_b32 v130, a123\n"
  "v_accvgpr_read_b32 v131, a127\n"
  "v_pk_mul_f32 v[196:197], v[196:197], s[80:81]\n"
  "v_pk_mul_f32 v[198:199], v[198:199], s[80:81]\n"
  "v_pk_mul_f32 v[200:201], v[200:201], s[80:81]\n"
  "v_pk_mul_f32 v[202:203], v[202:203], s[80:81]\n"
  "v_pk_mul_f32 v[204:205], v[204:205], s[80:81]\n"
  "v_pk_mul_f32 v[206:207], v[206:207], s[80:81]\n"
  "v_pk_mul_f32 v[208:209], v[208:209], s[80:81]\n"
  "v_pk_mul_f32 v[210:211], v[210:211], s[80:81]\n"
  "v_pk_mul_f32 v[212:213], v[212:213], s[80:81]\n"
  "v_pk_mul_f32 v[214:215], v[214:215], s[80:81]\n"
  "v_pk_mul_f32 v[216:217], v[216:217], s[80:81]\n"
  "v_pk_mul_f32 v[218:219], v[218:219], s[80:81]\n"
  "v_pk_mul_f32 v[220:221], v[220:221], s[80:81]\n"
  "v_pk_mul_f32 v[222:223], v[222:223], s[80:81]\n"
  "v_pk_mul_f32 v[128:129], v[128:129], s[80:81]\n"
  "v_pk_mul_f32 v[130:131], v[130:131], s[80:81]\n"
  "v_cvt_pk_bf16_f32 v48, v196, v197\n"
  "v_cvt_pk_bf16_f32 v49, v198, v199\n"
  "v_cvt_pk_bf16_f32 v50, v200, v201\n"
  "v_cvt_pk_bf16_f32 v51, v202, v203\n"
  "v_cvt_pk_bf16_f32 v52, v204, v205\n"
  "v_mfma_f32_32x32x16_bf16 a[96:111], v[240:243], v[240:243], 0\n"
  "v_cvt_pk_bf16_f32 v53, v206, v207\n"
  "v_cvt_pk_bf16_f32 v54, v208, v209\n"
  "v_cvt_pk_bf16_f32 v55, v210, v211\n"
  "v_cvt_pk_bf16_f32 v56, v212, v213\n"
  "v_cvt_pk_bf16_f32 v57, v214, v215\n"
  "v_mfma_f32_32x32x16_bf16 a[112:127], v[240:243], v[240:243], 0\n"
  "v_cvt_pk_bf16_f32 v58, v216, v217\n"
  "v_cvt_pk_bf16_f32 v59, v218, v219\n"
  "v_cvt_pk_bf16_f32 v60, v220, v221\n"
  "v_cvt_pk_bf16_f32 v61, v222, v223\n"
  "v_cvt_pk_bf16_f32 v62, v128, v129\n"
  "s_add_u32 s76, s76, 0x2000\n"
  "s_and_b32 s76, s76, 0x1ffff\n"
  "s_add_u32 s77, s76, 4096\n"
  "s_add_u32 m0, s72, 0\n"
  "v_cvt_pk_bf16_f32 v63, v130, v131\n"
  "buffer_load_dwordx4 v244, s[44:47], s76 offen nt lds\n"
  "v_accvgpr_read_b32 v196, a128\n"
  "v_accvgpr_read_b32 v197, a132\n"
  "v_accvgpr_read_b32 v198, a136\n"
  "v_mfma_f32_16x16x32_bf16 v[180:183], v[224:227], v[48:51], 0\n"
  "v_accvgpr_read_b32 v199, a140\n"
  "v_accvgpr_read_b32 v200, a144\n"
  "v_accvgpr_read_b32 v201, a148\n"
  "v_mfma_f32_16x16x32_bf16 v[180:183], v[228:231], v[52:55], v[180:183]\n"
  "v_accvgpr_read_b32 v202, a152\n"
  "v_accvgpr_read_b32 v203, a156\n"
  "v_accvgpr_read_b32 v204, a129\n"
  "v_mfma_f32_16x16x32_bf16 v[180:183], v[232:235], v[56:59], v[180:183]\n"
  "v_accvgpr_read_b32 v205, a133\n"
  "v_accvgpr_read_b32 v206, a137\n"
  "v_accvgpr_read_b32 v207, a141\n"
  "v_mfma_f32_16x16x32_bf16 v[180:183], v[236:239], v[60:63], v[180:183]\n"
  "v_accvgpr_read_b32 v208, a145\n"
  "v_accvgpr_read_b32 v209, a149\n"
  "v_accvgpr_read_b32 v210, a153\n"
  "v_mfma_f32_16x16x32_bf16 v[152:155], v[112:115], v[48:51], 0\n"
  "v_accvgpr_read_b32 v211, a157\n"
  "v_accvgpr_read_b32 v212, a130\n"
  "v_accvgpr_read_b32 v213, a134\n"
  "v_mfma_f32_16x16x32_bf16 v[152:155], v[116:119], v[52:55], v[152:155]\n"
  "v_accvgpr_read_b32 v214, a138\n"
  "v_accvgpr_read_b32 v215, a142\n"
  "v_accvgpr_read_b32 v216, a146\n"
  "v_mfma_f32_16x16x32_bf16 v[152:155], v[120:123], v[56:59], v[152:155]\n"
  "v_accvgpr_read_b32 v217, a150\n"
  "v_accvgpr_read_b32 v218, a154\n"
  "v_accvgpr_read_b32 v219, a158\n"
  "v_mfma_f32_16x16x32_bf16 v[152:155], v[124:127], v[60:63], v[152:155]\n"
  "v_accvgpr_read_b32 v220, a131\n"
  "v_accvgpr_read_b32 v221, a135\n"
  "v_accvgpr_read_b32 v222, a139\n"
  "v_accvgpr_read_b32 v223, a143\n"
  "v_accvgpr_read_b32 v128, a147\n"
  "v_accvgpr_read_b32 v129, a151\n"
  "v_accvgpr_read_b32 v130, a155\n"
  "v_accvgpr_read_b32 v131, a159\n"
  "v_pk_mul_f32 v[196:197], v[196:197], s[80:81]\n"
  "v_pk_mul_f32 v[198:199], v[198:199], s[80:81]\n"
  "v_pk_mul_f32 v[200:201], v[200:201], s[80:81]\n"
  "v_pk_mul_f32 v[202:203], v[202:203], s[80:81]\n"
  "buffer_load_dwordx4 v244, s[44:47], s76 offen offset:1024 nt lds\n"
  "v_pk_mul_f32 v[204:205], v[204:205], s[80:81]\n"
  "v_pk_mul_f32 v[206:207], v[206:207], s[80:81]\n"
  "v_pk_mul_f32 v[208:209], v[208:209], s[80:81]\n"
  "v_pk_mul_f32 v[210:211], v[210:211], s[80:81]\n"
  "v_pk_mul_f32 v[212:213], v[212:213], s[80:81]\n"
  "v_pk_mul_f32 v[214:215], v[214:215], s[80:81]\n"
  "v_pk_mul_f32 v[216:217], v[216:217], s[80:81]\n"
  "v_pk_mul_f32 v[218:219], v[218:219], s[80:81]\n"
  "v_pk_mul_f32 v[220:221], v[220:221], s[80:81]\n"
  "v_pk_mul_f32 v[222:223], v[222:223], s[80:81]\n"
  "v_pk_mul_f32 v[128:129], v[128:129], s[80:81]\n"
  "v_pk_mul_f32 v[130:131], v[130:131], s[80:81]\n"
  "v_cvt_pk_bf16_f32 v64, v196, v197\n"
  "v_cvt_pk_bf16_f32 v65, v198, v199\n"
  "v_cvt_pk_bf16_f32 v66, v200, v201\n"
  "v_cvt_pk_bf16_f32 v67, v202, v203\n"
  "v_cvt_pk_bf16_f32 v68, v204, v205\n"
  "v_mfma_f32_32x32x16_bf16 a[128:143], v[240:243], v[240:243], 0\n"
  "v_cvt_pk_bf16_f32 v69, v206, v207\n"
  "v_cvt_pk_bf16_f32 v70, v208, v209\n"
  "v_cvt_pk_bf16_f32 v71, v210, v211\n"
  "v_cvt_pk_bf16_f32 v72, v212, v213\n"
  "v_cvt_pk_bf16_f32 v73, v214, v215\n"
  "v_mfma_f32_32x32x16_bf16 a[144:159], v[240:243], v[240:243], 0\n"
  "v_cvt_pk_bf16_f32 v74, v216, v217\n"
  "v_cvt_pk_bf16_f32 v75, v218, v219\n"
  "v_cvt_pk_bf16_f32 v76, v220, v221\n"
  "v_cvt_pk_bf16_f32 v77, v222, v223\n"
  "v_cvt_pk_bf16_f32 v78, v128, v129\n"
  "v_cvt_pk_bf16_f32 v79, v130, v131\n"
  "v_accvgpr_read_b32 v196, a160\n"
  "v_accvgpr_read_b32 v197, a164\n"
  "v_accvgpr_read_b32 v198, a168\n"
  "v_mfma_f32_16x16x32_bf16 v[184:187], v[224:227], v[64:67], 0\n"
  "v_accvgpr_read_b32 v199, a172\n"
  "v_accvgpr_read_b32 v200, a176\n"
  "v_accvgpr_read_b32 v201, a180\n"
  "v_mfma_f32_16x16x32_bf16 v[184:187], v[228:231], v[68:71], v[184:187]\n"
  "v_accvgpr_read_b32 v202, a184\n"
  "v_accvgpr_read_b32 v203, a188\n"
  "v_accvgpr_read_b32 v204, a161\n"
  "v_mfma_f32_16x16x32_bf16 v[184:187], v[232:235], v[72:75], v[184:187]\n"
  "v_accvgpr_read_b32 v205, a165\n"
  "v_accvgpr_read_b32 v206, a169\n"
  "buffer_load_dwordx4 v244, s[44:47], s76 offen offset:2048 nt lds\n"
  "v_accvgpr_read_b32 v207, a173\n"
  "v_mfma_f32_16x16x32_bf16 v[184:187], v[236:239], v[76:79], v[184:187]\n"
  "v_accvgpr_read_b32 v208, a177\n"
  "v_accvgpr_read_b32 v209, a181\n"
  "v_accvgpr_read_b32 v210, a185\n"
  "v_mfma_f32_16x16x32_bf16 v[156:159], v[112:115], v[64:67], 0\n"
  "v_accvgpr_read_b32 v211, a189\n"
  "v_accvgpr_read_b32 v212, a162\n"
  "v_accvgpr_read_b32 v213, a166\n"
  "v_mfma_f32_16x16x32_bf16 v[156:159], v[116:119], v[68:71], v[156:159]\n"
  "v_accvgpr_read_b32 v214, a170\n"
  "v_accvgpr_read_b32 v215, a174\n"
  "v_accvgpr_read_b32 v216, a178\n"
  "v_mfma_f32_16x16x32_bf16 v[156:159], v[120:123], v[72:75], v[156:159]\n"
  "v_accvgpr_read_b32 v217, a182\n"
  "v_accvgpr_read_b32 v218, a186\n"
  "v_accvgpr_read_b32 v219, a190\n"
  "v_mfma_f32_16x16x32_bf16 v[156:159], v[124:127], v[76:79], v[156:159]\n"
  "v_accvgpr_read_b32 v220, a163\n"
  "v_accvgpr_read_b32 v221, a167\n"
  "v_accvgpr_read_b32 v222, a171\n"
  "v_accvgpr_read_b32 v223, a175\n"
  "v_accvgpr_read_b32 v128, a179\n"
  "v_accvgpr_read_b32 v129, a183\n"
  "v_accvgpr_read_b32 v130, a187\n"
  "v_accvgpr_read_b32 v131, a191\n"
  "v_pk_mul_f32 v[196:197], v[196:197], s[80:81]\n"
  "v_pk_mul_f32 v[198:199], v[198:199], s[80:81]\n"
  "v_pk_mul_f32 v[200:201], v[200:201], s[80:81]\n"
  "v_pk_mul_f32 v[202:203], v[202:203], s[80:81]\n"
  "v_pk_mul_f32 v[204:205], v[204:205], s[80:81]\n"
  "v_pk_mul_f32 v[206:207], v[206:207], s[80:81]\n"
  "v_pk_mul_f32 v[208:209], v[208:209], s[80:81]\n"
  "v_pk_mul_f32 v[210:211], v[210:211], s[80:81]\n"
  "v_pk_mul_f32 v[212:213], v[212:213], s[80:81]\n"
  "v_pk_mul_f32 v[214:215], v[214:215], s[80:81]\n"
  "v_pk_mul_f32 v[216:217], v[216:217], s[80:81]\n"
  "v_pk_mul_f32 v[218:219], v[218:219], s[80:81]\n"
  "v_pk_mul_f32 v[220:221], v[220:221], s[80:81]\n"
  "v_pk_mul_f32 v[222:223], v[222:223], s[80:81]\n"
  "v_pk_mul_f32 v[128:129], v[128:129], s[80:81]\n"
  "v_pk_mul_f32 v[130:131], v[130:131], s[80:81]\n"
  "v_cvt_pk_bf16_f32 v80, v196, v197\n"
  "v_cvt_pk_bf16_f32 v81, v198, v199\n"
  "buffer_load_dwordx4 v244, s[44:47], s76 offen offset:3072 nt lds\n"
  "v_cvt_pk_bf16_f32 v82, v200, v201\n"
  "v_cvt_pk_bf16_f32 v83, v202, v203\n"
  "v_cvt_pk_bf16_f32 v84, v204, v205\n"
  "v_mfma_f32_32x32x16_bf16 a[160:175], v[240:243], v[240:243], 0\n"
  "v_cvt_pk_bf16_f32 v85, v206, v207\n"
  "v_cvt_pk_bf16_f32 v86, v208, v209\n"
  "v_cvt_pk_bf16_f32 v87, v210, v211\n"
  "v_cvt_pk_bf16_f32 v88, v212, v213\n"
  "v_cvt_pk_bf16_f32 v89, v214, v215\n"
  "v_mfma_f32_32x32x16_bf16 a[176:191], v[240:243], v[240:243], 0\n"
  "v_cvt_pk_bf16_f32 v90, v216, v217\n"
  "v_cvt_pk_bf16_f32 v91, v218, v219\n"
  "v_cvt_pk_bf16_f32 v92, v220, v221\n"
  "v_cvt_pk_bf16_f32 v93, v222, v223\n"
  "v_cvt_pk_bf16_f32 v94, v128, v129\n"
  "v_cvt_pk_bf16_f32 v95, v130, v131\n"
  "v_accvgpr_read_b32 v196, a192\n"
  "v_accvgpr_read_b32 v197, a196\n"
  "v_accvgpr_read_b32 v198, a200\n"
  "v_mfma_f32_16x16x32_bf16 v[188:191], v[224:227], v[80:83], 0\n"
  "v_accvgpr_read_b32 v199, a204\n"
  "v_accvgpr_read_b32 v200, a208\n"
  "v_accvgpr_read_b32 v201, a212\n"
  "v_mfma_f32_16x16x32_bf16 v[188:191], v[228:231], v[84:87], v[188:191]\n"
  "v_accvgpr_read_b32 v202, a216\n"
  "v_accvgpr_read_b32 v203, a220\n"
  "v_accvgpr_read_b32 v204, a193\n"
  "v_mfma_f32_16x16x32_bf16 v[188:191], v[232:235], v[88:91], v[188:191]\n"
  "v_accvgpr_read_b32 v205, a197\n"
  "v_accvgpr_read_b32 v206, a201\n"
  "v_accvgpr_read_b32 v207, a205\n"
  "v_mfma_f32_16x16x32_bf16 v[188:191], v[236:239], v[92:95], v[188:191]\n"
  "v_accvgpr_read_b32 v208, a209\n"
  "v_accvgpr_read_b32 v209, a213\n"
  "v_accvgpr_read_b32 v210, a217\n"
  "v_mfma_f32_16x16x32_bf16 v[160:163], v[112:115], v[80:83], 0\n"
  "v_accvgpr_read_b32 v211, a221\n"
  "v_accvgpr_read_b32 v212, a194\n"
  "v_accvgpr_read_b32 v213, a198\n"
  "v_mfma_f32_16x16x32_bf16 v[160:163], v[116:119], v[84:87], v[160:163]\n"
  "v_accvgpr_read_b32 v214, a202\n"
  "v_accvgpr_read_b32 v215, a206\n"
  "v_accvgpr_read_b32 v216, a210\n"
  "s_add_u32 m0, s72, 4128\n"
  "v_mfma_f32_16x16x32_bf16 v[160:163], v[120:123], v[88:91], v[160:163]\n"
  "buffer_load_dwordx4 v244, s[44:47], s77 offen nt lds\n"
  "v_accvgpr_read_b32 v217, a214\n"
  "v_accvgpr_read_b32 v218, a218\n"
  "v_accvgpr_read_b32 v219, a222\n"
  "v_mfma_f32_16x16x32_bf16 v[160:163], v[124:127], v[92:95], v[160:163]\n"
  "v_accvgpr_read_b32 v220, a195\n"
  "v_accvgpr_read_b32 v221, a199\n"
  "v_accvgpr_read_b32 v222, a203\n"
  "v_accvgpr_read_b32 v223, a207\n"
  "v_accvgpr_read_b32 v128, a211\n"
  "v_accvgpr_read_b32 v129, a215\n"
  "v_accvgpr_read_b32 v130, a219\n"
  "v_accvgpr_read_b32 v131, a223\n"
  "v_pk_mul_f32 v[196:197], v[196:197], s[80:81]\n"
  "v_pk_mul_f32 v[198:199], v[198:199], s[80:81]\n"
  "v_pk_mul_f32 v[200:201], v[200:201], s[80:81]\n"
  "v_pk_mul_f32 v[202:203], v[202:203], s[80:81]\n"
  "v_pk_mul_f32 v[204:205], v[204:205], s[80:81]\n"
  "v_pk_mul_f32 v[206:207], v[206:207], s[80:81]\n"
  "v_pk_mul_f32 v[208:209], v[208:209], s[80:81]\n"
  "v_pk_mul_f32 v[210:211], v[210:211], s[80:81]\n"
  "v_pk_mul_f32 v[212:213], v[212:213], s[80:81]\n"
  "v_pk_mul_f32 v[214:215], v[214:215], s[80:81]\n"
  "v_pk_mul_f32 v[216:217], v[216:217], s[80:81]\n"
  "v_pk_mul_f32 v[218:219], v[218:219], s[80:81]\n"
  "v_pk_mul_f32 v[220:221], v[220:221], s[80:81]\n"
  "v_pk_mul_f32 v[222:223], v[222:223], s[80:81]\n"
  "v_pk_mul_f32 v[128:129], v[128:129], s[80:81]\n"
  "v_pk_mul_f32 v[130:131], v[130:131], s[80:81]\n"
  "v_cvt_pk_bf16_f32 v96, v196, v197\n"
  "v_cvt_pk_bf16_f32 v97, v198, v199\n"
  "v_cvt_pk_bf16_f32 v98, v200, v201\n"
  "v_cvt_pk_bf16_f32 v99, v202, v203\n"
  "v_cvt_pk_bf16_f32 v100, v204, v205\n"
  "v_mfma_f32_32x32x16_bf16 a[192:207], v[240:243], v[240:243], 0\n"
  "v_cvt_pk_bf16_f32 v101, v206, v207\n"
  "v_cvt_pk_bf16_f32 v102, v208, v209\n"
  "v_cvt_pk_bf16_f32 v103, v210, v211\n"
  "v_cvt_pk_bf16_f32 v104, v212, v213\n"
  "v_cvt_pk_bf16_f32 v105, v214, v215\n"
  "v_mfma_f32_32x32x16_bf16 a[208:223], v[240:243], v[240:243], 0\n"
  "v_cvt_pk_bf16_f32 v106, v216, v217\n"
  "v_cvt_pk_bf16_f32 v107, v218, v219\n"
  "v_cvt_pk_bf16_f32 v108, v220, v221\n"
  "v_cvt_pk_bf16_f32 v109, v222, v223\n"
  "buffer_load_dwordx4 v244, s[44:47], s77 offen offset:1024 nt lds\n"
  "v_cvt_pk_bf16_f32 v110, v128, v129\n"
  "v_cvt_pk_bf16_f32 v111, v130, v131\n"
  "v_mov_b32 v253, 0x3f803f80\n"
  "v_accvgpr_write_b32 a252, v253\n"
  "v_accvgpr_write_b32 a253, v253\n"
  "v_mfma_f32_16x16x32_bf16 v[192:195], v[224:227], v[96:99], 0\n"
  "v_accvgpr_write_b32 a254, v253\n"
  "v_accvgpr_write_b32 a255, v253\n"
  "v_mfma_f32_16x16x32_bf16 v[192:195], v[228:231], v[100:103], v[192:195]\n"
  "v_max3_f32 v128, v168, v169, v170\n"
  "v_max3_f32 v128, v128, v171, v140\n"
  "v_mfma_f32_16x16x32_bf16 v[192:195], v[232:235], v[104:107], v[192:195]\n"
  "v_max3_f32 v128, v128, v141, v142\n"
  "v_max_f32 v128, v128, v143\n"
  "v_mfma_f32_16x16x32_bf16 v[192:195], v[236:239], v[108:111], v[192:195]\n"
  "v_max3_f32 v129, v172, v173, v174\n"
  "v_max3_f32 v129, v129, v175, v144\n"
  "v_mfma_f32_16x16x32_bf16 v[164:167], v[112:115], v[96:99], 0\n"
  "v_max3_f32 v129, v129, v145, v146\n"
  "v_max_f32 v129, v129, v147\n"
  "v_mfma_f32_16x16x32_bf16 v[164:167], v[116:119], v[100:103], v[164:167]\n"
  "v_max3_f32 v130, v176, v177, v178\n"
  "v_max3_f32 v130, v130, v179, v148\n"
  "v_mfma_f32_16x16x32_bf16 v[164:167], v[120:123], v[104:107], v[164:167]\n"
  "v_max3_f32 v130, v130, v149, v150\n"
  "v_max_f32 v130, v130, v151\n"
  "v_mfma_f32_16x16x32_bf16 v[164:167], v[124:127], v[108:111], v[164:167]\n"
  "v_max3_f32 v131, v180, v181, v182\n"
  "v_max3_f32 v131, v131, v183, v152\n"
  "v_max3_f32 v131, v131, v153, v154\n"
  "v_max_f32 v131, v131, v155\n"
  "v_max3_f32 v132, v184, v185, v186\n"
  "v_max3_f32 v132, v132, v187, v156\n"
  "v_max3_f32 v132, v132, v157, v158\n"
  "v_max_f32 v132, v132, v159\n"
  "v_max3_f32 v133, v188, v189, v190\n"
  "v_max3_f32 v133, v133, v191, v160\n"
  "v_max3_f32 v133, v133, v161, v162\n"
  "v_max_f32 v133, v133, v163\n"
  "v_max3_f32 v134, v192, v193, v194\n"
  "v_max3_f32 v134, v134, v195, v164\n"
  "v_max3_f32 v134, v134, v165, v166\n"
  "v_max_f32 v134, v134, v167\n"
  "v_mov_b32 v112, v128\n"
  "buffer_load_dwordx4 v244, s[44:47], s77 offen offset:2048 nt lds\n"
  "v_mov_b32 v113, v129\n"
  "v_mov_b32 v114, v130\n"
  "v_mov_b32 v115, v131\n"
  "v_mov_b32 v116, v132\n"
  "v_mov_b32 v117, v133\n"
  "v_mov_b32 v118, v134\n"
  "v_permlane16_swap_b32 v128, v112\n"
  "v_permlane16_swap_b32 v129, v113\n"
  "v_permlane16_swap_b32 v130, v114\n"
  "v_permlane16_swap_b32 v131, v115\n"
  "v_permlane16_swap_b32 v132, v116\n"
  "v_permlane16_swap_b32 v133, v117\n"
  "v_permlane16_swap_b32 v134, v118\n"
  "v_max_f32 v128, v128, v112\n"
  "v_max_f32 v129, v129, v113\n"
  "v_max_f32 v130, v130, v114\n"
  "v_max_f32 v131, v131, v115\n"
  "v_max_f32 v132, v132, v116\n"
  "v_max_f32 v133, v133, v117\n"
  "v_max_f32 v134, v134, v118\n"
  "v_mov_b32 v112, v128\n"
  "v_mov_b32 v113, v129\n"
  "v_mov_b32 v114, v130\n"
  "v_mov_b32 v115, v131\n"
  "v_mov_b32 v116, v132\n"
  "v_mov_b32 v117, v133\n"
  "v_mov_b32 v118, v134\n"
  "v_permlane32_swap_b32 v128, v112\n"
  "v_permlane32_swap_b32 v129, v113\n"
  "v_permlane32_swap_b32 v130, v114\n"
  "v_permlane32_swap_b32 v131, v115\n"
  "v_permlane32_swap_b32 v132, v116\n"
  "v_permlane32_swap_b32 v133, v117\n"
  "v_permlane32_swap_b32 v134, v118\n"
  "v_max_f32 v128, v128, v112\n"
  "v_max_f32 v129, v129, v113\n"
  "v_max_f32 v130, v130, v114\n"
  "v_max_f32 v131, v131, v115\n"
  "v_max_f32 v132, v132, v116\n"
  "v_max_f32 v133, v133, v117\n"
  "v_max_f32 v134, v134, v118\n"
  "v_sub_f32 v196, 0xc1800000, v128\n"
  "v_sub_f32 v200, 0xc1800000, v129\n"
  "v_sub_f32 v204, 0xc1800000, v130\n"
  "buffer_load_dwordx4 v244, s[44:47], s77 offen offset:3072 nt lds\n"
  "v_sub_f32 v208, 0xc1800000, v131\n"
  "v_sub_f32 v212, 0xc1800000, v132\n"
  "v_sub_f32 v216, 0xc1800000, v133\n"
  "v_sub_f32 v220, 0xc1800000, v134\n"
  "v_mov_b32 v197, v196\n"
  "v_mov_b32 v201, v200\n"
  "v_mov_b32 v205, v204\n"
  "v_mov_b32 v209, v208\n"
  "v_mov_b32 v213, v212\n"
  "v_mov_b32 v217, v216\n"
  "v_mov_b32 v221, v220\n"
  "v_pk_mov_b32 v[198:199], v[196:197], v[196:197]\n"
  "v_pk_mov_b32 v[202:203], v[200:201], v[200:201]\n"
  "v_pk_mov_b32 v[206:207], v[204:205], v[204:205]\n"
  "v_pk_mov_b32 v[210:211], v[208:209], v[208:209]\n"
  "v_pk_mov_b32 v[214:215], v[212:213], v[212:213]\n"
  "v_pk_mov_b32 v[218:219], v[216:217], v[216:217]\n"
  "v_pk_mov_b32 v[222:223], v[220:221], v[220:221]\n"
  "v_pk_add_f32 v[168:169], v[168:169], v[196:197]\n"
  "v_pk_add_f32 v[170:171], v[170:171], v[196:197]\n"
  "v_pk_add_f32 v[140:141], v[140:141], v[196:197]\n"
  "v_pk_add_f32 v[142:143], v[142:143], v[196:197]\n"
  "v_pk_add_f32 v[172:173], v[172:173], v[200:201]\n"
  "v_pk_add_f32 v[174:175], v[174:175], v[200:201]\n"
  "v_pk_add_f32 v[144:145], v[144:145], v[200:201]\n"
  "v_pk_add_f32 v[146:147], v[146:147], v[200:201]\n"
  "v_pk_add_f32 v[176:177], v[176:177], v[204:205]\n"
  "v_pk_add_f32 v[178:179], v[178:179], v[204:205]\n"
  "v_pk_add_f32 v[148:149], v[148:149], v[204:205]\n"
  "v_pk_add_f32 v[150:151], v[150:151], v[204:205]\n"
  "v_pk_add_f32 v[180:181], v[180:181], v[208:209]\n"
  "v_pk_add_f32 v[182:183], v[182:183], v[208:209]\n"
  "v_pk_add_f32 v[152:153], v[152:153], v[208:209]\n"
  "v_pk_add_f32 v[154:155], v[154:155], v[208:209]\n"
  "v_pk_add_f32 v[184:185], v[184:185], v[212:213]\n"
  "v_pk_add_f32 v[186:187], v[186:187], v[212:213]\n"
  "v_pk_add_f32 v[156:157], v[156:157], v[212:213]\n"
  "v_pk_add_f32 v[158:159], v[158:159], v[212:213]\n"
  "v_pk_add_f32 v[188:189], v[188:189], v[216:217]\n"
  "v_pk_add_f32 v[190:191], v[190:191], v[216:217]\n"
  "v_pk_add_f32 v[160:161], v[160:161], v[216:217]\n"
  "v_pk_add_f32 v[162:163], v[162:163], v[216:217]\n"
  "v_pk_add_f32 v[192:193], v[192:193], v[220:221]\n"
  "v_pk_add_f32 v[194:195], v[194:195], v[220:221]\n"
  "v_pk_add_f32 v[164:165], v[164:165], v[220:221]\n"
  "v_pk_add_f32 v[166:167], v[166:167], v[220:221]\n"
  "v_exp_f32 v168, v168\n"
  "v_exp_f32 v169, v169\n"
  "v_exp_f32 v170, v170\n"
  "v_exp_f32 v171, v171\n"
  "v_exp_f32 v140, v140\n"
  "v_exp_f32 v141, v141\n"
  "v_exp_f32 v142, v142\n"
  "v_exp_f32 v143, v143\n"
  "v_cvt_pk_bf16_f32 v168, v168, v169\n"
  "v_cvt_pk_bf16_f32 v169, v170, v171\n"
  "v_cvt_pk_bf16_f32 v170, v140, v141\n"
  "v_cvt_pk_bf16_f32 v171, v142, v143\n"
  "v_exp_f32 v172, v172\n"
  "v_exp_f32 v173, v173\n"
  "v_exp_f32 v174, v174\n"
  "v_exp_f32 v175, v175\n"
  "v_exp_f32 v144, v144\n"
  "v_exp_f32 v145, v145\n"
  "v_exp_f32 v146, v146\n"
  "v_exp_f32 v147, v147\n"
  "v_cvt_pk_bf16_f32 v172, v172, v173\n"
  "v_cvt_pk_bf16_f32 v173, v174, v175\n"
  "v_cvt_pk_bf16_f32 v174, v144, v145\n"
  "v_cvt_pk_bf16_f32 v175, v146, v147\n"
  "v_exp_f32 v176, v176\n"
  "v_exp_f32 v177, v177\n"
  "v_exp_f32 v178, v178\n"
  "v_exp_f32 v179, v179\n"
  "v_exp_f32 v148, v148\n"
  "v_exp_f32 v149, v149\n"
  "v_exp_f32 v150, v150\n"
  "v_exp_f32 v151, v151\n"
  "v_cvt_pk_bf16_f32 v176, v176, v177\n"
  "v_cvt_pk_bf16_f32 v177, v178, v179\n"
  "v_cvt_pk_bf16_f32 v178, v148, v149\n"
  "v_cvt_pk_bf16_f32 v179, v150, v151\n"
  "v_exp_f32 v180, v180\n"
  "v_exp_f32 v181, v181\n"
  "v_exp_f32 v182, v182\n"
  "v_exp_f32 v183, v183\n"
  "v_exp_f32 v152, v152\n"
  "v_exp_f32 v153, v153\n"
  "v_exp_f32 v154, v154\n"
  "v_exp_f32 v155, v155\n"
  "v_cvt_pk_bf16_f32 v180, v180, v181\n"
  "v_cvt_pk_bf16_f32 v181, v182, v183\n"
  "v_cvt_pk_bf16_f32 v182, v152, v153\n"
  "v_cvt_pk_bf16_f32 v183, v154, v155\n"
  "v_exp_f32 v184, v184\n"
  "v_exp_f32 v185, v185\n"
  "v_exp_f32 v186, v186\n"
  "v_exp_f32 v187, v187\n"
  "v_exp_f32 v156, v156\n"
  "v_exp_f32 v157, v157\n"
  "v_exp_f32 v158, v158\n"
  "v_exp_f32 v159, v159\n"
  "v_cvt_pk_bf16_f32 v184, v184, v185\n"
  "v_cvt_pk_bf16_f32 v185, v186, v187\n"
  "v_cvt_pk_bf16_f32 v186, v156, v157\n"
  "v_cvt_pk_bf16_f32 v187, v158, v159\n"
  "s_waitcnt vmcnt(28)\n"
  "ds_read_b128 v[224:227], v246 offset:33024\n"
  "ds_read_b128 v[228:231], v247 offset:33024\n"
  "v_exp_f32 v188, v188\n"
  "v_exp_f32 v189, v189\n"
  "v_exp_f32 v190, v190\n"
  "v_exp_f32 v191, v191\n"
  "v_exp_f32 v160, v160\n"
  "v_exp_f32 v161, v161\n"
  "v_exp_f32 v162, v162\n"
  "v_exp_f32 v163, v163\n"
  "v_cvt_pk_bf16_f32 v188, v188, v189\n"
  "v_cvt_pk_bf16_f32 v189, v190, v191\n"
  "v_cvt_pk_bf16_f32 v190, v160, v161\n"
  "v_cvt_pk_bf16_f32 v191, v162, v163\n"
  "v_exp_f32 v192, v192\n"
  "v_exp_f32 v193, v193\n"
  "v_exp_f32 v194, v194\n"
  "v_exp_f32 v195, v195\n"
  "v_exp_f32 v164, v164\n"
  "v_exp_f32 v165, v165\n"
  "v_exp_f32 v166, v166\n"
  "v_exp_f32 v167, v167\n"
  "v_cvt_pk_bf16_f32 v192, v192, v193\n"
  "v_cvt_pk_bf16_f32 v193, v194, v195\n"
  "v_cvt_pk_bf16_f32 v194, v164, v165\n"
  "v_cvt_pk_bf16_f32 v195, v166, v167\n"
  "s_waitcnt lgkmcnt(0)\n"
  "v_mov_b32 v253, s72\n"
  "s_bitcmp1_b32 s74, 1\n"
  "s_cbranch_scc0 L_first\n"
  "v_subrev_u32 v242, 16512, v253\n"
  "s_setprio 0\n"
  "s_mov_b32 s87, 13\n"
  "L_lw:\n"
  "ds_read_b32 v241, v242 offset:8228\n"
  "s_waitcnt lgkmcnt(0)\n"
  "v_cmp_le_u32 vcc, s87, v241\n"
  "s_cbranch_vccnz L_first\n"
  "s_sleep 6\n"
  "s_branch L_lw\n"
  "L_first:\n"
  "v_mfma_f32_16x16x32_bf16 v[112:115], v[224:227], v[0:3], v[196:199]\n"
  "ds_read_b128 v[232:235], v246 offset:33056\n"
  "ds_read_b128 v[236:239], v247 offset:33056\n"
  "v_mfma_f32_16x16x32_bf16 v[116:119], v[224:227], v[16:19], v[200:203]\n"
  "v_mfma_f32_16x16x32_bf16 v[120:123], v[224:227], v[32:35], v[204:207]\n"
  "v_mfma_f32_16x16x32_bf16 v[124:127], v[224:227], v[48:51], v[208:211]\n"
  "s_add_u32 s76, s76, 0x2000\n"
  "s_and_b32 s76, s76, 0x1ffff\n"
  "s_add_u32 s77, s76, 4096\n"
  "v_mfma_f32_16x16x32_bf16 v[128:131], v[224:227], v[64:67], v[212:215]\n"
  "v_mfma_f32_16x16x32_bf16 v[132:135], v[224:227], v[80:83], v[216:219]\n"
  "s_cmp_lt_u32 s60, 13\n"
  "s_cselect_b32 s46, 0x20000, 0\n"
  "s_add_u32 s60, s60, 1\n"
  "v_mfma_f32_16x16x32_bf16 v[136:139], v[224:227], v[96:99], v[220:223]\n"
  "v_mfma_f32_16x16x32_bf16 v[112:115], v[228:231], v[4:7], v[112:115]\n"
  "v_mfma_f32_16x16x32_bf16 v[116:119], v[228:231], v[20:23], v[116:119]\n"
  "v_mfma_f32_16x16x32_bf16 v[120:123], v[228:231], v[36:39], v[120:123]\n"
  "v_mfma_f32_16x16x32_bf16 v[124:127], v[228:231], v[52:55], v[124:127]\n"
  "v_mfma_f32_16x16x32_bf16 v[128:131], v[228:231], v[68:71], v[128:131]\n"
  "v_mfma_f32_16x16x32_bf16 v[132:135], v[228:231], v[84:87], v[132:135]\n"
  "v_mfma_f32_16x16x32_bf16 v[136:139], v[228:231], v[100:103], v[136:139]\n"
  "s_waitcnt lgkmcnt(0)\n"
  "v_mfma_f32_16x16x32_bf16 v[112:115], v[232:235], v[8:11], v[112:115]\n"
  "ds_read_b128 v[224:227], v246 offset:34048\n"
  "ds_read_b128 v[228:231], v247 offset:34048\n"
  "v_mfma_f32_16x16x32_bf16 v[112:115], v[236:239], v[12:15], v[112:115]\n"
  "v_mov_b32 v241, s60\n"
  "v_mfma_f32_16x16x32_bf16 v[116:119], v[232:235], v[24:27], v[116:119]\n"
  "v_mfma_f32_16x16x32_bf16 v[116:119], v[236:239], v[28:31], v[116:119]\n"
  "ds_write_b32 v253, v241 offset:8228\n"
  "v_mfma_f32_16x16x32_bf16 v[120:123], v[232:235], v[40:43], v[120:123]\n"
  "v_mfma_f32_16x16x32_bf16 v[120:123], v[236:239], v[44:47], v[120:123]\n"
  "s_add_u32 m0, s72, 33024\n"
  "v_mfma_f32_16x16x32_bf16 v[124:127], v[232:235], v[56:59], v[124:127]\n"
  "v_mfma_f32_16x16x32_bf16 v[124:127], v[236:239], v[60:63], v[124:127]\n"
  "v_mfma_f32_16x16x32_bf16 v[128:131], v[232:235], v[72:75], v[128:131]\n"
  "v_exp_f32 v112, v112\n"
  "v_mfma_f32_16x16x32_bf16 v[128:131], v[236:239], v[76:79], v[128:131]\n"
  "v_exp_f32 v113, v113\n"
  "v_mfma_f32_16x16x32_bf16 v[132:135], v[232:235], v[88:91], v[132:135]\n"
  "v_exp_f32 v114, v114\n"
  "v_mfma_f32_16x16x32_bf16 v[132:135], v[236:239], v[92:95], v[132:135]\n"
  "v_exp_f32 v115, v115\n"
  "v_mfma_f32_16x16x32_bf16 v[136:139], v[232:235], v[104:107], v[136:139]\n"
  "v_exp_f32 v116, v116\n"
  "v_mfma_f32_16x16x32_bf16 v[136:139], v[236:239], v[108:111], v[136:139]\n"
  "s_waitcnt lgkmcnt(1)\n"
  "v_exp_f32 v117, v117\n"
  "v_mfma_f32_16x16x32_bf16 v[140:143], v[224:227], v[0:3], v[196:199]\n"
  "ds_read_b128 v[232:235], v246 offset:34080\n"
  "ds_read_b128 v[236:239], v247 offset:34080\n"
  "v_mfma_f32_16x16x32_bf16 v[144:147], v[224:227], v[16:19], v[200:203]\n"
  "v_cvt_pk_bf16_f32 v112, v112, v113\n"
  "v_cvt_pk_bf16_f32 v113, v114, v115\n"
  "v_mfma_f32_16x16x32_bf16 v[148:151], v[224:227], v[32:35], v[204:207]\n"
  "v_or_b32 v240, v112, v113\n"
  "v_cvt_pk_bf16_f32 v116, v116, v117\n"
  "v_mfma_f32_16x16x32_bf16 v[152:155], v[224:227], v[48:51], v[208:211]\n"
  "v_exp_f32 v118, v118\n"
  "v_mfma_f32_16x16x32_bf16 v[156:159], v[224:227], v[64:67], v[212:215]\n"
  "v_exp_f32 v119, v119\n"
  "v_mfma_f32_16x16x32_bf16 v[160:163], v[224:227], v[80:83], v[216:219]\n"
  "v_exp_f32 v120, v120\n"
  "v_mfma_f32_16x16x32_bf16 v[164:167], v[224:227], v[96:99], v[220:223]\n"
  "v_exp_f32 v121, v121\n"
  "v_mfma_f32_16x16x32_bf16 v[140:143], v[228:231], v[4:7], v[140:143]\n"
  "v_cvt_pk_bf16_f32 v117, v118, v119\n"
  "v_or3_b32 v240, v240, v116, v117\n"
  "v_mfma_f32_16x16x32_bf16 v[144:147], v[228:231], v[20:23], v[144:147]\n"
  "v_exp_f32 v122, v122\n"
  "v_mfma_f32_16x16x32_bf16 v[148:151], v[228:231], v[36:39], v[148:151]\n"
  "v_exp_f32 v123, v123\n"
  "v_mfma_f32_16x16x32_bf16 v[152:155], v[228:231], v[52:55], v[152:155]\n"
  "v_exp_f32 v124, v124\n"
  "v_mfma_f32_16x16x32_bf16 v[156:159], v[228:231], v[68:71], v[156:159]\n"
  "v_exp_f32 v125, v125\n"
  "v_mfma_f32_16x16x32_bf16 v[160:163], v[228:231], v[84:87], v[160:163]\n"
  "v_cvt_pk_bf16_f32 v120, v120, v121\n"
  "v_cvt_pk_bf16_f32 v121, v122, v123\n"
  "v_mfma_f32_16x16x32_bf16 v[164:167], v[228:231], v[100:103], v[164:167]\n"
  "s_waitcnt lgkmcnt(0)\n"
  "v_or3_b32 v240, v240, v120, v121\n"
  "v_cvt_pk_bf16_f32 v124, v124, v125\n"
  "v_mfma_f32_16x16x32_bf16 v[140:143], v[232:235], v[8:11], v[140:143]\n"
  "s_waitcnt vmcnt(16)\n"
  "ds_read_b64_tr_b16 v[224:225], v248 offset:0\n"
  "ds_read_b64_tr_b16 v[226:227], v248 offset:1024\n"
  "v_mfma_f32_16x16x32_bf16 v[140:143], v[236:239], v[12:15], v[140:143]\n"
  "ds_read_b64_tr_b16 v[228:229], v248 offset:32\n"
  "ds_read_b64_tr_b16 v[230:231], v248 offset:1056\n"
  "v_mfma_f32_16x16x32_bf16 v[144:147], v[232:235], v[24:27], v[144:147]\n"
  "v_exp_f32 v126, v126\n"
  "v_mfma_f32_16x16x32_bf16 v[144:147], v[236:239], v[28:31], v[144:147]\n"
  "v_exp_f32 v127, v127\n"
  "v_mfma_f32_16x16x32_bf16 v[148:151], v[232:235], v[40:43], v[148:151]\n"
  "v_exp_f32 v128, v128\n"
  "v_mfma_f32_16x16x32_bf16 v[148:151], v[236:239], v[44:47], v[148:151]\n"
  "v_exp_f32 v129, v129\n"
  "v_mfma_f32_16x16x32_bf16 v[152:155], v[232:235], v[56:59], v[152:155]\n"
  "buffer_load_dwordx4 v244, s[44:47], s76 offen nt lds\n"
  "v_mfma_f32_16x16x32_bf16 v[152:155], v[236:239], v[60:63], v[152:155]\n"
  "v_cvt_pk_bf16_f32 v125, v126, v127\n"
  "v_or3_b32 v240, v240, v124, v125\n"
  "v_mfma_f32_16x16x32_bf16 v[156:159], v[232:235], v[72:75], v[156:159]\n"
  "v_exp_f32 v130, v130\n"
  "v_mfma_f32_16x16x32_bf16 v[156:159], v[236:239], v[76:79], v[156:159]\n"
  "v_exp_f32 v131, v131\n"
  "v_mfma_f32_16x16x32_bf16 v[160:163], v[232:235], v[88:91], v[160:163]\n"
  "v_exp_f32 v132, v132\n"
  "v_mfma_f32_16x16x32_bf16 v[160:163], v[236:239], v[92:95], v[160:163]\n"
  "buffer_load_dwordx4 v244, s[44:47], s76 offen offset:1024 nt lds\n"
  "v_mfma_f32_16x16x32_bf16 v[164:167], v[232:235], v[104:107], v[164:167]\n"
  "v_exp_f32 v133, v133\n"
  "v_mfma_f32_16x16x32_bf16 v[164:167], v[236:239], v[108:111], v[164:167]\n"
  "v_cvt_pk_bf16_f32 v128, v128, v129\n"
  "v_cvt_pk_bf16_f32 v129, v130, v131\n"
  "v_mfma_f32_16x16x32_bf16 a[224:227], a[252:255], v[168:171], a[224:227]\n"
  "ds_read_b64_tr_b16 v[232:233], v249 offset:0\n"
  "ds_read_b64_tr_b16 v[234:235], v249 offset:1024\n"
  "v_mfma_f32_16x16x32_bf16 a[228:231], a[252:255], v[172:175], a[228:231]\n"
  "ds_read_b64_tr_b16 v[236:237], v249 offset:32\n"
  "ds_read_b64_tr_b16 v[238:239], v249 offset:1056\n"
  "v_mfma_f32_16x16x32_bf16 a[232:235], a[252:255], v[176:179], a[232:235]\n"
  "v_or3_b32 v240, v240, v128, v129\n"
  "v_cvt_pk_bf16_f32 v132, v132, v133\n"
  "v_mfma_f32_16x16x32_bf16 a[236:239], a[252:255], v[180:183], a[236:239]\n"
  "v_exp_f32 v134, v134\n"
  "v_mfma_f32_16x16x32_bf16 a[240:243], a[252:255], v[184:187], a[240:243]\n"
  "buffer_load_dwordx4 v244, s[44:47], s76 offen offset:2048 nt lds\n"
  "v_mfma_f32_16x16x32_bf16 a[244:247], a[252:255], v[188:191], a[244:247]\n"
  "v_exp_f32 v135, v135\n"
  "v_mfma_f32_16x16x32_bf16 a[248:251], a[252:255], v[192:195], a[248:251]\n"
  "s_waitcnt lgkmcnt(4)\n"
  "v_exp_f32 v136, v136\n"
  "v_mfma_f32_16x16x32_bf16 a[0:3], v[224:227], v[168:171], a[0:3]\n"
  "v_exp_f32 v137, v137\n"
  "v_mfma_f32_16x16x32_bf16 a[32:35], v[224:227], v[172:175], a[32:35]\n"
  "v_cvt_pk_bf16_f32 v133, v134, v135\n"
  "v_or3_b32 v240, v240, v132, v133\n"
  "v_mfma_f32_16x16x32_bf16 a[64:67], v[224:227], v[176:179], a[64:67]\n"
  "v_exp_f32 v138, v138\n"
  "v_mfma_f32_16x16x32_bf16 a[96:99], v[224:227], v[180:183], a[96:99]\n"
  "buffer_load_dwordx4 v244, s[44:47], s76 offen offset:3072 nt lds\n"
  "v_mfma_f32_16x16x32_bf16 a[128:131], v[224:227], v[184:187], a[128:131]\n"
  "v_exp_f32 v139, v139\n"
  "v_mfma_f32_16x16x32_bf16 a[160:163], v[224:227], v[188:191], a[160:163]\n"
  "s_add_u32 m0, s72, 37152\n"
  "v_exp_f32 v140, v140\n"
  "v_mfma_f32_16x16x32_bf16 a[192:195], v[224:227], v[192:195], a[192:195]\n"
  "v_exp_f32 v141, v141\n"
  "v_mfma_f32_16x16x32_bf16 a[4:7], v[228:231], v[168:171], a[4:7]\n"
  "v_cvt_pk_bf16_f32 v136, v136, v137\n"
  "v_cvt_pk_bf16_f32 v137, v138, v139\n"
  "v_mfma_f32_16x16x32_bf16 a[36:39], v[228:231], v[172:175], a[36:39]\n"
  "buffer_load_dwordx4 v244, s[44:47], s77 offen nt lds\n"
  "v_mfma_f32_16x16x32_bf16 a[68:71], v[228:231], v[176:179], a[68:71]\n"
  "v_or3_b32 v240, v240, v136, v137\n"
  "v_cvt_pk_bf16_f32 v114, v140, v141\n"
  "v_mfma_f32_16x16x32_bf16 a[100:103], v[228:231], v[180:183], a[100:103]\n"
  "v_exp_f32 v142, v142\n"
  "v_mfma_f32_16x16x32_bf16 a[132:135], v[228:231], v[184:187], a[132:135]\n"
  "v_exp_f32 v143, v143\n"
  "v_mfma_f32_16x16x32_bf16 a[164:167], v[228:231], v[188:191], a[164:167]\n"
  "v_exp_f32 v144, v144\n"
  "v_mfma_f32_16x16x32_bf16 a[196:199], v[228:231], v[192:195], a[196:199]\n"
  "s_waitcnt lgkmcnt(0)\n"
  "v_exp_f32 v145, v145\n"
  "v_mfma_f32_16x16x32_bf16 a[8:11], v[232:235], v[168:171], a[8:11]\n"
  "ds_read_b64_tr_b16 v[224:225], v250 offset:0\n"
  "ds_read_b64_tr_b16 v[226:227], v250 offset:1024\n"
  "v_mfma_f32_16x16x32_bf16 a[40:43], v[232:235], v[172:175], a[40:43]\n"
  "ds_read_b64_tr_b16 v[228:229], v250 offset:32\n"
  "ds_read_b64_tr_b16 v[230:231], v250 offset:1056\n"
  "v_mfma_f32_16x16x32_bf16 a[72:75], v[232:235], v[176:179], a[72:75]\n"
  "v_cvt_pk_bf16_f32 v115, v142, v143\n"
  "v_or3_b32 v240, v240, v114, v115\n"
  "v_mfma_f32_16x16x32_bf16 a[104:107], v[232:235], v[180:183], a[104:107]\n"
  "v_exp_f32 v146, v146\n"
  "v_mfma_f32_16x16x32_bf16 a[136:139], v[232:235], v[184:187], a[136:139]\n"
  "v_exp_f32 v147, v147\n"
  "v_mfma_f32_16x16x32_bf16 a[168:171], v[232:235], v[188:191], a[168:171]\n"
  "buffer_load_dwordx4 v244, s[44:47], s77 offen offset:1024 nt lds\n"
  "v_mfma_f32_16x16x32_bf16 a[200:203], v[232:235], v[192:195], a[200:203]\n"
  "v_cvt_pk_bf16_f32 v118, v144, v145\n"
  "v_cvt_pk_bf16_f32 v119, v146, v147\n"
  "v_mfma_f32_16x16x32_bf16 a[12:15], v[236:239], v[168:171], a[12:15]\n"
  "v_exp_f32 v148, v148\n"
  "v_mfma_f32_16x16x32_bf16 a[44:47], v[236:239], v[172:175], a[44:47]\n"
  "v_exp_f32 v149, v149\n"
  "v_mfma_f32_16x16x32_bf16 a[76:79], v[236:239], v[176:179], a[76:79]\n"
  "v_or3_b32 v240, v240, v118, v119\n"
  "v_cvt_pk_bf16_f32 v122, v148, v149\n"
  "v_mfma_f32_16x16x32_bf16 a[108:111], v[236:239], v[180:183], a[108:111]\n"
  "buffer_load_dwordx4 v244, s[44:47], s77 offen offset:2048 nt lds\n"
  "v_mfma_f32_16x16x32_bf16 a[140:143], v[236:239], v[184:187], a[140:143]\n"
  "v_exp_f32 v150, v150\n"
  "v_mfma_f32_16x16x32_bf16 a[172:175], v[236:239], v[188:191], a[172:175]\n"
  "v_exp_f32 v151, v151\n"
  "v_mfma_f32_16x16x32_bf16 a[204:207], v[236:239], v[192:195], a[204:207]\n"
  "s_waitcnt lgkmcnt(0)\n"
  "v_exp_f32 v152, v152\n"
  "v_mfma_f32_16x16x32_bf16 a[16:19], v[224:227], v[168:171], a[16:19]\n"
  "ds_read_b64_tr_b16 v[232:233], v251 offset:0\n"
  "ds_read_b64_tr_b16 v[234:235], v251 offset:1024\n"
  "v_mfma_f32_16x16x32_bf16 a[48:51], v[224:227], v[172:175], a[48:51]\n"
  "ds_read_b64_tr_b16 v[236:237], v251 offset:32\n"
  "ds_read_b64_tr_b16 v[238:239], v251 offset:1056\n"
  "v_mfma_f32_16x16x32_bf16 a[80:83], v[224:227], v[176:179], a[80:83]\n"
  "v_exp_f32 v153, v153\n"
  "v_mfma_f32_16x16x32_bf16 a[112:115], v[224:227], v[180:183], a[112:115]\n"
  "v_cvt_pk_bf16_f32 v123, v150, v151\n"
  "v_or3_b32 v240, v240, v122, v123\n"
  "v_mfma_f32_16x16x32_bf16 a[144:147], v[224:227], v[184:187], a[144:147]\n"
  "v_exp_f32 v154, v154\n"
  "v_mfma_f32_16x16x32_bf16 a[176:179], v[224:227], v[188:191], a[176:179]\n"
  "buffer_load_dwordx4 v244, s[44:47], s77 offen offset:3072 nt lds\n"
  "v_mfma_f32_16x16x32_bf16 a[208:211], v[224:227], v[192:195], a[208:211]\n"
  "v_exp_f32 v155, v155\n"
  "v_mfma_f32_16x16x32_bf16 a[20:23], v[228:231], v[168:171], a[20:23]\n"
  "v_cvt_pk_bf16_f32 v126, v152, v153\n"
  "v_cvt_pk_bf16_f32 v127, v154, v155\n"
  "v_mfma_f32_16x16x32_bf16 a[52:55], v[228:231], v[172:175], a[52:55]\n"
  "v_exp_f32 v156, v156\n"
  "v_mfma_f32_16x16x32_bf16 a[84:87], v[228:231], v[176:179], a[84:87]\n"
  "v_exp_f32 v157, v157\n"
  "v_mfma_f32_16x16x32_bf16 a[116:119], v[228:231], v[180:183], a[116:119]\n"
  "v_or3_b32 v240, v240, v126, v127\n"
  "v_cvt_pk_bf16_f32 v130, v156, v157\n"
  "v_mfma_f32_16x16x32_bf16 a[148:151], v[228:231], v[184:187], a[148:151]\n"
  "v_exp_f32 v158, v158\n"
  "v_mfma_f32_16x16x32_bf16 a[180:183], v[228:231], v[188:191], a[180:183]\n"
  "v_exp_f32 v159, v159\n"
  "v_mfma_f32_16x16x32_bf16 a[212:215], v[228:231], v[192:195], a[212:215]\n"
  "s_waitcnt vmcnt(8) lgkmcnt(0)\n"
  "v_exp_f32 v160, v160\n"
  "v_mfma_f32_16x16x32_bf16 a[24:27], v[232:235], v[168:171], a[24:27]\n"
  "ds_read_b128 v[224:227], v246 offset:0\n"
  "ds_read_b128 v[228:231], v247 offset:0\n"
  "v_mfma_f32_16x16x32_bf16 a[56:59], v[232:235], v[172:175], a[56:59]\n"
  "s_cmp_lt_u32 s60, 15\n"
  "v_exp_f32 v161, v161\n"
  "v_mfma_f32_16x16x32_bf16 a[88:91], v[232:235], v[176:179], a[88:91]\n"
  "v_cvt_pk_bf16_f32 v131, v158, v159\n"
  "v_or3_b32 v240, v240, v130, v131\n"
  "v_mfma_f32_16x16x32_bf16 a[120:123], v[232:235], v[180:183], a[120:123]\n"
  "v_exp_f32 v162, v162\n"
  "v_mfma_f32_16x16x32_bf16 a[152:155], v[232:235], v[184:187], a[152:155]\n"
  "v_exp_f32 v163, v163\n"
  "v_mfma_f32_16x16x32_bf16 a[184:187], v[232:235], v[188:191], a[184:187]\n"
  "v_cvt_pk_bf16_f32 v134, v160, v161\n"
  "v_cvt_pk_bf16_f32 v135, v162, v163\n"
  "v_mfma_f32_16x16x32_bf16 a[216:219], v[232:235], v[192:195], a[216:219]\n"
  "v_exp_f32 v164, v164\n"
  "v_mfma_f32_16x16x32_bf16 a[28:31], v[236:239], v[168:171], a[28:31]\n"
  "v_exp_f32 v165, v165\n"
  "v_mfma_f32_16x16x32_bf16 a[60:63], v[236:239], v[172:175], a[60:63]\n"
  "v_or3_b32 v240, v240, v134, v135\n"
  "v_cvt_pk_bf16_f32 v138, v164, v165\n"
  "v_mfma_f32_16x16x32_bf16 a[92:95], v[236:239], v[176:179], a[92:95]\n"
  "v_exp_f32 v166, v166\n"
  "v_mfma_f32_16x16x32_bf16 a[124:127], v[236:239], v[180:183], a[124:127]\n"
  "v_exp_f32 v167, v167\n"
  "v_mfma_f32_16x16x32_bf16 a[156:159], v[236:239], v[184:187], a[156:159]\n"
  "v_cvt_pk_bf16_f32 v139, v166, v167\n"
  "v_or3_b32 v240, v240, v138, v139\n"
  "v_mfma_f32_16x16x32_bf16 a[188:191], v[236:239], v[188:191], a[188:191]\n"
  "v_and_b32 v240, s82, v240\n"
  "v_cmp_ne_u32 vcc, 0, v240\n"
  "v_mfma_f32_16x16x32_bf16 a[220:223], v[236:239], v[192:195], a[220:223]\n"
  "s_waitcnt lgkmcnt(0)\n"
  "s_cbranch_vccnz L_rare_even\n"
  "L_odd:\n"
  "v_mfma_f32_16x16x32_bf16 v[168:171], v[224:227], v[0:3], v[196:199]\n"
  "ds_read_b128 v[232:235], v246 offset:32\n"
  "ds_read_b128 v[236:239], v247 offset:32\n"
  "v_mfma_f32_16x16x32_bf16 v[172:175], v[224:227], v[16:19], v[200:203]\n"
  "s_add_u32 s78, s78, 0x2000\n"
  "s_and_b32 s78, s78, 0x1ffff\n"
  "s_add_u32 m0, s72, 66048\n"
  "v_mfma_f32_16x16x32_bf16 v[176:179], v[224:227], v[32:35], v[204:207]\n"
  "s_add_u32 s79, s78, 4096\n"
  "buffer_load_dwordx4 v245, s[48:51], s78 offen nt lds\n"
  "v_mfma_f32_16x16x32_bf16 v[180:183], v[224:227], v[48:51], v[208:211]\n"
  "s_add_u32 s76, s76, 0x2000\n"
  "s_and_b32 s76, s76, 0x1ffff\n"
  "s_add_u32 s77, s76, 4096\n"
  "v_mfma_f32_16x16x32_bf16 v[184:187], v[224:227], v[64:67], v[212:215]\n"
  "buffer_load_dwordx4 v245, s[48:51], s78 offen offset:1024 nt lds\n"
  "v_mfma_f32_16x16x32_bf16 v[188:191], v[224:227], v[80:83], v[216:219]\n"
  "s_cmp_lt_u32 s60, 13\n"
  "s_cselect_b32 s46, 0x20000, 0\n"
  "s_add_u32 s60, s60, 1\n"
  "v_mfma_f32_16x16x32_bf16 v[192:195], v[224:227], v[96:99], v[220:223]\n"
  "buffer_load_dwordx4 v245, s[48:51], s78 offen offset:2048 nt lds\n"
  "v_mfma_f32_16x16x32_bf16 v[168:171], v[228:231], v[4:7], v[168:171]\n"
  "v_mfma_f32_16x16x32_bf16 v[172:175], v[228:231], v[20:23], v[172:175]\n"
  "buffer_load_dwordx4 v245, s[48:51], s78 offen offset:3072 nt lds\n"
  "v_mfma_f32_16x16x32_bf16 v[176:179], v[228:231], v[36:39], v[176:179]\n"
  "s_add_u32 m0, s72, 70176\n"
  "v_mfma_f32_16x16x32_bf16 v[180:183], v[228:231], v[52:55], v[180:183]\n"
  "buffer_load_dwordx4 v245, s[48:51], s79 offen nt lds\n"
  "v_mfma_f32_16x16x32_bf16 v[184:187], v[228:231], v[68:71], v[184:187]\n"
  "v_mfma_f32_16x16x32_bf16 v[188:191], v[228:231], v[84:87], v[188:191]\n"
  "buffer_load_dwordx4 v245, s[48:51], s79 offen offset:1024 nt lds\n"
  "v_mfma_f32_16x16x32_bf16 v[192:195], v[228:231], v[100:103], v[192:195]\n"
  "s_waitcnt lgkmcnt(0)\n"
  "v_mfma_f32_16x16x32_bf16 v[168:171], v[232:235], v[8:11], v[168:171]\n"
  "ds_read_b128 v[224:227], v246 offset:1024\n"
  "ds_read_b128 v[228:231], v247 offset:1024\n"
  "v_mfma_f32_16x16x32_bf16 v[168:171], v[236:239], v[12:15], v[168:171]\n"
  "v_mov_b32 v241, s60\n"
  "v_mfma_f32_16x16x32_bf16 v[172:175], v[232:235], v[24:27], v[172:175]\n"
  "buffer_load_dwordx4 v245, s[48:51], s79 offen offset:2048 nt lds\n"
  "v_mfma_f32_16x16x32_bf16 v[172:175], v[236:239], v[28:31], v[172:175]\n"
  "ds_write_b32 v253, v241 offset:8228\n"
  "v_mfma_f32_16x16x32_bf16 v[176:179], v[232:235], v[40:43], v[176:179]\n"
  "buffer_load_dwordx4 v245, s[48:51], s79 offen offset:3072 nt lds\n"
  "v_mfma_f32_16x16x32_bf16 v[176:179], v[236:239], v[44:47], v[176:179]\n"
  "s_add_u32 m0, s72, 0\n"
  "v_mfma_f32_16x16x32_bf16 v[180:183], v[232:235], v[56:59], v[180:183]\n"
  "v_exp_f32 v168, v168\n"
  "v_mfma_f32_16x16x32_bf16 v[180:183], v[236:239], v[60:63], v[180:183]\n"
  "v_exp_f32 v169, v169\n"
  "v_mfma_f32_16x16x32_bf16 v[184:187], v[232:235], v[72:75], v[184:187]\n"
  "v_exp_f32 v170, v170\n"
  "v_mfma_f32_16x16x32_bf16 v[184:187], v[236:239], v[76:79], v[184:187]\n"
  "v_exp_f32 v171, v171\n"
  "v_mfma_f32_16x16x32_bf16 v[188:191], v[232:235], v[88:91], v[188:191]\n"
  "v_exp_f32 v172, v172\n"
  "v_mfma_f32_16x16x32_bf16 v[188:191], v[236:239], v[92:95], v[188:191]\n"
  "v_exp_f32 v173, v173\n"
  "v_mfma_f32_16x16x32_bf16 v[192:195], v[232:235], v[104:107], v[192:195]\n"
  "v_cvt_pk_bf16_f32 v168, v168, v169\n"
  "v_cvt_pk_bf16_f32 v169, v170, v171\n"
  "v_mfma_f32_16x16x32_bf16 v[192:195], v[236:239], v[108:111], v[192:195]\n"
  "s_waitcnt lgkmcnt(1)\n"
  "v_or_b32 v240, v168, v169\n"
  "v_cvt_pk_bf16_f32 v172, v172, v173\n"
  "v_mfma_f32_16x16x32_bf16 v[140:143], v[224:227], v[0:3], v[196:199]\n"
  "ds_read_b128 v[232:235], v246 offset:1056\n"
  "ds_read_b128 v[236:239], v247 offset:1056\n"
  "v_mfma_f32_16x16x32_bf16 v[144:147], v[224:227], v[16:19], v[200:203]\n"
  "v_exp_f32 v174, v174\n"
  "v_mfma_f32_16x16x32_bf16 v[148:151], v[224:227], v[32:35], v[204:207]\n"
  "v_exp_f32 v175, v175\n"
  "v_mfma_f32_16x16x32_bf16 v[152:155], v[224:227], v[48:51], v[208:211]\n"
  "v_exp_f32 v176, v176\n"
  "v_mfma_f32_16x16x32_bf16 v[156:159], v[224:227], v[64:67], v[212:215]\n"
  "v_exp_f32 v177, v177\n"
  "v_mfma_f32_16x16x32_bf16 v[160:163], v[224:227], v[80:83], v[216:219]\n"
  "v_cvt_pk_bf16_f32 v173, v174, v175\n"
  "v_or3_b32 v240, v240, v172, v173\n"
  "v_mfma_f32_16x16x32_bf16 v[164:167], v[224:227], v[96:99], v[220:223]\n"
  "v_exp_f32 v178, v178\n"
  "v_mfma_f32_16x16x32_bf16 v[140:143], v[228:231], v[4:7], v[140:143]\n"
  "v_exp_f32 v179, v179\n"
  "v_mfma_f32_16x16x32_bf16 v[144:147], v[228:231], v[20:23], v[144:147]\n"
  "v_exp_f32 v180, v180\n"
  "v_mfma_f32_16x16x32_bf16 v[148:151], v[228:231], v[36:39], v[148:151]\n"
  "v_exp_f32 v181, v181\n"
  "v_mfma_f32_16x16x32_bf16 v[152:155], v[228:231], v[52:55], v[152:155]\n"
  "v_cvt_pk_bf16_f32 v176, v176, v177\n"
  "v_cvt_pk_bf16_f32 v177, v178, v179\n"
  "v_mfma_f32_16x16x32_bf16 v[156:159], v[228:231], v[68:71], v[156:159]\n"
  "v_or3_b32 v240, v240, v176, v177\n"
  "v_cvt_pk_bf16_f32 v180, v180, v181\n"
  "v_mfma_f32_16x16x32_bf16 v[160:163], v[228:231], v[84:87], v[160:163]\n"
  "v_exp_f32 v182, v182\n"
  "v_mfma_f32_16x16x32_bf16 v[164:167], v[228:231], v[100:103], v[164:167]\n"
  "s_waitcnt lgkmcnt(0)\n"
  "v_exp_f32 v183, v183\n"
  "v_mfma_f32_16x16x32_bf16 v[140:143], v[232:235], v[8:11], v[140:143]\n"
  "s_waitcnt vmcnt(16)\n"
  "ds_read_b64_tr_b16 v[224:225], v248 offset:33024\n"
  "ds_read_b64_tr_b16 v[226:227], v248 offset:34048\n"
  "v_mfma_f32_16x16x32_bf16 v[140:143], v[236:239], v[12:15], v[140:143]\n"
  "ds_read_b64_tr_b16 v[228:229], v248 offset:33056\n"
  "ds_read_b64_tr_b16 v[230:231], v248 offset:34080\n"
  "v_mfma_f32_16x16x32_bf16 v[144:147], v[232:235], v[24:27], v[144:147]\n"
  "v_exp_f32 v184, v184\n"
  "v_mfma_f32_16x16x32_bf16 v[144:147], v[236:239], v[28:31], v[144:147]\n"
  "v_exp_f32 v185, v185\n"
  "v_mfma_f32_16x16x32_bf16 v[148:151], v[232:235], v[40:43], v[148:151]\n"
  "v_cvt_pk_bf16_f32 v181, v182, v183\n"
  "v_or3_b32 v240, v240, v180, v181\n"
  "v_mfma_f32_16x16x32_bf16 v[148:151], v[236:239], v[44:47], v[148:151]\n"
  "v_exp_f32 v186, v186\n"
  "v_mfma_f32_16x16x32_bf16 v[152:155], v[232:235], v[56:59], v[152:155]\n"
  "buffer_load_dwordx4 v244, s[44:47], s76 offen nt lds\n"
  "v_mfma_f32_16x16x32_bf16 v[152:155], v[236:239], v[60:63], v[152:155]\n"
  "v_exp_f32 v187, v187\n"
  "v_mfma_f32_16x16x32_bf16 v[156:159], v[232:235], v[72:75], v[156:159]\n"
  "v_exp_f32 v188, v188\n"
  "v_mfma_f32_16x16x32_bf16 v[156:159], v[236:239], v[76:79], v[156:159]\n"
  "v_exp_f32 v189, v189\n"
  "v_mfma_f32_16x16x32_bf16 v[160:163], v[232:235], v[88:91], v[160:163]\n"
  "v_cvt_pk_bf16_f32 v184, v184, v185\n"
  "v_cvt_pk_bf16_f32 v185, v186, v187\n"
  "v_mfma_f32_16x16x32_bf16 v[160:163], v[236:239], v[92:95], v[160:163]\n"
  "buffer_load_dwordx4 v244, s[44:47], s76 offen offset:1024 nt lds\n"
  "v_mfma_f32_16x16x32_bf16 v[164:167], v[232:235], v[104:107], v[164:167]\n"
  "v_or3_b32 v240, v240, v184, v185\n"
  "v_cvt_pk_bf16_f32 v188, v188, v189\n"
  "v_mfma_f32_16x16x32_bf16 v[164:167], v[236:239], v[108:111], v[164:167]\n"
  "v_exp_f32 v190, v190\n"
  "v_mfma_f32_16x16x32_bf16 a[224:227], a[252:255], v[112:115], a[224:227]\n"
  "ds_read_b64_tr_b16 v[232:233], v249 offset:33024\n"
  "ds_read_b64_tr_b16 v[234:235], v249 offset:34048\n"
  "v_mfma_f32_16x16x32_bf16 a[228:231], a[252:255], v[116:119], a[228:231]\n"
  "ds_read_b64_tr_b16 v[236:237], v249 offset:33056\n"
  "ds_read_b64_tr_b16 v[238:239], v249 offset:34080\n"
  "v_mfma_f32_16x16x32_bf16 a[232:235], a[252:255], v[120:123], a[232:235]\n"
  "v_exp_f32 v191, v191\n"
  "v_mfma_f32_16x16x32_bf16 a[236:239], a[252:255], v[124:127], a[236:239]\n"
  "v_exp_f32 v192, v192\n"
  "v_mfma_f32_16x16x32_bf16 a[240:243], a[252:255], v[128:131], a[240:243]\n"
  "buffer_load_dwordx4 v244, s[44:47], s76 offen offset:2048 nt lds\n"
  "v_mfma_f32_16x16x32_bf16 a[244:247], a[252:255], v[132:135], a[244:247]\n"
  "v_exp_f32 v193, v193\n"
  "v_mfma_f32_16x16x32_bf16 a[248:251], a[252:255], v[136:139], a[248:251]\n"
  "s_waitcnt lgkmcnt(4)\n"
  "v_cvt_pk_bf16_f32 v189, v190, v191\n"
  "v_or3_b32 v240, v240, v188, v189\n"
  "v_mfma_f32_16x16x32_bf16 a[0:3], v[224:227], v[112:115], a[0:3]\n"
  "v_exp_f32 v194, v194\n"
  "v_mfma_f32_16x16x32_bf16 a[32:35], v[224:227], v[116:119], a[32:35]\n"
  "v_exp_f32 v195, v195\n"
  "v_mfma_f32_16x16x32_bf16 a[64:67], v[224:227], v[120:123], a[64:67]\n"
  "v_exp_f32 v140, v140\n"
  "v_mfma_f32_16x16x32_bf16 a[96:99], v[224:227], v[124:127], a[96:99]\n"
  "buffer_load_dwordx4 v244, s[44:47], s76 offen offset:3072 nt lds\n"
  "v_mfma_f32_16x16x32_bf16 a[128:131], v[224:227], v[128:131], a[128:131]\n"
  "v_exp_f32 v141, v141\n"
  "v_mfma_f32_16x16x32_bf16 a[160:163], v[224:227], v[132:135], a[160:163]\n"
  "s_add_u32 m0, s72, 4128\n"
  "v_cvt_pk_bf16_f32 v192, v192, v193\n"
  "v_cvt_pk_bf16_f32 v193, v194, v195\n"
  "v_mfma_f32_16x16x32_bf16 a[192:195], v[224:227], v[136:139], a[192:195]\n"
  "v_or3_b32 v240, v240, v192, v193\n"
  "v_cvt_pk_bf16_f32 v170, v140, v141\n"
  "v_mfma_f32_16x16x32_bf16 a[4:7], v[228:231], v[112:115], a[4:7]\n"
  "v_exp_f32 v142, v142\n"
  "v_mfma_f32_16x16x32_bf16 a[36:39], v[228:231], v[116:119], a[36:39]\n"
  "buffer_load_dwordx4 v244, s[44:47], s77 offen nt lds\n"
  "v_mfma_f32_16x16x32_bf16 a[68:71], v[228:231], v[120:123], a[68:71]\n"
  "v_exp_f32 v143, v143\n"
  "v_mfma_f32_16x16x32_bf16 a[100:103], v[228:231], v[124:127], a[100:103]\n"
  "v_exp_f32 v144, v144\n"
  "v_mfma_f32_16x16x32_bf16 a[132:135], v[228:231], v[128:131], a[132:135]\n"
  "v_exp_f32 v145, v145\n"
  "v_mfma_f32_16x16x32_bf16 a[164:167], v[228:231], v[132:135], a[164:167]\n"
  "v_cvt_pk_bf16_f32 v171, v142, v143\n"
  "v_or3_b32 v240, v240, v170, v171\n"
  "v_mfma_f32_16x16x32_bf16 a[196:199], v[228:231], v[136:139], a[196:199]\n"
  "s_waitcnt lgkmcnt(0)\n"
  "v_exp_f32 v146, v146\n"
  "v_mfma_f32_16x16x32_bf16 a[8:11], v[232:235], v[112:115], a[8:11]\n"
  "ds_read_b64_tr_b16 v[224:225], v250 offset:33024\n"
  "ds_read_b64_tr_b16 v[226:227], v250 offset:34048\n"
  "v_mfma_f32_16x16x32_bf16 a[40:43], v[232:235], v[116:119], a[40:43]\n"
  "ds_read_b64_tr_b16 v[228:229], v250 offset:33056\n"
  "ds_read_b64_tr_b16 v[230:231], v250 offset:34080\n"
  "v_mfma_f32_16x16x32_bf16 a[72:75], v[232:235], v[120:123], a[72:75]\n"
  "v_exp_f32 v147, v147\n"
  "v_mfma_f32_16x16x32_bf16 a[104:107], v[232:235], v[124:127], a[104:107]\n"
  "v_cvt_pk_bf16_f32 v174, v144, v145\n"
  "v_cvt_pk_bf16_f32 v175, v146, v147\n"
  "v_mfma_f32_16x16x32_bf16 a[136:139], v[232:235], v[128:131], a[136:139]\n"
  "v_exp_f32 v148, v148\n"
  "v_mfma_f32_16x16x32_bf16 a[168:171], v[232:235], v[132:135], a[168:171]\n"
  "buffer_load_dwordx4 v244, s[44:47], s77 offen offset:1024 nt lds\n"
  "v_mfma_f32_16x16x32_bf16 a[200:203], v[232:235], v[136:139], a[200:203]\n"
  "v_exp_f32 v149, v149\n"
  "v_mfma_f32_16x16x32_bf16 a[12:15], v[236:239], v[112:115], a[12:15]\n"
  "v_or3_b32 v240, v240, v174, v175\n"
  "v_cvt_pk_bf16_f32 v178, v148, v149\n"
  "v_mfma_f32_16x16x32_bf16 a[44:47], v[236:239], v[116:119], a[44:47]\n"
  "v_exp_f32 v150, v150\n"
  "v_mfma_f32_16x16x32_bf16 a[76:79], v[236:239], v[120:123], a[76:79]\n"
  "v_exp_f32 v151, v151\n"
  "v_mfma_f32_16x16x32_bf16 a[108:111], v[236:239], v[124:127], a[108:111]\n"
  "buffer_load_dwordx4 v244, s[44:47], s77 offen offset:2048 nt lds\n"
  "v_mfma_f32_16x16x32_bf16 a[140:143], v[236:239], v[128:131], a[140:143]\n"
  "v_exp_f32 v152, v152\n"
  "v_mfma_f32_16x16x32_bf16 a[172:175], v[236:239], v[132:135], a[172:175]\n"
  "v_exp_f32 v153, v153\n"
  "v_mfma_f32_16x16x32_bf16 a[204:207], v[236:239], v[136:139], a[204:207]\n"
  "s_waitcnt lgkmcnt(0)\n"
  "v_cvt_pk_bf16_f32 v179, v150, v151\n"
  "v_or3_b32 v240, v240, v178, v179\n"
  "v_mfma_f32_16x16x32_bf16 a[16:19], v[224:227], v[112:115], a[16:19]\n"
  "ds_read_b64_tr_b16 v[232:233], v251 offset:33024\n"
  "ds_read_b64_tr_b16 v[234:235], v251 offset:34048\n"
  "v_mfma_f32_16x16x32_bf16 a[48:51], v[224:227], v[116:119], a[48:51]\n"
  "ds_read_b64_tr_b16 v[236:237], v251 offset:33056\n"
  "ds_read_b64_tr_b16 v[238:239], v251 offset:34080\n"
  "v_mfma_f32_16x16x32_bf16 a[80:83], v[224:227], v[120:123], a[80:83]\n"
  "v_exp_f32 v154, v154\n"
  "v_mfma_f32_16x16x32_bf16 a[112:115], v[224:227], v[124:127], a[112:115]\n"
  "v_exp_f32 v155, v155\n"
  "v_mfma_f32_16x16x32_bf16 a[144:147], v[224:227], v[128:131], a[144:147]\n"
  "v_cvt_pk_bf16_f32 v182, v152, v153\n"
  "v_cvt_pk_bf16_f32 v183, v154, v155\n"
  "v_mfma_f32_16x16x32_bf16 a[176:179], v[224:227], v[132:135], a[176:179]\n"
  "buffer_load_dwordx4 v244, s[44:47], s77 offen offset:3072 nt lds\n"
  "v_mfma_f32_16x16x32_bf16 a[208:211], v[224:227], v[136:139], a[208:211]\n"
  "v_exp_f32 v156, v156\n"
  "v_mfma_f32_16x16x32_bf16 a[20:23], v[228:231], v[112:115], a[20:23]\n"
  "v_exp_f32 v157, v157\n"
  "v_mfma_f32_16x16x32_bf16 a[52:55], v[228:231], v[116:119], a[52:55]\n"
  "v_or3_b32 v240, v240, v182, v183\n"
  "v_cvt_pk_bf16_f32 v186, v156, v157\n"
  "v_mfma_f32_16x16x32_bf16 a[84:87], v[228:231], v[120:123], a[84:87]\n"
  "v_exp_f32 v158, v158\n"
  "v_mfma_f32_16x16x32_bf16 a[116:119], v[228:231], v[124:127], a[116:119]\n"
  "v_exp_f32 v159, v159\n"
  "v_mfma_f32_16x16x32_bf16 a[148:151], v[228:231], v[128:131], a[148:151]\n"
  "v_exp_f32 v160, v160\n"
  "v_mfma_f32_16x16x32_bf16 a[180:183], v[228:231], v[132:135], a[180:183]\n"
  "v_exp_f32 v161, v161\n"
  "v_mfma_f32_16x16x32_bf16 a[212:215], v[228:231], v[136:139], a[212:215]\n"
  "s_waitcnt vmcnt(16) lgkmcnt(0)\n"
  "v_cvt_pk_bf16_f32 v187, v158, v159\n"
  "v_or3_b32 v240, v240, v186, v187\n"
  "v_mfma_f32_16x16x32_bf16 a[24:27], v[232:235], v[112:115], a[24:27]\n"
  "ds_read_b128 v[224:227], v246 offset:33024\n"
  "ds_read_b128 v[228:231], v247 offset:33024\n"
  "v_mfma_f32_16x16x32_bf16 a[56:59], v[232:235], v[116:119], a[56:59]\n"
  "v_exp_f32 v162, v162\n"
  "v_mfma_f32_16x16x32_bf16 a[88:91], v[232:235], v[120:123], a[88:91]\n"
  "v_exp_f32 v163, v163\n"
  "v_mfma_f32_16x16x32_bf16 a[120:123], v[232:235], v[124:127], a[120:123]\n"
  "v_cvt_pk_bf16_f32 v190, v160, v161\n"
  "v_cvt_pk_bf16_f32 v191, v162, v163\n"
  "v_mfma_f32_16x16x32_bf16 a[152:155], v[232:235], v[128:131], a[152:155]\n"
  "v_exp_f32 v164, v164\n"
  "v_mfma_f32_16x16x32_bf16 a[184:187], v[232:235], v[132:135], a[184:187]\n"
  "v_exp_f32 v165, v165\n"
  "v_mfma_f32_16x16x32_bf16 a[216:219], v[232:235], v[136:139], a[216:219]\n"
  "v_or3_b32 v240, v240, v190, v191\n"
  "v_cvt_pk_bf16_f32 v194, v164, v165\n"
  "v_mfma_f32_16x16x32_bf16 a[28:31], v[236:239], v[112:115], a[28:31]\n"
  "v_exp_f32 v166, v166\n"
  "v_mfma_f32_16x16x32_bf16 a[60:63], v[236:239], v[116:119], a[60:63]\n"
  "v_exp_f32 v167, v167\n"
  "v_mfma_f32_16x16x32_bf16 a[92:95], v[236:239], v[120:123], a[92:95]\n"
  "v_cvt_pk_bf16_f32 v195, v166, v167\n"
  "v_or3_b32 v240, v240, v194, v195\n"
  "v_mfma_f32_16x16x32_bf16 a[124:127], v[236:239], v[124:127], a[124:127]\n"
  "v_and_b32 v240, s82, v240\n"
  "v_cmp_ne_u32 vcc, 0, v240\n"
  "v_mfma_f32_16x16x32_bf16 a[156:159], v[236:239], v[128:131], a[156:159]\n"
  "v_mfma_f32_16x16x32_bf16 a[188:191], v[236:239], v[132:135], a[188:191]\n"
  "v_mfma_f32_16x16x32_bf16 a[220:223], v[236:239], v[136:139], a[220:223]\n"
  "s_waitcnt lgkmcnt(0)\n"
  "s_cbranch_vccnz L_rare_odd\n"
  "L_ret_odd:\n"
  "L_even:\n"
  "v_mfma_f32_16x16x32_bf16 v[112:115], v[224:227], v[0:3], v[196:199]\n"
  "ds_read_b128 v[232:235], v246 offset:33056\n"
  "ds_read_b128 v[236:239], v247 offset:33056\n"
  "v_mfma_f32_16x16x32_bf16 v[116:119], v[224:227], v[16:19], v[200:203]\n"
  "s_add_u32 s78, s78, 0x2000\n"
  "s_and_b32 s78, s78, 0x1ffff\n"
  "s_add_u32 m0, s72, 99072\n"
  "v_mfma_f32_16x16x32_bf16 v[120:123], v[224:227], v[32:35], v[204:207]\n"
  "s_add_u32 s79, s78, 4096\n"
  "buffer_load_dwordx4 v245, s[48:51], s78 offen nt lds\n"
  "v_mfma_f32_16x16x32_bf16 v[124:127], v[224:227], v[48:51], v[208:211]\n"
  "s_add_u32 s76, s76, 0x2000\n"
  "s_and_b32 s76, s76, 0x1ffff\n"
  "s_add_u32 s77, s76, 4096\n"
  "v_mfma_f32_16x16x32_bf16 v[128:131], v[224:227], v[64:67], v[212:215]\n"
  "buffer_load_dwordx4 v245, s[48:51], s78 offen offset:1024 nt lds\n"
  "v_mfma_f32_16x16x32_bf16 v[132:135], v[224:227], v[80:83], v[216:219]\n"
  "s_cmp_lt_u32 s60, 13\n"
  "s_cselect_b32 s46, 0x20000, 0\n"
  "s_add_u32 s60, s60, 1\n"
  "v_mfma_f32_16x16x32_bf16 v[136:139], v[224:227], v[96:99], v[220:223]\n"
  "buffer_load_dwordx4 v245, s[48:51], s78 offen offset:2048 nt lds\n"
  "v_mfma_f32_16x16x32_bf16 v[112:115], v[228:231], v[4:7], v[112:115]\n"
  "v_mfma_f32_16x16x32_bf16 v[116:119], v[228:231], v[20:23], v[116:119]\n"
  "buffer_load_dwordx4 v245, s[48:51], s78 offen offset:3072 nt lds\n"
  "v_mfma_f32_16x16x32_bf16 v[120:123], v[228:231], v[36:39], v[120:123]\n"
  "s_add_u32 m0, s72, 103200\n"
  "v_mfma_f32_16x16x32_bf16 v[124:127], v[228:231], v[52:55], v[124:127]\n"
  "buffer_load_dwordx4 v245, s[48:51], s79 offen nt lds\n"
  "v_mfma_f32_16x16x32_bf16 v[128:131], v[228:231], v[68:71], v[128:131]\n"
  "v_mfma_f32_16x16x32_bf16 v[132:135], v[228:231], v[84:87], v[132:135]\n"
  "buffer_load_dwordx4 v245, s[48:51], s79 offen offset:1024 nt lds\n"
  "v_mfma_f32_16x16x32_bf16 v[136:139], v[228:231], v[100:103], v[136:139]\n"
  "s_waitcnt lgkmcnt(0)\n"
  "v_mfma_f32_16x16x32_bf16 v[112:115], v[232:235], v[8:11], v[112:115]\n"
  "ds_read_b128 v[224:227], v246 offset:34048\n"
  "ds_read_b128 v[228:231], v247 offset:34048\n"
  "v_mfma_f32_16x16x32_bf16 v[112:115], v[236:239], v[12:15], v[112:115]\n"
  "v_mov_b32 v241, s60\n"
  "v_mfma_f32_16x16x32_bf16 v[116:119], v[232:235], v[24:27], v[116:119]\n"
  "buffer_load_dwordx4 v245, s[48:51], s79 offen offset:2048 nt lds\n"
  "v_mfma_f32_16x16x32_bf16 v[116:119], v[236:239], v[28:31], v[116:119]\n"
  "ds_write_b32 v253, v241 offset:8228\n"
  "v_mfma_f32_16x16x32_bf16 v[120:123], v[232:235], v[40:43], v[120:123]\n"
  "buffer_load_dwordx4 v245, s[48:51], s79 offen offset:3072 nt lds\n"
  "v_mfma_f32_16x16x32_bf16 v[120:123], v[236:239], v[44:47], v[120:123]\n"
  "s_add_u32 m0, s72, 33024\n"
  "v_mfma_f32_16x16x32_bf16 v[124:127], v[232:235], v[56:59], v[124:127]\n"
  "v_exp_f32 v112, v112\n"
  "v_mfma_f32_16x16x32_bf16 v[124:127], v[236:239], v[60:63], v[124:127]\n"
  "v_exp_f32 v113, v113\n"
  "v_mfma_f32_16x16x32_bf16 v[128:131], v[232:235], v[72:75], v[128:131]\n"
  "v_exp_f32 v114, v114\n"
  "v_mfma_f32_16x16x32_bf16 v[128:131], v[236:239], v[76:79], v[128:131]\n"
  "v_exp_f32 v115, v115\n"
  "v_mfma_f32_16x16x32_bf16 v[132:135], v[232:235], v[88:91], v[132:135]\n"
  "v_exp_f32 v116, v116\n"
  "v_mfma_f32_16x16x32_bf16 v[132:135], v[236:239], v[92:95], v[132:135]\n"
  "v_exp_f32 v117, v117\n"
  "v_mfma_f32_16x16x32_bf16 v[136:139], v[232:235], v[104:107], v[136:139]\n"
  "v_cvt_pk_bf16_f32 v112, v112, v113\n"
  "v_cvt_pk_bf16_f32 v113, v114, v115\n"
  "v_mfma_f32_16x16x32_bf16 v[136:139], v[236:239], v[108:111], v[136:139]\n"
  "s_waitcnt lgkmcnt(1)\n"
  "v_or_b32 v240, v112, v113\n"
  "v_cvt_pk_bf16_f32 v116, v116, v117\n"
  "v_mfma_f32_16x16x32_bf16 v[140:143], v[224:227], v[0:3], v[196:199]\n"
  "ds_read_b128 v[232:235], v246 offset:34080\n"
  "ds_read_b128 v[236:239], v247 offset:34080\n"
  "v_mfma_f32_16x16x32_bf16 v[144:147], v[224:227], v[16:19], v[200:203]\n"
  "v_exp_f32 v118, v118\n"
  "v_mfma_f32_16x16x32_bf16 v[148:151], v[224:227], v[32:35], v[204:207]\n"
  "v_exp_f32 v119, v119\n"
  "v_mfma_f32_16x16x32_bf16 v[152:155], v[224:227], v[48:51], v[208:211]\n"
  "v_exp_f32 v120, v120\n"
  "v_mfma_f32_16x16x32_bf16 v[156:159], v[224:227], v[64:67], v[212:215]\n"
  "v_exp_f32 v121, v121\n"
  "v_mfma_f32_16x16x32_bf16 v[160:163], v[224:227], v[80:83], v[216:219]\n"
  "v_cvt_pk_bf16_f32 v117, v118, v119\n"
  "v_or3_b32 v240, v240, v116, v117\n"
  "v_mfma_f32_16x16x32_bf16 v[164:167], v[224:227], v[96:99], v[220:223]\n"
  "v_exp_f32 v122, v122\n"
  "v_mfma_f32_16x16x32_bf16 v[140:143], v[228:231], v[4:7], v[140:143]\n"
  "v_exp_f32 v123, v123\n"
  "v_mfma_f32_16x16x32_bf16 v[144:147], v[228:231], v[20:23], v[144:147]\n"
  "v_exp_f32 v124, v124\n"
  "v_mfma_f32_16x16x32_bf16 v[148:151], v[228:231], v[36:39], v[148:151]\n"
  "v_exp_f32 v125, v125\n"
  "v_mfma_f32_16x16x32_bf16 v[152:155], v[228:231], v[52:55], v[152:155]\n"
  "v_cvt_pk_bf16_f32 v120, v120, v121\n"
  "v_cvt_pk_bf16_f32 v121, v122, v123\n"
  "v_mfma_f32_16x16x32_bf16 v[156:159], v[228:231], v[68:71], v[156:159]\n"
  "v_or3_b32 v240, v240, v120, v121\n"
  "v_cvt_pk_bf16_f32 v124, v124, v125\n"
  "v_mfma_f32_16x16x32_bf16 v[160:163], v[228:231], v[84:87], v[160:163]\n"
  "v_exp_f32 v126, v126\n"
  "v_mfma_f32_16x16x32_bf16 v[164:167], v[228:231], v[100:103], v[164:167]\n"
  "s_waitcnt lgkmcnt(0)\n"
  "v_exp_f32 v127, v127\n"
  "v_mfma_f32_16x16x32_bf16 v[140:143], v[232:235], v[8:11], v[140:143]\n"
  "s_waitcnt vmcnt(16)\n"
  "ds_read_b64_tr_b16 v[224:225], v248 offset:0\n"
  "ds_read_b64_tr_b16 v[226:227], v248 offset:1024\n"
  "v_mfma_f32_16x16x32_bf16 v[140:143], v[236:239], v[12:15], v[140:143]\n"
  "ds_read_b64_tr_b16 v[228:229], v248 offset:32\n"
  "ds_read_b64_tr_b16 v[230:231], v248 offset:1056\n"
  "v_mfma_f32_16x16x32_bf16 v[144:147], v[232:235], v[24:27], v[144:147]\n"
  "v_exp_f32 v128, v128\n"
  "v_mfma_f32_16x16x32_bf16 v[144:147], v[236:239], v[28:31], v[144:147]\n"
  "v_exp_f32 v129, v129\n"
  "v_mfma_f32_16x16x32_bf16 v[148:151], v[232:235], v[40:43], v[148:151]\n"
  "v_cvt_pk_bf16_f32 v125, v126, v127\n"
  "v_or3_b32 v240, v240, v124, v125\n"
  "v_mfma_f32_16x16x32_bf16 v[148:151], v[236:239], v[44:47], v[148:151]\n"
  "v_exp_f32 v130, v130\n"
  "v_mfma_f32_16x16x32_bf16 v[152:155], v[232:235], v[56:59], v[152:155]\n"
  "buffer_load_dwordx4 v244, s[44:47], s76 offen nt lds\n"
  "v_mfma_f32_16x16x32_bf16 v[152:155], v[236:239], v[60:63], v[152:155]\n"
  "v_exp_f32 v131, v131\n"
  "v_mfma_f32_16x16x32_bf16 v[156:159], v[232:235], v[72:75], v[156:159]\n"
  "v_exp_f32 v132, v132\n"
  "v_mfma_f32_16x16x32_bf16 v[156:159], v[236:239], v[76:79], v[156:159]\n"
  "v_exp_f32 v133, v133\n"
  "v_mfma_f32_16x16x32_bf16 v[160:163], v[232:235], v[88:91], v[160:163]\n"
  "v_cvt_pk_bf16_f32 v128, v128, v129\n"
  "v_cvt_pk_bf16_f32 v129, v130, v131\n"
  "v_mfma_f32_16x16x32_bf16 v[160:163], v[236:239], v[92:95], v[160:163]\n"
  "buffer_load_dwordx4 v244, s[44:47], s76 offen offset:1024 nt lds\n"
  "v_mfma_f32_16x16x32_bf16 v[164:167], v[232:235], v[104:107], v[164:167]\n"
  "v_or3_b32 v240, v240, v128, v129\n"
  "v_cvt_pk_bf16_f32 v132, v132, v133\n"
  "v_mfma_f32_16x16x32_bf16 v[164:167], v[236:239], v[108:111], v[164:167]\n"
  "v_exp_f32 v134, v134\n"
  "v_mfma_f32_16x16x32_bf16 a[224:227], a[252:255], v[168:171], a[224:227]\n"
  "ds_read_b64_tr_b16 v[232:233], v249 offset:0\n"
  "ds_read_b64_tr_b16 v[234:235], v249 offset:1024\n"
  "v_mfma_f32_16x16x32_bf16 a[228:231], a[252:255], v[172:175], a[228:231]\n"
  "ds_read_b64_tr_b16 v[236:237], v249 offset:32\n"
  "ds_read_b64_tr_b16 v[238:239], v249 offset:1056\n"
  "v_mfma_f32_16x16x32_bf16 a[232:235], a[252:255], v[176:179], a[232:235]\n"
  "v_exp_f32 v135, v135\n"
  "v_mfma_f32_16x16x32_bf16 a[236:239], a[252:255], v[180:183], a[236:239]\n"
  "v_exp_f32 v136, v136\n"
  "v_mfma_f32_16x16x32_bf16 a[240:243], a[252:255], v[184:187], a[240:243]\n"
  "buffer_load_dwordx4 v244, s[44:47], s76 offen offset:2048 nt lds\n"
  "v_mfma_f32_16x16x32_bf16 a[244:247], a[252:255], v[188:191], a[244:247]\n"
  "v_exp_f32 v137, v137\n"
  "v_mfma_f32_16x16x32_bf16 a[248:251], a[252:255], v[192:195], a[248:251]\n"
  "s_waitcnt lgkmcnt(4)\n"
  "v_cvt_pk_bf16_f32 v133, v134, v135\n"
  "v_or3_b32 v240, v240, v132, v133\n"
  "v_mfma_f32_16x16x32_bf16 a[0:3], v[224:227], v[168:171], a[0:3]\n"
  "v_exp_f32 v138, v138\n"
  "v_mfma_f32_16x16x32_bf16 a[32:35], v[224:227], v[172:175], a[32:35]\n"
  "v_exp_f32 v139, v139\n"
  "v_mfma_f32_16x16x32_bf16 a[64:67], v[224:227], v[176:179], a[64:67]\n"
  "v_exp_f32 v140, v140\n"
  "v_mfma_f32_16x16x32_bf16 a[96:99], v[224:227], v[180:183], a[96:99]\n"
  "buffer_load_dwordx4 v244, s[44:47], s76 offen offset:3072 nt lds\n"
  "v_mfma_f32_16x16x32_bf16 a[128:131], v[224:227], v[184:187], a[128:131]\n"
  "v_exp_f32 v141, v141\n"
  "v_mfma_f32_16x16x32_bf16 a[160:163], v[224:227], v[188:191], a[160:163]\n"
  "s_add_u32 m0, s72, 37152\n"
  "v_cvt_pk_bf16_f32 v136, v136, v137\n"
  "v_cvt_pk_bf16_f32 v137, v138, v139\n"
  "v_mfma_f32_16x16x32_bf16 a[192:195], v[224:227], v[192:195], a[192:195]\n"
  "v_or3_b32 v240, v240, v136, v137\n"
  "v_cvt_pk_bf16_f32 v114, v140, v141\n"
  "v_mfma_f32_16x16x32_bf16 a[4:7], v[228:231], v[168:171], a[4:7]\n"
  "v_exp_f32 v142, v142\n"
  "v_mfma_f32_16x16x32_bf16 a[36:39], v[228:231], v[172:175], a[36:39]\n"
  "buffer_load_dwordx4 v244, s[44:47], s77 offen nt lds\n"
  "v_mfma_f32_16x16x32_bf16 a[68:71], v[228:231], v[176:179], a[68:71]\n"
  "v_exp_f32 v143, v143\n"
  "v_mfma_f32_16x16x32_bf16 a[100:103], v[228:231], v[180:183], a[100:103]\n"
  "v_exp_f32 v144, v144\n"
  "v_mfma_f32_16x16x32_bf16 a[132:135], v[228:231], v[184:187], a[132:135]\n"
  "v_exp_f32 v145, v145\n"
  "v_mfma_f32_16x16x32_bf16 a[164:167], v[228:231], v[188:191], a[164:167]\n"
  "v_cvt_pk_bf16_f32 v115, v142, v143\n"
  "v_or3_b32 v240, v240, v114, v115\n"
  "v_mfma_f32_16x16x32_bf16 a[196:199], v[228:231], v[192:195], a[196:199]\n"
  "s_waitcnt lgkmcnt(0)\n"
  "v_exp_f32 v146, v146\n"
  "v_mfma_f32_16x16x32_bf16 a[8:11], v[232:235], v[168:171], a[8:11]\n"
  "ds_read_b64_tr_b16 v[224:225], v250 offset:0\n"
  "ds_read_b64_tr_b16 v[226:227], v250 offset:1024\n"
  "v_mfma_f32_16x16x32_bf16 a[40:43], v[232:235], v[172:175], a[40:43]\n"
  "ds_read_b64_tr_b16 v[228:229], v250 offset:32\n"
  "ds_read_b64_tr_b16 v[230:231], v250 offset:1056\n"
  "v_mfma_f32_16x16x32_bf16 a[72:75], v[232:235], v[176:179], a[72:75]\n"
  "v_exp_f32 v147, v147\n"
  "v_mfma_f32_16x16x32_bf16 a[104:107], v[232:235], v[180:183], a[104:107]\n"
  "v_cvt_pk_bf16_f32 v118, v144, v145\n"
  "v_cvt_pk_bf16_f32 v119, v146, v147\n"
  "v_mfma_f32_16x16x32_bf16 a[136:139], v[232:235], v[184:187], a[136:139]\n"
  "v_exp_f32 v148, v148\n"
  "v_mfma_f32_16x16x32_bf16 a[168:171], v[232:235], v[188:191], a[168:171]\n"
  "buffer_load_dwordx4 v244, s[44:47], s77 offen offset:1024 nt lds\n"
  "v_mfma_f32_16x16x32_bf16 a[200:203], v[232:235], v[192:195], a[200:203]\n"
  "v_exp_f32 v149, v149\n"
  "v_mfma_f32_16x16x32_bf16 a[12:15], v[236:239], v[168:171], a[12:15]\n"
  "v_or3_b32 v240, v240, v118, v119\n"
  "v_cvt_pk_bf16_f32 v122, v148, v149\n"
  "v_mfma_f32_16x16x32_bf16 a[44:47], v[236:239], v[172:175], a[44:47]\n"
  "v_exp_f32 v150, v150\n"
  "v_mfma_f32_16x16x32_bf16 a[76:79], v[236:239], v[176:179], a[76:79]\n"
  "v_exp_f32 v151, v151\n"
  "v_mfma_f32_16x16x32_bf16 a[108:111], v[236:239], v[180:183], a[108:111]\n"
  "buffer_load_dwordx4 v244, s[44:47], s77 offen offset:2048 nt lds\n"
  "v_mfma_f32_16x16x32_bf16 a[140:143], v[236:239], v[184:187], a[140:143]\n"
  "v_exp_f32 v152, v152\n"
  "v_mfma_f32_16x16x32_bf16 a[172:175], v[236:239], v[188:191], a[172:175]\n"
  "v_exp_f32 v153, v153\n"
  "v_mfma_f32_16x16x32_bf16 a[204:207], v[236:239], v[192:195], a[204:207]\n"
  "s_waitcnt lgkmcnt(0)\n"
  "v_cvt_pk_bf16_f32 v123, v150, v151\n"
  "v_or3_b32 v240, v240, v122, v123\n"
  "v_mfma_f32_16x16x32_bf16 a[16:19], v[224:227], v[168:171], a[16:19]\n"
  "ds_read_b64_tr_b16 v[232:233], v251 offset:0\n"
  "ds_read_b64_tr_b16 v[234:235], v251 offset:1024\n"
  "v_mfma_f32_16x16x32_bf16 a[48:51], v[224:227], v[172:175], a[48:51]\n"
  "ds_read_b64_tr_b16 v[236:237], v251 offset:32\n"
  "ds_read_b64_tr_b16 v[238:239], v251 offset:1056\n"
  "v_mfma_f32_16x16x32_bf16 a[80:83], v[224:227], v[176:179], a[80:83]\n"
  "v_exp_f32 v154, v154\n"
  "v_mfma_f32_16x16x32_bf16 a[112:115], v[224:227], v[180:183], a[112:115]\n"
  "v_exp_f32 v155, v155\n"
  "v_mfma_f32_16x16x32_bf16 a[144:147], v[224:227], v[184:187], a[144:147]\n"
  "v_cvt_pk_bf16_f32 v126, v152, v153\n"
  "v_cvt_pk_bf16_f32 v127, v154, v155\n"
  "v_mfma_f32_16x16x32_bf16 a[176:179], v[224:227], v[188:191], a[176:179]\n"
  "buffer_load_dwordx4 v244, s[44:47], s77 offen offset:3072 nt lds\n"
  "v_mfma_f32_16x16x32_bf16 a[208:211], v[224:227], v[192:195], a[208:211]\n"
  "v_exp_f32 v156, v156\n"
  "v_mfma_f32_16x16x32_bf16 a[20:23], v[228:231], v[168:171], a[20:23]\n"
  "v_exp_f32 v157, v157\n"
  "v_mfma_f32_16x16x32_bf16 a[52:55], v[228:231], v[172:175], a[52:55]\n"
  "v_or3_b32 v240, v240, v126, v127\n"
  "v_cvt_pk_bf16_f32 v130, v156, v157\n"
  "v_mfma_f32_16x16x32_bf16 a[84:87], v[228:231], v[176:179], a[84:87]\n"
  "v_exp_f32 v158, v158\n"
  "v_mfma_f32_16x16x32_bf16 a[116:119], v[228:231], v[180:183], a[116:119]\n"
  "v_exp_f32 v159, v159\n"
  "v_mfma_f32_16x16x32_bf16 a[148:151], v[228:231], v[184:187], a[148:151]\n"
  "v_exp_f32 v160, v160\n"
  "v_mfma_f32_16x16x32_bf16 a[180:183], v[228:231], v[188:191], a[180:183]\n"
  "v_exp_f32 v161, v161\n"
  "v_mfma_f32_16x16x32_bf16 a[212:215], v[228:231], v[192:195], a[212:215]\n"
  "s_waitcnt vmcnt(16) lgkmcnt(0)\n"
  "v_cvt_pk_bf16_f32 v131, v158, v159\n"
  "v_or3_b32 v240, v240, v130, v131\n"
  "v_mfma_f32_16x16x32_bf16 a[24:27], v[232:235], v[168:171], a[24:27]\n"
  "ds_read_b128 v[224:227], v246 offset:0\n"
  "ds_read_b128 v[228:231], v247 offset:0\n"
  "v_mfma_f32_16x16x32_bf16 a[56:59], v[232:235], v[172:175], a[56:59]\n"
  "s_cmp_lt_u32 s60, 15\n"
  "v_exp_f32 v162, v162\n"
  "v_mfma_f32_16x16x32_bf16 a[88:91], v[232:235], v[176:179], a[88:91]\n"
  "v_exp_f32 v163, v163\n"
  "v_mfma_f32_16x16x32_bf16 a[120:123], v[232:235], v[180:183], a[120:123]\n"
  "v_cvt_pk_bf16_f32 v134, v160, v161\n"
  "v_cvt_pk_bf16_f32 v135, v162, v163\n"
  "v_mfma_f32_16x16x32_bf16 a[152:155], v[232:235], v[184:187], a[152:155]\n"
  "v_exp_f32 v164, v164\n"
  "v_mfma_f32_16x16x32_bf16 a[184:187], v[232:235], v[188:191], a[184:187]\n"
  "v_exp_f32 v165, v165\n"
  "v_mfma_f32_16x16x32_bf16 a[216:219], v[232:235], v[192:195], a[216:219]\n"
  "v_or3_b32 v240, v240, v134, v135\n"
  "v_cvt_pk_bf16_f32 v138, v164, v165\n"
  "v_mfma_f32_16x16x32_bf16 a[28:31], v[236:239], v[168:171], a[28:31]\n"
  "v_exp_f32 v166, v166\n"
  "v_mfma_f32_16x16x32_bf16 a[60:63], v[236:239], v[172:175], a[60:63]\n"
  "v_exp_f32 v167, v167\n"
  "v_mfma_f32_16x16x32_bf16 a[92:95], v[236:239], v[176:179], a[92:95]\n"
  "v_cvt_pk_bf16_f32 v139, v166, v167\n"
  "v_or3_b32 v240, v240, v138, v139\n"
  "v_mfma_f32_16x16x32_bf16 a[124:127], v[236:239], v[180:183], a[124:127]\n"
  "v_and_b32 v240, s82, v240\n"
  "v_cmp_ne_u32 vcc, 0, v240\n"
  "v_mfma_f32_16x16x32_bf16 a[156:159], v[236:239], v[184:187], a[156:159]\n"
  "v_mfma_f32_16x16x32_bf16 a[188:191], v[236:239], v[188:191], a[188:191]\n"
  "v_mfma_f32_16x16x32_bf16 a[220:223], v[236:239], v[192:195], a[220:223]\n"
  "s_waitcnt lgkmcnt(0)\n"
  "s_cbranch_vccnz L_rare_even\n"
  "L_ret_even:\n"
  "s_cbranch_scc1 L_odd\n"
  "s_waitcnt vmcnt(8)\n"
  "ds_read_b64_tr_b16 v[224:225], v248 offset:33024\n"
  "ds_read_b64_tr_b16 v[226:227], v248 offset:34048\n"
  "ds_read_b64_tr_b16 v[228:229], v248 offset:33056\n"
  "ds_read_b64_tr_b16 v[230:231], v248 offset:34080\n"
  "ds_read_b64_tr_b16 v[232:233], v249 offset:33024\n"
  "ds_read_b64_tr_b16 v[234:235], v249 offset:34048\n"
  "ds_read_b64_tr_b16 v[236:237], v249 offset:33056\n"
  "ds_read_b64_tr_b16 v[238:239], v249 offset:34080\n"
  "ds_read_b64_tr_b16 v[140:141], v250 offset:33024\n"
  "ds_read_b64_tr_b16 v[142:143], v250 offset:34048\n"
  "ds_read_b64_tr_b16 v[144:145], v250 offset:33056\n"
  "ds_read_b64_tr_b16 v[146:147], v250 offset:34080\n"
  "ds_read_b64_tr_b16 v[148:149], v251 offset:33024\n"
  "ds_read_b64_tr_b16 v[150:151], v251 offset:34048\n"
  "ds_read_b64_tr_b16 v[152:153], v251 offset:33056\n"
  "ds_read_b64_tr_b16 v[154:155], v251 offset:34080\n"
  "v_mfma_f32_16x16x32_bf16 a[224:227], a[252:255], v[112:115], a[224:227]\n"
  "v_mfma_f32_16x16x32_bf16 a[228:231], a[252:255], v[116:119], a[228:231]\n"
  "v_mfma_f32_16x16x32_bf16 a[232:235], a[252:255], v[120:123], a[232:235]\n"
  "v_mfma_f32_16x16x32_bf16 a[236:239], a[252:255], v[124:127], a[236:239]\n"
  "v_mfma_f32_16x16x32_bf16 a[240:243], a[252:255], v[128:131], a[240:243]\n"
  "v_mfma_f32_16x16x32_bf16 a[244:247], a[252:255], v[132:135], a[244:247]\n"
  "v_mfma_f32_16x16x32_bf16 a[248:251], a[252:255], v[136:139], a[248:251]\n"
  "s_waitcnt lgkmcnt(0)\n"
  "s_mov_b64 s[40:41], %4\n"
  "s_and_b32 s41, s41, 0xffff\n"
  "s_mov_b32 s42, 589824\n"
  "s_mov_b32 s43, 0x20000\n"
  "v_mfma_f32_16x16x32_bf16 a[0:3], v[224:227], v[112:115], a[0:3]\n"
  "v_lshrrev_b32 v169, 4, v252\n"
  "v_mul_u32_u24 v169, 4608, v169\n"
  "v_and_b32 v168, 15, v252\n"
  "v_lshl_add_u32 v244, v168, 4, v169\n"
  "v_mfma_f32_16x16x32_bf16 a[4:7], v[228:231], v[112:115], a[4:7]\n"
  "v_add_u32 v245, 18432, v244\n"
  "v_lshrrev_b32 v169, 1, v252\n"
  "v_mul_u32_u24 v169, 1152, v169\n"
  "v_and_b32 v168, 1, v252\n"
  "v_mfma_f32_16x16x32_bf16 a[8:11], v[232:235], v[112:115], a[8:11]\n"
  "v_lshl_add_u32 v246, v168, 4, v169\n"
  "v_lshl_add_u32 v247, v252, 3, s72\n"
  "v_add_u32 v253, 66048, v247\n"
  "v_lshlrev_b32 v243, 4, v252\n"
  "v_mfma_f32_16x16x32_bf16 a[12:15], v[236:239], v[112:115], a[12:15]\n"
  "v_add_u32 v241, s72, v243\n"
  "v_add_u32 v242, 66048, v241\n"
  "s_bfe_u32 s79, s99, 0x20001\n"
  "s_mov_b32 s62, -1\n"
  "v_mfma_f32_16x16x32_bf16 a[32:35], v[224:227], v[116:119], a[32:35]\n"
  "s_mov_b32 s63, -1\n"
  "s_or_b32 s86, s74, 2\n"
  "s_lshr_b32 s87, s74, 1\n"
  "s_mul_i32 s87, s87, 294912\n"
  "v_mfma_f32_16x16x32_bf16 a[36:39], v[228:231], v[116:119], a[36:39]\n"
  "s_lshl_b32 s66, s86, 8\n"
  "s_add_u32 s66, s66, s87\n"
  "s_lshl_b32 s67, s86, 5\n"
  "s_add_u32 s67, s67, s87\n"
  "v_mfma_f32_16x16x32_bf16 a[40:43], v[232:235], v[116:119], a[40:43]\n"
  "s_add_u32 s67, s67, 1024\n"
  "s_lshl_b32 s68, s74, 8\n"
  "s_add_u32 s68, s68, 0\n"
  "s_lshl_b32 s69, s74, 5\n"
  "v_mfma_f32_16x16x32_bf16 a[44:47], v[236:239], v[116:119], a[44:47]\n"
  "s_add_u32 s69, s69, 0\n"
  "s_add_u32 s69, s69, 1024\n"
  "s_add_u32 s62, s62, 1\n"
  "s_xor_b32 s64, s62, s79\n"
  "v_mfma_f32_16x16x32_bf16 a[64:67], v[224:227], v[120:123], a[64:67]\n"
  "s_and_b32 s65, s64, 1\n"
  "s_mul_i32 s65, s65, 147456\n"
  "s_bfe_u32 s61, s64, 0x20001\n"
  "s_mul_i32 s61, s61, 36864\n"
  "v_mfma_f32_16x16x32_bf16 a[68:71], v[228:231], v[120:123], a[68:71]\n"
  "s_add_u32 s65, s61, s65\n"
  "s_add_u32 s61, s65, s66\n"
  "s_add_u32 s64, s65, s67\n"
  "buffer_load_dwordx4 v[0:3], v244, s[40:43], s61 offen offset:0\n"
  "v_mfma_f32_16x16x32_bf16 a[72:75], v[232:235], v[120:123], a[72:75]\n"
  "buffer_load_dwordx4 v[4:7], v244, s[40:43], s61 offen offset:1152\n"
  "buffer_load_dwordx4 v[8:11], v244, s[40:43], s61 offen offset:2304\n"
  "buffer_load_dwordx4 v[12:15], v244, s[40:43], s61 offen offset:3456\n"
  "buffer_load_dwordx4 v[16:19], v245, s[40:43], s61 offen offset:0\n"
  "v_mfma_f32_16x16x32_bf16 a[76:79], v[236:239], v[120:123], a[76:79]\n"
  "buffer_load_dwordx4 v[20:23], v245, s[40:43], s61 offen offset:1152\n"
  "buffer_load_dwordx4 v[24:27], v245, s[40:43], s61 offen offset:2304\n"
  "buffer_load_dwordx4 v[28:31], v245, s[40:43], s61 offen offset:3456\n"
  "s_add_u32 m0, s72, 7168\n"
  "v_mfma_f32_16x16x32_bf16 a[96:99], v[224:227], v[124:127], a[96:99]\n"
  "buffer_load_dwordx4 v246, s[40:43], s64 offen lds\n"
  "v_mov_b32 v248, 0\n"
  "v_mov_b32 v249, 0\n"
  "v_mov_b32 v250, 0\n"
  "v_mfma_f32_16x16x32_bf16 a[100:103], v[228:231], v[124:127], a[100:103]\n"
  "v_mov_b32 v251, 0\n"
  "v_accvgpr_read_b32 v196, a224\n"
  "v_accvgpr_read_b32 v197, a228\n"
  "v_accvgpr_read_b32 v198, a232\n"
  "v_mfma_f32_16x16x32_bf16 a[104:107], v[232:235], v[124:127], a[104:107]\n"
  "v_accvgpr_read_b32 v199, a236\n"
  "v_accvgpr_read_b32 v200, a240\n"
  "v_accvgpr_read_b32 v201, a244\n"
  "v_accvgpr_read_b32 v202, a248\n"
  "v_mfma_f32_16x16x32_bf16 a[108:111], v[236:239], v[124:127], a[108:111]\n"
  "v_rcp_f32 v196, v196\n"
  "v_rcp_f32 v197, v197\n"
  "v_rcp_f32 v198, v198\n"
  "v_rcp_f32 v199, v199\n"
  "v_rcp_f32 v200, v200\n"
  "v_rcp_f32 v201, v201\n"
  "v_rcp_f32 v202, v202\n"
  "v_mfma_f32_32x32x16_bf16 a[224:239], v[248:251], v[248:251], 0\n"
  "v_mfma_f32_32x32x16_bf16 a[240:255], v[248:251], v[248:251], 0\n"
  "v_accvgpr_read_b32 v168, a0\n"
  "s_add_u32 s62, s62, 1\n"
  "s_xor_b32 s64, s62, s79\n"
  "s_and_b32 s65, s64, 1\n"
  "s_mul_i32 s65, s65, 147456\n"
  "s_bfe_u32 s61, s64, 0x20001\n"
  "s_mul_i32 s61, s61, 36864\n"
  "s_add_u32 s65, s61, s65\n"
  "s_add_u32 s61, s65, s66\n"
  "s_add_u32 s64, s65, s67\n"
  "v_accvgpr_read_b32 v169, a1\n"
  "buffer_load_dwordx4 v[32:35], v244, s[40:43], s61 offen offset:0\n"
  "v_accvgpr_read_b32 v170, a2\n"
  "v_accvgpr_read_b32 v171, a3\n"
  "v_accvgpr_read_b32 v172, a4\n"
  "v_accvgpr_read_b32 v173, a5\n"
  "v_accvgpr_read_b32 v174, a6\n"
  "v_accvgpr_read_b32 v175, a7\n"
  "v_accvgpr_read_b32 v176, a8\n"
  "v_accvgpr_read_b32 v177, a9\n"
  "v_accvgpr_read_b32 v178, a10\n"
  "v_accvgpr_read_b32 v179, a11\n"
  "v_accvgpr_read_b32 v180, a12\n"
  "v_accvgpr_read_b32 v181, a13\n"
  "v_accvgpr_read_b32 v182, a14\n"
  "v_accvgpr_read_b32 v183, a15\n"
  "v_accvgpr_read_b32 v184, a32\n"
  "v_accvgpr_read_b32 v185, a33\n"
  "v_accvgpr_read_b32 v186, a34\n"
  "v_accvgpr_read_b32 v187, a35\n"
  "v_accvgpr_read_b32 v188, a36\n"
  "v_accvgpr_read_b32 v189, a37\n"
  "v_accvgpr_read_b32 v190, a38\n"
  "v_accvgpr_read_b32 v191, a39\n"
  "v_accvgpr_read_b32 v192, a40\n"
  "v_accvgpr_read_b32 v193, a41\n"
  "v_accvgpr_read_b32 v194, a42\n"
  "v_accvgpr_read_b32 v195, a43\n"
  "v_accvgpr_read_b32 v156, a44\n"
  "v_accvgpr_read_b32 v157, a45\n"
  "buffer_load_dwordx4 v[36:39], v244, s[40:43], s61 offen offset:1152\n"
  "v_accvgpr_read_b32 v158, a46\n"
  "v_accvgpr_read_b32 v159, a47\n"
  "v_pk_mul_f32 v[168:169], v[168:169], v[196:197] op_sel:[0,0] op_sel_hi:[1,0]\n"
  "v_pk_mul_f32 v[170:171], v[170:171], v[196:197] op_sel:[0,0] op_sel_hi:[1,0]\n"
  "v_pk_mul_f32 v[172:173], v[172:173], v[196:197] op_sel:[0,0] op_sel_hi:[1,0]\n"
  "v_pk_mul_f32 v[174:175], v[174:175], v[196:197] op_sel:[0,0] op_sel_hi:[1,0]\n"
  "v_pk_mul_f32 v[176:177], v[176:177], v[196:197] op_sel:[0,0] op_sel_hi:[1,0]\n"
  "v_pk_mul_f32 v[178:179], v[178:179], v[196:197] op_sel:[0,0] op_sel_hi:[1,0]\n"
  "v_pk_mul_f32 v[180:181], v[180:181], v[196:197] op_sel:[0,0] op_sel_hi:[1,0]\n"
  "v_pk_mul_f32 v[182:183], v[182:183], v[196:197] op_sel:[0,0] op_sel_hi:[1,0]\n"
  "v_pk_mul_f32 v[184:185], v[184:185], v[196:197] op_sel:[0,1] op_sel_hi:[1,1]\n"
  "v_pk_mul_f32 v[186:187], v[186:187], v[196:197] op_sel:[0,1] op_sel_hi:[1,1]\n"
  "v_pk_mul_f32 v[188:189], v[188:189], v[196:197] op_sel:[0,1] op_sel_hi:[1,1]\n"
  "v_pk_mul_f32 v[190:191], v[190:191], v[196:197] op_sel:[0,1] op_sel_hi:[1,1]\n"
  "v_pk_mul_f32 v[192:193], v[192:193], v[196:197] op_sel:[0,1] op_sel_hi:[1,1]\n"
  "v_pk_mul_f32 v[194:195], v[194:195], v[196:197] op_sel:[0,1] op_sel_hi:[1,1]\n"
  "v_pk_mul_f32 v[156:157], v[156:157], v[196:197] op_sel:[0,1] op_sel_hi:[1,1]\n"
  "v_pk_mul_f32 v[158:159], v[158:159], v[196:197] op_sel:[0,1] op_sel_hi:[1,1]\n"
  "v_cvt_pk_bf16_f32 v208, v168, v169\n"
  "v_cvt_pk_bf16_f32 v209, v170, v171\n"
  "v_cvt_pk_bf16_f32 v210, v172, v173\n"
  "v_cvt_pk_bf16_f32 v211, v174, v175\n"
  "v_mfma_f32_16x16x32_bf16 a[128:131], v[224:227], v[128:131], a[128:131]\n"
  "s_waitcnt vmcnt(11)\n"
  "ds_write_b128 v241, v[208:211] offset:0\n"
  "v_mfma_f32_16x16x32_bf16 a[132:135], v[228:231], v[128:131], a[132:135]\n"
  "v_mfma_f32_32x32x16_bf16 a[0:15], v[248:251], v[248:251], 0\n"
  "v_cvt_pk_bf16_f32 v212, v176, v177\n"
  "buffer_load_dwordx4 v[40:43], v244, s[40:43], s61 offen offset:2304\n"
  "v_cvt_pk_bf16_f32 v213, v178, v179\n"
  "v_cvt_pk_bf16_f32 v214, v180, v181\n"
  "v_cvt_pk_bf16_f32 v215, v182, v183\n"
  "v_mfma_f32_16x16x32_bf16 a[136:139], v[232:235], v[128:131], a[136:139]\n"
  "ds_write_b128 v241, v[212:215] offset:33024\n"
  "v_mfma_f32_16x16x32_bf16 a[140:143], v[236:239], v[128:131], a[140:143]\n"
  "v_cvt_pk_bf16_f32 v216, v184, v185\n"
  "v_cvt_pk_bf16_f32 v217, v186, v187\n"
  "v_cvt_pk_bf16_f32 v218, v188, v189\n"
  "v_cvt_pk_bf16_f32 v219, v190, v191\n"
  "v_mfma_f32_16x16x32_bf16 a[160:163], v[224:227], v[132:135], a[160:163]\n"
  "ds_write_b128 v241, v[216:219] offset:1024\n"
  "v_mfma_f32_16x16x32_bf16 a[164:167], v[228:231], v[132:135], a[164:167]\n"
  "v_mfma_f32_32x32x16_bf16 a[32:47], v[248:251], v[248:251], 0\n"
  "v_cvt_pk_bf16_f32 v220, v192, v193\n"
  "v_cvt_pk_bf16_f32 v221, v194, v195\n"
  "v_cvt_pk_bf16_f32 v222, v156, v157\n"
  "v_cvt_pk_bf16_f32 v223, v158, v159\n"
  "v_mfma_f32_16x16x32_bf16 a[168:171], v[232:235], v[132:135], a[168:171]\n"
  "ds_write_b128 v241, v[220:223] offset:34048\n"
  "v_mfma_f32_16x16x32_bf16 a[172:175], v[236:239], v[132:135], a[172:175]\n"
  "v_accvgpr_read_b32 v168, a64\n"
  "v_accvgpr_read_b32 v169, a65\n"
  "v_accvgpr_read_b32 v170, a66\n"
  "v_accvgpr_read_b32 v171, a67\n"
  "v_accvgpr_read_b32 v172, a68\n"
  "v_accvgpr_read_b32 v173, a69\n"
  "v_accvgpr_read_b32 v174, a70\n"
  "buffer_load_dwordx4 v[44:47], v244, s[40:43], s61 offen offset:3456\n"
  "v_accvgpr_read_b32 v175, a71\n"
  "v_accvgpr_read_b32 v176, a72\n"
  "v_accvgpr_read_b32 v177, a73\n"
  "v_accvgpr_read_b32 v178, a74\n"
  "v_accvgpr_read_b32 v179, a75\n"
  "v_accvgpr_read_b32 v180, a76\n"
  "v_accvgpr_read_b32 v181, a77\n"
  "v_accvgpr_read_b32 v182, a78\n"
  "v_accvgpr_read_b32 v183, a79\n"
  "v_accvgpr_read_b32 v184, a96\n"
  "v_accvgpr_read_b32 v185, a97\n"
  "v_accvgpr_read_b32 v186, a98\n"
  "v_accvgpr_read_b32 v187, a99\n"
  "v_accvgpr_read_b32 v188, a100\n"
  "v_accvgpr_read_b32 v189, a101\n"
  "v_accvgpr_read_b32 v190, a102\n"
  "v_accvgpr_read_b32 v191, a103\n"
  "v_accvgpr_read_b32 v192, a104\n"
  "v_accvgpr_read_b32 v193, a105\n"
  "v_accvgpr_read_b32 v194, a106\n"
  "v_accvgpr_read_b32 v195, a107\n"
  "v_accvgpr_read_b32 v156, a108\n"
  "v_accvgpr_read_b32 v157, a109\n"
  "v_accvgpr_read_b32 v158, a110\n"
  "v_accvgpr_read_b32 v159, a111\n"
  "v_pk_mul_f32 v[168:169], v[168:169], v[198:199] op_sel:[0,0] op_sel_hi:[1,0]\n"
  "v_pk_mul_f32 v[170:171], v[170:171], v[198:199] op_sel:[0,0] op_sel_hi:[1,0]\n"
  "v_pk_mul_f32 v[172:173], v[172:173], v[198:199] op_sel:[0,0] op_sel_hi:[1,0]\n"
  "buffer_load_dwordx4 v[48:51], v245, s[40:43], s61 offen offset:0\n"
  "v_pk_mul_f32 v[174:175], v[174:175], v[198:199] op_sel:[0,0] op_sel_hi:[1,0]\n"
  "v_pk_mul_f32 v[176:177], v[176:177], v[198:199] op_sel:[0,0] op_sel_hi:[1,0]\n"
  "v_pk_mul_f32 v[178:179], v[178:179], v[198:199] op_sel:[0,0] op_sel_hi:[1,0]\n"
  "v_pk_mul_f32 v[180:181], v[180:181], v[198:199] op_sel:[0,0] op_sel_hi:[1,0]\n"
  "v_pk_mul_f32 v[182:183], v[182:183], v[198:199] op_sel:[0,0] op_sel_hi:[1,0]\n"
  "v_pk_mul_f32 v[184:185], v[184:185], v[198:199] op_sel:[0,1] op_sel_hi:[1,1]\n"
  "v_pk_mul_f32 v[186:187], v[186:187], v[198:199] op_sel:[0,1] op_sel_hi:[1,1]\n"
  "v_pk_mul_f32 v[188:189], v[188:189], v[198:199] op_sel:[0,1] op_sel_hi:[1,1]\n"
  "v_pk_mul_f32 v[190:191], v[190:191], v[198:199] op_sel:[0,1] op_sel_hi:[1,1]\n"
  "v_pk_mul_f32 v[192:193], v[192:193], v[198:199] op_sel:[0,1] op_sel_hi:[1,1]\n"
  "v_pk_mul_f32 v[194:195], v[194:195], v[198:199] op_sel:[0,1] op_sel_hi:[1,1]\n"
  "v_pk_mul_f32 v[156:157], v[156:157], v[198:199] op_sel:[0,1] op_sel_hi:[1,1]\n"
  "v_pk_mul_f32 v[158:159], v[158:159], v[198:199] op_sel:[0,1] op_sel_hi:[1,1]\n"
  "v_cvt_pk_bf16_f32 v208, v168, v169\n"
  "v_cvt_pk_bf16_f32 v209, v170, v171\n"
  "v_cvt_pk_bf16_f32 v210, v172, v173\n"
  "v_cvt_pk_bf16_f32 v211, v174, v175\n"
  "v_mfma_f32_16x16x32_bf16 a[192:195], v[224:227], v[136:139], a[192:195]\n"
  "ds_write_b128 v241, v[208:211] offset:2048\n"
  "v_mfma_f32_16x16x32_bf16 a[196:199], v[228:231], v[136:139], a[196:199]\n"
  "v_mfma_f32_32x32x16_bf16 a[64:79], v[248:251], v[248:251], 0\n"
  "v_cvt_pk_bf16_f32 v212, v176, v177\n"
  "v_cvt_pk_bf16_f32 v213, v178, v179\n"
  "v_cvt_pk_bf16_f32 v214, v180, v181\n"
  "v_cvt_pk_bf16_f32 v215, v182, v183\n"
  "v_mfma_f32_16x16x32_bf16 a[200:203], v[232:235], v[136:139], a[200:203]\n"
  "ds_write_b128 v241, v[212:215] offset:35072\n"
  "v_mfma_f32_16x16x32_bf16 a[204:207], v[236:239], v[136:139], a[204:207]\n"
  "buffer_load_dwordx4 v[52:55], v245, s[40:43], s61 offen offset:1152\n"
  "v_cvt_pk_bf16_f32 v216, v184, v185\n"
  "v_cvt_pk_bf16_f32 v217, v186, v187\n"
  "v_cvt_pk_bf16_f32 v218, v188, v189\n"
  "v_cvt_pk_bf16_f32 v219, v190, v191\n"
  "v_mfma_f32_16x16x32_bf16 a[16:19], v[140:143], v[112:115], a[16:19]\n"
  "ds_write_b128 v241, v[216:219] offset:3072\n"
  "v_mfma_f32_16x16x32_bf16 a[20:23], v[144:147], v[112:115], a[20:23]\n"
  "v_mfma_f32_32x32x16_bf16 a[96:111], v[248:251], v[248:251], 0\n"
  "v_cvt_pk_bf16_f32 v220, v192, v193\n"
  "v_cvt_pk_bf16_f32 v221, v194, v195\n"
  "v_cvt_pk_bf16_f32 v222, v156, v157\n"
  "v_cvt_pk_bf16_f32 v223, v158, v159\n"
  "v_mfma_f32_16x16x32_bf16 a[24:27], v[148:151], v[112:115], a[24:27]\n"
  "ds_write_b128 v241, v[220:223] offset:36096\n"
  "v_mfma_f32_16x16x32_bf16 a[28:31], v[152:155], v[112:115], a[28:31]\n"
  "v_accvgpr_read_b32 v168, a128\n"
  "v_accvgpr_read_b32 v169, a129\n"
  "v_accvgpr_read_b32 v170, a130\n"
  "v_accvgpr_read_b32 v171, a131\n"
  "v_accvgpr_read_b32 v172, a132\n"
  "v_accvgpr_read_b32 v173, a133\n"
  "v_accvgpr_read_b32 v174, a134\n"
  "v_accvgpr_read_b32 v175, a135\n"
  "v_accvgpr_read_b32 v176, a136\n"
  "v_accvgpr_read_b32 v177, a137\n"
  "v_accvgpr_read_b32 v178, a138\n"
  "v_accvgpr_read_b32 v179, a139\n"
  "v_accvgpr_read_b32 v180, a140\n"
  "buffer_load_dwordx4 v[56:59], v245, s[40:43], s61 offen offset:2304\n"
  "v_accvgpr_read_b32 v181, a141\n"
  "v_accvgpr_read_b32 v182, a142\n"
  "v_accvgpr_read_b32 v183, a143\n"
  "v_accvgpr_read_b32 v184, a160\n"
  "v_accvgpr_read_b32 v185, a161\n"
  "v_accvgpr_read_b32 v186, a162\n"
  "v_accvgpr_read_b32 v187, a163\n"
  "v_accvgpr_read_b32 v188, a164\n"
  "v_accvgpr_read_b32 v189, a165\n"
  "v_accvgpr_read_b32 v190, a166\n"
  "v_accvgpr_read_b32 v191, a167\n"
  "v_accvgpr_read_b32 v192, a168\n"
  "v_accvgpr_read_b32 v193, a169\n"
  "v_accvgpr_read_b32 v194, a170\n"
  "v_accvgpr_read_b32 v195, a171\n"
  "v_accvgpr_read_b32 v156, a172\n"
  "v_accvgpr_read_b32 v157, a173\n"
  "v_accvgpr_read_b32 v158, a174\n"
  "v_accvgpr_read_b32 v159, a175\n"
  "v_pk_mul_f32 v[168:169], v[168:169], v[200:201] op_sel:[0,0] op_sel_hi:[1,0]\n"
  "v_pk_mul_f32 v[170:171], v[170:171], v[200:201] op_sel:[0,0] op_sel_hi:[1,0]\n"
  "v_pk_mul_f32 v[172:173], v[172:173], v[200:201] op_sel:[0,0] op_sel_hi:[1,0]\n"
  "v_pk_mul_f32 v[174:175], v[174:175], v[200:201] op_sel:[0,0] op_sel_hi:[1,0]\n"
  "v_pk_mul_f32 v[176:177], v[176:177], v[200:201] op_sel:[0,0] op_sel_hi:[1,0]\n"
  "v_pk_mul_f32 v[178:179], v[178:179], v[200:201] op_sel:[0,0] op_sel_hi:[1,0]\n"
  "v_pk_mul_f32 v[180:181], v[180:181], v[200:201] op_sel:[0,0] op_sel_hi:[1,0]\n"
  "v_pk_mul_f32 v[182:183], v[182:183], v[200:201] op_sel:[0,0] op_sel_hi:[1,0]\n"
  "v_pk_mul_f32 v[184:185], v[184:185], v[200:201] op_sel:[0,1] op_sel_hi:[1,1]\n"
  "buffer_load_dwordx4 v[60:63], v245, s[40:43], s61 offen offset:3456\n"
  "v_pk_mul_f32 v[186:187], v[186:187], v[200:201] op_sel:[0,1] op_sel_hi:[1,1]\n"
  "v_pk_mul_f32 v[188:189], v[188:189], v[200:201] op_sel:[0,1] op_sel_hi:[1,1]\n"
  "v_pk_mul_f32 v[190:191], v[190:191], v[200:201] op_sel:[0,1] op_sel_hi:[1,1]\n"
  "v_pk_mul_f32 v[192:193], v[192:193], v[200:201] op_sel:[0,1] op_sel_hi:[1,1]\n"
  "v_pk_mul_f32 v[194:195], v[194:195], v[200:201] op_sel:[0,1] op_sel_hi:[1,1]\n"
  "v_pk_mul_f32 v[156:157], v[156:157], v[200:201] op_sel:[0,1] op_sel_hi:[1,1]\n"
  "v_pk_mul_f32 v[158:159], v[158:159], v[200:201] op_sel:[0,1] op_sel_hi:[1,1]\n"
  "v_cvt_pk_bf16_f32 v208, v168, v169\n"
  "v_cvt_pk_bf16_f32 v209, v170, v171\n"
  "v_cvt_pk_bf16_f32 v210, v172, v173\n"
  "v_cvt_pk_bf16_f32 v211, v174, v175\n"
  "v_mfma_f32_16x16x32_bf16 a[48:51], v[140:143], v[116:119], a[48:51]\n"
  "ds_write_b128 v241, v[208:211] offset:4096\n"
  "v_mfma_f32_16x16x32_bf16 a[52:55], v[144:147], v[116:119], a[52:55]\n"
  "v_mfma_f32_32x32x16_bf16 a[128:143], v[248:251], v[248:251], 0\n"
  "v_cvt_pk_bf16_f32 v212, v176, v177\n"
  "v_cvt_pk_bf16_f32 v213, v178, v179\n"
  "v_cvt_pk_bf16_f32 v214, v180, v181\n"
  "v_cvt_pk_bf16_f32 v215, v182, v183\n"
  "v_mfma_f32_16x16x32_bf16 a[56:59], v[148:151], v[116:119], a[56:59]\n"
  "ds_write_b128 v241, v[212:215] offset:37120\n"
  "v_mfma_f32_16x16x32_bf16 a[60:63], v[152:155], v[116:119], a[60:63]\n"
  "v_cvt_pk_bf16_f32 v216, v184, v185\n"
  "v_cvt_pk_bf16_f32 v217, v186, v187\n"
  "v_cvt_pk_bf16_f32 v218, v188, v189\n"
  "v_cvt_pk_bf16_f32 v219, v190, v191\n"
  "v_mfma_f32_16x16x32_bf16 a[80:83], v[140:143], v[120:123], a[80:83]\n"
  "s_add_u32 m0, s72, 40192\n"
  "ds_write_b128 v241, v[216:219] offset:5120\n"
  "buffer_load_dwordx4 v246, s[40:43], s64 offen lds\n"
  "v_mfma_f32_16x16x32_bf16 a[84:87], v[144:147], v[120:123], a[84:87]\n"
  "v_mfma_f32_32x32x16_bf16 a[160:175], v[248:251], v[248:251], 0\n"
  "v_cvt_pk_bf16_f32 v220, v192, v193\n"
  "v_cvt_pk_bf16_f32 v221, v194, v195\n"
  "v_cvt_pk_bf16_f32 v222, v156, v157\n"
  "v_cvt_pk_bf16_f32 v223, v158, v159\n"
  "v_mfma_f32_16x16x32_bf16 a[88:91], v[148:151], v[120:123], a[88:91]\n"
  "ds_write_b128 v241, v[220:223] offset:38144\n"
  "v_mfma_f32_16x16x32_bf16 a[92:95], v[152:155], v[120:123], a[92:95]\n"
  "v_accvgpr_read_b32 v168, a192\n"
  "v_accvgpr_read_b32 v169, a193\n"
  "v_accvgpr_read_b32 v170, a194\n"
  "v_accvgpr_read_b32 v171, a195\n"
  "v_accvgpr_read_b32 v172, a196\n"
  "v_accvgpr_read_b32 v173, a197\n"
  "v_accvgpr_read_b32 v174, a198\n"
  "v_accvgpr_read_b32 v175, a199\n"
  "v_accvgpr_read_b32 v176, a200\n"
  "v_accvgpr_read_b32 v177, a201\n"
  "v_accvgpr_read_b32 v178, a202\n"
  "v_accvgpr_read_b32 v179, a203\n"
  "v_accvgpr_read_b32 v180, a204\n"
  "v_accvgpr_read_b32 v181, a205\n"
  "v_accvgpr_read_b32 v182, a206\n"
  "v_accvgpr_read_b32 v183, a207\n"
  "v_accvgpr_read_b32 v184, a16\n"
  "v_accvgpr_read_b32 v185, a17\n"
  "s_add_u32 s62, s62, 1\n"
  "s_xor_b32 s64, s62, s79\n"
  "s_and_b32 s65, s64, 1\n"
  "s_mul_i32 s65, s65, 147456\n"
  "s_bfe_u32 s61, s64, 0x20001\n"
  "s_mul_i32 s61, s61, 36864\n"
  "s_add_u32 s65, s61, s65\n"
  "s_add_u32 s61, s65, s66\n"
  "s_add_u32 s64, s65, s67\n"
  "v_accvgpr_read_b32 v186, a18\n"
  "buffer_load_dwordx4 v[64:67], v244, s[40:43], s61 offen offset:0\n"
  "v_accvgpr_read_b32 v187, a19\n"
  "v_accvgpr_read_b32 v188, a20\n"
  "v_accvgpr_read_b32 v189, a21\n"
  "v_accvgpr_read_b32 v190, a22\n"
  "v_accvgpr_read_b32 v191, a23\n"
  "v_accvgpr_read_b32 v192, a24\n"
  "v_accvgpr_read_b32 v193, a25\n"
  "v_accvgpr_read_b32 v194, a26\n"
  "v_accvgpr_read_b32 v195, a27\n"
  "v_accvgpr_read_b32 v156, a28\n"
  "v_accvgpr_read_b32 v157, a29\n"
  "v_accvgpr_read_b32 v158, a30\n"
  "v_accvgpr_read_b32 v159, a31\n"
  "v_pk_mul_f32 v[168:169], v[168:169], v[202:203] op_sel:[0,0] op_sel_hi:[1,0]\n"
  "v_pk_mul_f32 v[170:171], v[170:171], v[202:203] op_sel:[0,0] op_sel_hi:[1,0]\n"
  "v_pk_mul_f32 v[172:173], v[172:173], v[202:203] op_sel:[0,0] op_sel_hi:[1,0]\n"
  "v_pk_mul_f32 v[174:175], v[174:175], v[202:203] op_sel:[0,0] op_sel_hi:[1,0]\n"
  "v_pk_mul_f32 v[176:177], v[176:177], v[202:203] op_sel:[0,0] op_sel_hi:[1,0]\n"
  "v_pk_mul_f32 v[178:179], v[178:179], v[202:203] op_sel:[0,0] op_sel_hi:[1,0]\n"
  "v_pk_mul_f32 v[180:181], v[180:181], v[202:203] op_sel:[0,0] op_sel_hi:[1,0]\n"
  "v_pk_mul_f32 v[182:183], v[182:183], v[202:203] op_sel:[0,0] op_sel_hi:[1,0]\n"
  "v_pk_mul_f32 v[184:185], v[184:185], v[196:197] op_sel:[0,0] op_sel_hi:[1,0]\n"
  "v_pk_mul_f32 v[186:187], v[186:187], v[196:197] op_sel:[0,0] op_sel_hi:[1,0]\n"
  "v_pk_mul_f32 v[188:189], v[188:189], v[196:197] op_sel:[0,0] op_sel_hi:[1,0]\n"
  "v_pk_mul_f32 v[190:191], v[190:191], v[196:197] op_sel:[0,0] op_sel_hi:[1,0]\n"
  "v_pk_mul_f32 v[192:193], v[192:193], v[196:197] op_sel:[0,0] op_sel_hi:[1,0]\n"
  "v_pk_mul_f32 v[194:195], v[194:195], v[196:197] op_sel:[0,0] op_sel_hi:[1,0]\n"
  "v_pk_mul_f32 v[156:157], v[156:157], v[196:197] op_sel:[0,0] op_sel_hi:[1,0]\n"
  "buffer_load_dwordx4 v[68:71], v244, s[40:43], s61 offen offset:1152\n"
  "v_pk_mul_f32 v[158:159], v[158:159], v[196:197] op_sel:[0,0] op_sel_hi:[1,0]\n"
  "v_cvt_pk_bf16_f32 v208, v168, v169\n"
  "v_cvt_pk_bf16_f32 v209, v170, v171\n"
  "v_cvt_pk_bf16_f32 v210, v172, v173\n"
  "v_cvt_pk_bf16_f32 v211, v174, v175\n"
  "v_mfma_f32_16x16x32_bf16 a[112:115], v[140:143], v[124:127], a[112:115]\n"
  "ds_write_b128 v241, v[208:211] offset:6144\n"
  "v_mfma_f32_16x16x32_bf16 a[116:119], v[144:147], v[124:127], a[116:119]\n"
  "v_mfma_f32_32x32x16_bf16 a[192:207], v[248:251], v[248:251], 0\n"
  "v_cvt_pk_bf16_f32 v212, v176, v177\n"
  "v_cvt_pk_bf16_f32 v213, v178, v179\n"
  "v_cvt_pk_bf16_f32 v214, v180, v181\n"
  "v_cvt_pk_bf16_f32 v215, v182, v183\n"
  "v_mfma_f32_16x16x32_bf16 a[120:123], v[148:151], v[124:127], a[120:123]\n"
  "ds_write_b128 v241, v[212:215] offset:39168\n"
  "v_mfma_f32_16x16x32_bf16 a[124:127], v[152:155], v[124:127], a[124:127]\n"
  "v_mov_b32 v204, s72\n"
  "v_mov_b32 v205, 1\n"
  "ds_write_b32 v204, v205 offset:8224\n"
  "v_cvt_pk_bf16_f32 v216, v184, v185\n"
  "v_cvt_pk_bf16_f32 v217, v186, v187\n"
  "v_cvt_pk_bf16_f32 v218, v188, v189\n"
  "v_cvt_pk_bf16_f32 v219, v190, v191\n"
  "v_mfma_f32_16x16x32_bf16 a[144:147], v[140:143], v[128:131], a[144:147]\n"
  "ds_write_b128 v242, v[216:219] offset:0\n"
  "v_mfma_f32_16x16x32_bf16 a[148:151], v[144:147], v[128:131], a[148:151]\n"
  "v_mfma_f32_32x32x16_bf16 a[16:31], v[248:251], v[248:251], 0\n"
  "v_cvt_pk_bf16_f32 v220, v192, v193\n"
  "buffer_load_dwordx4 v[72:75], v244, s[40:43], s61 offen offset:2304\n"
  "v_cvt_pk_bf16_f32 v221, v194, v195\n"
  "v_cvt_pk_bf16_f32 v222, v156, v157\n"
  "v_cvt_pk_bf16_f32 v223, v158, v159\n"
  "v_mfma_f32_16x16x32_bf16 a[152:155], v[148:151], v[128:131], a[152:155]\n"
  "ds_write_b128 v242, v[220:223] offset:33024\n"
  "v_mfma_f32_16x16x32_bf16 a[156:159], v[152:155], v[128:131], a[156:159]\n"
  "v_accvgpr_read_b32 v168, a48\n"
  "v_accvgpr_read_b32 v169, a49\n"
  "v_accvgpr_read_b32 v170, a50\n"
  "v_accvgpr_read_b32 v171, a51\n"
  "v_accvgpr_read_b32 v172, a52\n"
  "v_accvgpr_read_b32 v173, a53\n"
  "v_accvgpr_read_b32 v174, a54\n"
  "v_accvgpr_read_b32 v175, a55\n"
  "v_accvgpr_read_b32 v176, a56\n"
  "v_accvgpr_read_b32 v177, a57\n"
  "v_accvgpr_read_b32 v178, a58\n"
  "v_accvgpr_read_b32 v179, a59\n"
  "v_accvgpr_read_b32 v180, a60\n"
  "v_accvgpr_read_b32 v181, a61\n"
  "v_accvgpr_read_b32 v182, a62\n"
  "v_accvgpr_read_b32 v183, a63\n"
  "v_accvgpr_read_b32 v184, a80\n"
  "v_accvgpr_read_b32 v185, a81\n"
  "v_accvgpr_read_b32 v186, a82\n"
  "v_accvgpr_read_b32 v187, a83\n"
  "v_accvgpr_read_b32 v188, a84\n"
  "v_accvgpr_read_b32 v189, a85\n"
  "buffer_load_dwordx4 v[76:79], v244, s[40:43], s61 offen offset:3456\n"
  "v_accvgpr_read_b32 v190, a86\n"
  "v_accvgpr_read_b32 v191, a87\n"
  "v_accvgpr_read_b32 v192, a88\n"
  "v_accvgpr_read_b32 v193, a89\n"
  "v_accvgpr_read_b32 v194, a90\n"
  "v_accvgpr_read_b32 v195, a91\n"
  "v_accvgpr_read_b32 v156, a92\n"
  "v_accvgpr_read_b32 v157, a93\n"
  "v_accvgpr_read_b32 v158, a94\n"
  "v_accvgpr_read_b32 v159, a95\n"
  "v_pk_mul_f32 v[168:169], v[168:169], v[196:197] op_sel:[0,1] op_sel_hi:[1,1]\n"
  "v_pk_mul_f32 v[170:171], v[170:171], v[196:197] op_sel:[0,1] op_sel_hi:[1,1]\n"
  "v_pk_mul_f32 v[172:173], v[172:173], v[196:197] op_sel:[0,1] op_sel_hi:[1,1]\n"
  "v_pk_mul_f32 v[174:175], v[174:175], v[196:197] op_sel:[0,1] op_sel_hi:[1,1]\n"
  "v_pk_mul_f32 v[176:177], v[176:177], v[196:197] op_sel:[0,1] op_sel_hi:[1,1]\n"
  "v_pk_mul_f32 v[178:179], v[178:179], v[196:197] op_sel:[0,1] op_sel_hi:[1,1]\n"
  "v_pk_mul_f32 v[180:181], v[180:181], v[196:197] op_sel:[0,1] op_sel_hi:[1,1]\n"
  "v_pk_mul_f32 v[182:183], v[182:183], v[196:197] op_sel:[0,1] op_sel_hi:[1,1]\n"
  "v_pk_mul_f32 v[184:185], v[184:185], v[198:199] op_sel:[0,0] op_sel_hi:[1,0]\n"
  "v_pk_mul_f32 v[186:187], v[186:187], v[198:199] op_sel:[0,0] op_sel_hi:[1,0]\n"
  "v_pk_mul_f32 v[188:189], v[188:189], v[198:199] op_sel:[0,0] op_sel_hi:[1,0]\n"
  "v_pk_mul_f32 v[190:191], v[190:191], v[198:199] op_sel:[0,0] op_sel_hi:[1,0]\n"
  "v_pk_mul_f32 v[192:193], v[192:193], v[198:199] op_sel:[0,0] op_sel_hi:[1,0]\n"
  "v_pk_mul_f32 v[194:195], v[194:195], v[198:199] op_sel:[0,0] op_sel_hi:[1,0]\n"
  "v_pk_mul_f32 v[156:157], v[156:157], v[198:199] op_sel:[0,0] op_sel_hi:[1,0]\n"
  "v_pk_mul_f32 v[158:159], v[158:159], v[198:199] op_sel:[0,0] op_sel_hi:[1,0]\n"
  "v_cvt_pk_bf16_f32 v208, v168, v169\n"
  "v_cvt_pk_bf16_f32 v209, v170, v171\n"
  "buffer_load_dwordx4 v[80:83], v245, s[40:43], s61 offen offset:0\n"
  "v_cvt_pk_bf16_f32 v210, v172, v173\n"
  "v_cvt_pk_bf16_f32 v211, v174, v175\n"
  "v_mfma_f32_16x16x32_bf16 a[176:179], v[140:143], v[132:135], a[176:179]\n"
  "ds_write_b128 v242, v[208:211] offset:1024\n"
  "v_mfma_f32_16x16x32_bf16 a[180:183], v[144:147], v[132:135], a[180:183]\n"
  "v_mfma_f32_32x32x16_bf16 a[48:63], v[248:251], v[248:251], 0\n"
  "v_cvt_pk_bf16_f32 v212, v176, v177\n"
  "v_cvt_pk_bf16_f32 v213, v178, v179\n"
  "v_cvt_pk_bf16_f32 v214, v180, v181\n"
  "v_cvt_pk_bf16_f32 v215, v182, v183\n"
  "v_mfma_f32_16x16x32_bf16 a[184:187], v[148:151], v[132:135], a[184:187]\n"
  "ds_write_b128 v242, v[212:215] offset:34048\n"
  "v_mfma_f32_16x16x32_bf16 a[188:191], v[152:155], v[132:135], a[188:191]\n"
  "v_cvt_pk_bf16_f32 v216, v184, v185\n"
  "v_cvt_pk_bf16_f32 v217, v186, v187\n"
  "v_cvt_pk_bf16_f32 v218, v188, v189\n"
  "v_cvt_pk_bf16_f32 v219, v190, v191\n"
  "v_mfma_f32_16x16x32_bf16 a[208:211], v[140:143], v[136:139], a[208:211]\n"
  "ds_write_b128 v242, v[216:219] offset:2048\n"
  "v_mfma_f32_16x16x32_bf16 a[212:215], v[144:147], v[136:139], a[212:215]\n"
  "v_mfma_f32_32x32x16_bf16 a[80:95], v[248:251], v[248:251], 0\n"
  "v_cvt_pk_bf16_f32 v220, v192, v193\n"
  "v_cvt_pk_bf16_f32 v221, v194, v195\n"
  "v_cvt_pk_bf16_f32 v222, v156, v157\n"
  "v_cvt_pk_bf16_f32 v223, v158, v159\n"
  "v_mfma_f32_16x16x32_bf16 a[216:219], v[148:151], v[136:139], a[216:219]\n"
  "ds_write_b128 v242, v[220:223] offset:35072\n"
  "v_mfma_f32_16x16x32_bf16 a[220:223], v[152:155], v[136:139], a[220:223]\n"
  "buffer_load_dwordx4 v[84:87], v245, s[40:43], s61 offen offset:1152\n"
  "v_accvgpr_read_b32 v168, a112\n"
  "v_accvgpr_read_b32 v169, a113\n"
  "v_accvgpr_read_b32 v170, a114\n"
  "v_accvgpr_read_b32 v171, a115\n"
  "v_accvgpr_read_b32 v172, a116\n"
  "v_accvgpr_read_b32 v173, a117\n"
  "v_accvgpr_read_b32 v174, a118\n"
  "v_accvgpr_read_b32 v175, a119\n"
  "v_accvgpr_read_b32 v176, a120\n"
  "v_accvgpr_read_b32 v177, a121\n"
  "v_accvgpr_read_b32 v178, a122\n"
  "v_accvgpr_read_b32 v179, a123\n"
  "v_accvgpr_read_b32 v180, a124\n"
  "v_accvgpr_read_b32 v181, a125\n"
  "v_accvgpr_read_b32 v182, a126\n"
  "v_accvgpr_read_b32 v183, a127\n"
  "v_accvgpr_read_b32 v184, a144\n"
  "v_accvgpr_read_b32 v185, a145\n"
  "v_accvgpr_read_b32 v186, a146\n"
  "v_accvgpr_read_b32 v187, a147\n"
  "v_accvgpr_read_b32 v188, a148\n"
  "v_accvgpr_read_b32 v189, a149\n"
  "v_accvgpr_read_b32 v190, a150\n"
  "v_accvgpr_read_b32 v191, a151\n"
  "v_accvgpr_read_b32 v192, a152\n"
  "v_accvgpr_read_b32 v193, a153\n"
  "v_accvgpr_read_b32 v194, a154\n"
  "v_accvgpr_read_b32 v195, a155\n"
  "buffer_load_dwordx4 v[88:91], v245, s[40:43], s61 offen offset:2304\n"
  "v_accvgpr_read_b32 v156, a156\n"
  "v_accvgpr_read_b32 v157, a157\n"
  "v_accvgpr_read_b32 v158, a158\n"
  "v_accvgpr_read_b32 v159, a159\n"
  "v_pk_mul_f32 v[168:169], v[168:169], v[198:199] op_sel:[0,1] op_sel_hi:[1,1]\n"
  "v_pk_mul_f32 v[170:171], v[170:171], v[198:199] op_sel:[0,1] op_sel_hi:[1,1]\n"
  "v_pk_mul_f32 v[172:173], v[172:173], v[198:199] op_sel:[0,1] op_sel_hi:[1,1]\n"
  "v_pk_mul_f32 v[174:175], v[174:175], v[198:199] op_sel:[0,1] op_sel_hi:[1,1]\n"
  "v_pk_mul_f32 v[176:177], v[176:177], v[198:199] op_sel:[0,1] op_sel_hi:[1,1]\n"
  "v_pk_mul_f32 v[178:179], v[178:179], v[198:199] op_sel:[0,1] op_sel_hi:[1,1]\n"
  "v_pk_mul_f32 v[180:181], v[180:181], v[198:199] op_sel:[0,1] op_sel_hi:[1,1]\n"
  "v_pk_mul_f32 v[182:183], v[182:183], v[198:199] op_sel:[0,1] op_sel_hi:[1,1]\n"
  "v_pk_mul_f32 v[184:185], v[184:185], v[200:201] op_sel:[0,0] op_sel_hi:[1,0]\n"
  "v_pk_mul_f32 v[186:187], v[186:187], v[200:201] op_sel:[0,0] op_sel_hi:[1,0]\n"
  "v_pk_mul_f32 v[188:189], v[188:189], v[200:201] op_sel:[0,0] op_sel_hi:[1,0]\n"
  "v_pk_mul_f32 v[190:191], v[190:191], v[200:201] op_sel:[0,0] op_sel_hi:[1,0]\n"
  "v_pk_mul_f32 v[192:193], v[192:193], v[200:201] op_sel:[0,0] op_sel_hi:[1,0]\n"
  "v_pk_mul_f32 v[194:195], v[194:195], v[200:201] op_sel:[0,0] op_sel_hi:[1,0]\n"
  "v_pk_mul_f32 v[156:157], v[156:157], v[200:201] op_sel:[0,0] op_sel_hi:[1,0]\n"
  "v_pk_mul_f32 v[158:159], v[158:159], v[200:201] op_sel:[0,0] op_sel_hi:[1,0]\n"
  "v_cvt_pk_bf16_f32 v208, v168, v169\n"
  "v_cvt_pk_bf16_f32 v209, v170, v171\n"
  "v_cvt_pk_bf16_f32 v210, v172, v173\n"
  "v_cvt_pk_bf16_f32 v211, v174, v175\n"
  "ds_write_b128 v242, v[208:211] offset:3072\n"
  "v_mfma_f32_32x32x16_bf16 a[112:127], v[248:251], v[248:251], 0\n"
  "v_cvt_pk_bf16_f32 v212, v176, v177\n"
  "v_cvt_pk_bf16_f32 v213, v178, v179\n"
  "buffer_load_dwordx4 v[92:95], v245, s[40:43], s61 offen offset:3456\n"
  "v_cvt_pk_bf16_f32 v214, v180, v181\n"
  "v_cvt_pk_bf16_f32 v215, v182, v183\n"
  "ds_write_b128 v242, v[212:215] offset:36096\n"
  "v_cvt_pk_bf16_f32 v216, v184, v185\n"
  "v_cvt_pk_bf16_f32 v217, v186, v187\n"
  "v_cvt_pk_bf16_f32 v218, v188, v189\n"
  "v_cvt_pk_bf16_f32 v219, v190, v191\n"
  "ds_write_b128 v242, v[216:219] offset:4096\n"
  "v_mfma_f32_32x32x16_bf16 a[144:159], v[248:251], v[248:251], 0\n"
  "v_cvt_pk_bf16_f32 v220, v192, v193\n"
  "v_cvt_pk_bf16_f32 v221, v194, v195\n"
  "v_cvt_pk_bf16_f32 v222, v156, v157\n"
  "v_cvt_pk_bf16_f32 v223, v158, v159\n"
  "ds_write_b128 v242, v[220:223] offset:37120\n"
  "v_accvgpr_read_b32 v168, a176\n"
  "v_accvgpr_read_b32 v169, a177\n"
  "v_accvgpr_read_b32 v170, a178\n"
  "v_accvgpr_read_b32 v171, a179\n"
  "v_accvgpr_read_b32 v172, a180\n"
  "v_accvgpr_read_b32 v173, a181\n"
  "v_accvgpr_read_b32 v174, a182\n"
  "v_accvgpr_read_b32 v175, a183\n"
  "v_accvgpr_read_b32 v176, a184\n"
  "v_accvgpr_read_b32 v177, a185\n"
  "v_accvgpr_read_b32 v178, a186\n"
  "v_accvgpr_read_b32 v179, a187\n"
  "v_accvgpr_read_b32 v180, a188\n"
  "s_add_u32 m0, s72, 73216\n"
  "v_accvgpr_read_b32 v181, a189\n"
  "buffer_load_dwordx4 v246, s[40:43], s64 offen lds\n"
  "v_accvgpr_read_b32 v182, a190\n"
  "v_accvgpr_read_b32 v183, a191\n"
  "v_accvgpr_read_b32 v184, a208\n"
  "v_accvgpr_read_b32 v185, a209\n"
  "v_accvgpr_read_b32 v186, a210\n"
  "v_accvgpr_read_b32 v187, a211\n"
  "v_accvgpr_read_b32 v188, a212\n"
  "v_accvgpr_read_b32 v189, a213\n"
  "v_accvgpr_read_b32 v190, a214\n"
  "v_accvgpr_read_b32 v191, a215\n"
  "v_accvgpr_read_b32 v192, a216\n"
  "v_accvgpr_read_b32 v193, a217\n"
  "v_accvgpr_read_b32 v194, a218\n"
  "v_accvgpr_read_b32 v195, a219\n"
  "v_accvgpr_read_b32 v156, a220\n"
  "v_accvgpr_read_b32 v157, a221\n"
  "v_accvgpr_read_b32 v158, a222\n"
  "v_accvgpr_read_b32 v159, a223\n"
  "v_pk_mul_f32 v[168:169], v[168:169], v[200:201] op_sel:[0,1] op_sel_hi:[1,1]\n"
  "v_pk_mul_f32 v[170:171], v[170:171], v[200:201] op_sel:[0,1] op_sel_hi:[1,1]\n"
  "v_pk_mul_f32 v[172:173], v[172:173], v[200:201] op_sel:[0,1] op_sel_hi:[1,1]\n"
  "v_pk_mul_f32 v[174:175], v[174:175], v[200:201] op_sel:[0,1] op_sel_hi:[1,1]\n"
  "v_pk_mul_f32 v[176:177], v[176:177], v[200:201] op_sel:[0,1] op_sel_hi:[1,1]\n"
  "v_pk_mul_f32 v[178:179], v[178:179], v[200:201] op_sel:[0,1] op_sel_hi:[1,1]\n"
  "v_pk_mul_f32 v[180:181], v[180:181], v[200:201] op_sel:[0,1] op_sel_hi:[1,1]\n"
  "v_pk_mul_f32 v[182:183], v[182:183], v[200:201] op_sel:[0,1] op_sel_hi:[1,1]\n"
  "v_pk_mul_f32 v[184:185], v[184:185], v[202:203] op_sel:[0,0] op_sel_hi:[1,0]\n"
  "s_lshl_b32 s86, s92, 2\n"
  "v_lshl_add_u32 v240, v252, 8, s86\n"
  "v_pk_mul_f32 v[186:187], v[186:187], v[202:203] op_sel:[0,0] op_sel_hi:[1,0]\n"
  "buffer_load_dword v255, v240, s[88:91], 0 offen sc1\n"
  "v_pk_mul_f32 v[188:189], v[188:189], v[202:203] op_sel:[0,0] op_sel_hi:[1,0]\n"
  "v_pk_mul_f32 v[190:191], v[190:191], v[202:203] op_sel:[0,0] op_sel_hi:[1,0]\n"
  "v_pk_mul_f32 v[192:193], v[192:193], v[202:203] op_sel:[0,0] op_sel_hi:[1,0]\n"
  "v_pk_mul_f32 v[194:195], v[194:195], v[202:203] op_sel:[0,0] op_sel_hi:[1,0]\n"
  "v_pk_mul_f32 v[156:157], v[156:157], v[202:203] op_sel:[0,0] op_sel_hi:[1,0]\n"
  "v_pk_mul_f32 v[158:159], v[158:159], v[202:203] op_sel:[0,0] op_sel_hi:[1,0]\n"
  "v_cvt_pk_bf16_f32 v208, v168, v169\n"
  "v_cvt_pk_bf16_f32 v209, v170, v171\n"
  "v_cvt_pk_bf16_f32 v210, v172, v173\n"
  "v_cvt_pk_bf16_f32 v211, v174, v175\n"
  "ds_write_b128 v242, v[208:211] offset:5120\n"
  "v_mfma_f32_32x32x16_bf16 a[176:191], v[248:251], v[248:251], 0\n"
  "v_cvt_pk_bf16_f32 v212, v176, v177\n"
  "v_cvt_pk_bf16_f32 v213, v178, v179\n"
  "v_cvt_pk_bf16_f32 v214, v180, v181\n"
  "v_cvt_pk_bf16_f32 v215, v182, v183\n"
  "ds_write_b128 v242, v[212:215] offset:38144\n"
  "v_cvt_pk_bf16_f32 v216, v184, v185\n"
  "v_cvt_pk_bf16_f32 v217, v186, v187\n"
  "v_cvt_pk_bf16_f32 v218, v188, v189\n"
  "v_cvt_pk_bf16_f32 v219, v190, v191\n"
  "ds_write_b128 v242, v[216:219] offset:6144\n"
  "v_mfma_f32_32x32x16_bf16 a[208:223], v[248:251], v[248:251], 0\n"
  "v_cvt_pk_bf16_f32 v220, v192, v193\n"
  "v_cvt_pk_bf16_f32 v221, v194, v195\n"
  "v_cvt_pk_bf16_f32 v222, v156, v157\n"
  "v_cvt_pk_bf16_f32 v223, v158, v159\n"
  "ds_write_b128 v242, v[220:223] offset:39168\n"
  "v_mov_b32 v204, s72\n"
  "v_mov_b32 v205, 2\n"
  "ds_write_b32 v204, v205 offset:8224\n"
  "s_waitcnt vmcnt(19) lgkmcnt(0)\n"
  "v_perm_b32 v156, v4, v0, s70\n"
  "v_perm_b32 v157, v12, v8, s70\n"
  "v_perm_b32 v158, v20, v16, s70\n"
  "v_perm_b32 v159, v28, v24, s70\n"
  "ds_read_b64_tr_b16 v[164:165], v247 offset:7168\n"
  "ds_read_b64_tr_b16 v[166:167], v247 offset:7680\n"
  "s_bitcmp1_b32 s74, 1\n"
  "s_cbranch_scc1 L_T1\n"
  "s_mov_b32 s77, 0\n"
  "s_mov_b32 s78, 2\n"
  "s_mov_b32 s76, 0\n"
  "L_es:\n"
  "ds_read_b32 v128, v248 offset:8224\n"
  "ds_read_b32 v129, v248 offset:16480\n"
  "s_waitcnt lgkmcnt(0)\n"
  "v_min_u32 v128, v128, v129\n"
  "v_cmp_gt_u32 vcc, 2, v128\n"
  "s_cbranch_vccnz L_es\n"
  "L_opstart:\n"
  "s_mov_b32 s60, 0\n"
  "s_add_u32 s63, s63, 1\n"
  "s_xor_b32 s87, s63, s79\n"
  "s_and_b32 s86, s87, 1\n"
  "s_mul_i32 s86, s86, 8256\n"
  "s_bfe_u32 s87, s87, 0x20001\n"
  "s_mul_i32 s87, s87, 33024\n"
  "s_add_u32 s87, s87, s86\n"
  "s_add_u32 s87, s87, s76\n"
  "v_add_u32 v240, s87, v243\n"
  "ds_read_b128 v[128:131], v240 offset:0\n"
  "ds_read_b128 v[132:135], v240 offset:1024\n"
  "ds_read_b128 v[136:139], v240 offset:2048\n"
  "ds_read_b128 v[140:143], v240 offset:3072\n"
  "ds_read_b128 v[144:147], v240 offset:4096\n"
  "ds_read_b128 v[148:151], v240 offset:5120\n"
  "ds_read_b128 v[152:155], v240 offset:6144\n"
  "s_waitcnt lgkmcnt(0)\n"
  "L_op:\n"
  "s_cmp_eq_u32 s77, 2\n"
  "s_cbranch_scc1 L_ns0\n"
  "s_sleep 2\n"
  "L_ns0:\n"
  "v_mfma_f32_16x16x32_bf16 a[0:3], v[156:159], v[128:131], a[0:3]\n"
  "s_waitcnt lgkmcnt(5)\n"
  "v_mfma_f32_16x16x32_bf16 a[32:35], v[156:159], v[132:135], a[32:35]\n"
  "s_waitcnt lgkmcnt(4)\n"
  "v_perm_b32 v160, v4, v0, s71\n"
  "v_perm_b32 v161, v12, v8, s71\n"
  "v_mfma_f32_16x16x32_bf16 a[64:67], v[156:159], v[136:139], a[64:67]\n"
  "s_waitcnt lgkmcnt(3)\n"
  "v_perm_b32 v162, v20, v16, s71\n"
  "v_perm_b32 v163, v28, v24, s71\n"
  "v_mfma_f32_16x16x32_bf16 a[96:99], v[156:159], v[140:143], a[96:99]\n"
  "s_waitcnt lgkmcnt(2)\n"
  "v_mfma_f32_16x16x32_bf16 a[128:131], v[156:159], v[144:147], a[128:131]\n"
  "s_waitcnt lgkmcnt(1)\n"
  "v_mfma_f32_16x16x32_bf16 a[160:163], v[156:159], v[148:151], a[160:163]\n"
  "s_waitcnt lgkmcnt(0)\n"
  "v_mfma_f32_16x16x32_bf16 a[192:195], v[156:159], v[152:155], a[192:195]\n"
  "v_mfma_f32_16x16x32_bf16 a[4:7], v[160:163], v[128:131], a[4:7]\n"
  "s_add_u32 s62, s62, 1\n"
  "s_xor_b32 s64, s62, s79\n"
  "s_and_b32 s65, s64, 1\n"
  "v_mfma_f32_16x16x32_bf16 a[36:39], v[160:163], v[132:135], a[36:39]\n"
  "v_perm_b32 v156, v5, v1, s70\n"
  "v_perm_b32 v157, v13, v9, s70\n"
  "v_mfma_f32_16x16x32_bf16 a[68:71], v[160:163], v[136:139], a[68:71]\n"
  "v_perm_b32 v158, v21, v17, s70\n"
  "v_perm_b32 v159, v29, v25, s70\n"
  "v_mfma_f32_16x16x32_bf16 a[100:103], v[160:163], v[140:143], a[100:103]\n"
  "s_mul_i32 s65, s65, 147456\n"
  "s_bfe_u32 s61, s64, 0x20001\n"
  "s_mul_i32 s61, s61, 36864\n"
  "v_mfma_f32_16x16x32_bf16 a[132:135], v[160:163], v[144:147], a[132:135]\n"
  "s_add_u32 s65, s61, s65\n"
  "s_add_u32 s61, s65, s66\n"
  "s_add_u32 s64, s65, s67\n"
  "v_mfma_f32_16x16x32_bf16 a[164:167], v[160:163], v[148:151], a[164:167]\n"
  "buffer_load_dwordx4 v[96:99], v244, s[40:43], s61 offen offset:0\n"
  "v_mfma_f32_16x16x32_bf16 a[196:199], v[160:163], v[152:155], a[196:199]\n"
  "v_mfma_f32_16x16x32_bf16 a[8:11], v[156:159], v[128:131], a[8:11]\n"
  "v_mfma_f32_16x16x32_bf16 a[40:43], v[156:159], v[132:135], a[40:43]\n"
  "v_perm_b32 v160, v5, v1, s71\n"
  "v_perm_b32 v161, v13, v9, s71\n"
  "v_mfma_f32_16x16x32_bf16 a[72:75], v[156:159], v[136:139], a[72:75]\n"
  "v_perm_b32 v162, v21, v17, s71\n"
  "v_perm_b32 v163, v29, v25, s71\n"
  "v_mfma_f32_16x16x32_bf16 a[104:107], v[156:159], v[140:143], a[104:107]\n"
  "buffer_load_dwordx4 v[100:103], v244, s[40:43], s61 offen offset:1152\n"
  "v_mfma_f32_16x16x32_bf16 a[136:139], v[156:159], v[144:147], a[136:139]\n"
  "v_mfma_f32_16x16x32_bf16 a[168:171], v[156:159], v[148:151], a[168:171]\n"
  "v_mfma_f32_16x16x32_bf16 a[200:203], v[156:159], v[152:155], a[200:203]\n"
  "v_mfma_f32_16x16x32_bf16 a[12:15], v[160:163], v[128:131], a[12:15]\n"
  "buffer_load_dwordx4 v[104:107], v244, s[40:43], s61 offen offset:2304\n"
  "v_mfma_f32_16x16x32_bf16 a[44:47], v[160:163], v[132:135], a[44:47]\n"
  "v_perm_b32 v156, v6, v2, s70\n"
  "v_perm_b32 v157, v14, v10, s70\n"
  "v_mfma_f32_16x16x32_bf16 a[76:79], v[160:163], v[136:139], a[76:79]\n"
  "v_perm_b32 v158, v22, v18, s70\n"
  "v_perm_b32 v159, v30, v26, s70\n"
  "v_mfma_f32_16x16x32_bf16 a[108:111], v[160:163], v[140:143], a[108:111]\n"
  "v_mfma_f32_16x16x32_bf16 a[140:143], v[160:163], v[144:147], a[140:143]\n"
  "v_mfma_f32_16x16x32_bf16 a[172:175], v[160:163], v[148:151], a[172:175]\n"
  "buffer_load_dwordx4 v[108:111], v244, s[40:43], s61 offen offset:3456\n"
  "v_mfma_f32_16x16x32_bf16 a[204:207], v[160:163], v[152:155], a[204:207]\n"
  "v_mfma_f32_16x16x32_bf16 a[16:19], v[156:159], v[128:131], a[16:19]\n"
  "v_mfma_f32_16x16x32_bf16 a[48:51], v[156:159], v[132:135], a[48:51]\n"
  "v_perm_b32 v160, v6, v2, s71\n"
  "v_perm_b32 v161, v14, v10, s71\n"
  "v_mfma_f32_16x16x32_bf16 a[80:83], v[156:159], v[136:139], a[80:83]\n"
  "v_perm_b32 v162, v22, v18, s71\n"
  "v_perm_b32 v163, v30, v26, s71\n"
  "v_mfma_f32_16x16x32_bf16 a[112:115], v[156:159], v[140:143], a[112:115]\n"
  "buffer_load_dwordx4 v[112:115], v245, s[40:43], s61 offen offset:0\n"
  "v_mfma_f32_16x16x32_bf16 a[144:147], v[156:159], v[144:147], a[144:147]\n"
  "v_mfma_f32_16x16x32_bf16 a[176:179], v[156:159], v[148:151], a[176:179]\n"
  "v_mfma_f32_16x16x32_bf16 a[208:211], v[156:159], v[152:155], a[208:211]\n"
  "v_mfma_f32_16x16x32_bf16 a[20:23], v[160:163], v[128:131], a[20:23]\n"
  "buffer_load_dwordx4 v[116:119], v245, s[40:43], s61 offen offset:1152\n"
  "v_mfma_f32_16x16x32_bf16 a[52:55], v[160:163], v[132:135], a[52:55]\n"
  "v_perm_b32 v156, v7, v3, s70\n"
  "v_perm_b32 v157, v15, v11, s70\n"
  "v_mfma_f32_16x16x32_bf16 a[84:87], v[160:163], v[136:139], a[84:87]\n"
  "v_perm_b32 v158, v23, v19, s70\n"
  "v_perm_b32 v159, v31, v27, s70\n"
  "v_mfma_f32_16x16x32_bf16 a[116:119], v[160:163], v[140:143], a[116:119]\n"
  "s_add_u32 s63, s63, 1\n"
  "s_xor_b32 s87, s63, s79\n"
  "s_and_b32 s86, s87, 1\n"
  "v_mfma_f32_16x16x32_bf16 a[148:151], v[160:163], v[144:147], a[148:151]\n"
  "s_mul_i32 s86, s86, 8256\n"
  "s_bfe_u32 s87, s87, 0x20001\n"
  "s_mul_i32 s87, s87, 33024\n"
  "v_mfma_f32_16x16x32_bf16 a[180:183], v[160:163], v[148:151], a[180:183]\n"
  "buffer_load_dwordx4 v[120:123], v245, s[40:43], s61 offen offset:2304\n"
  "v_mfma_f32_16x16x32_bf16 a[212:215], v[160:163], v[152:155], a[212:215]\n"
  "s_add_u32 s87, s87, s86\n"
  "s_add_u32 s87, s87, s76\n"
  "v_add_u32 v240, s87, v243\n"
  "v_mfma_f32_16x16x32_bf16 a[24:27], v[156:159], v[128:131], a[24:27]\n"
  "v_mfma_f32_16x16x32_bf16 a[56:59], v[156:159], v[132:135], a[56:59]\n"
  "v_perm_b32 v160, v7, v3, s71\n"
  "v_perm_b32 v161, v15, v11, s71\n"
  "v_mfma_f32_16x16x32_bf16 a[88:91], v[156:159], v[136:139], a[88:91]\n"
  "v_perm_b32 v162, v23, v19, s71\n"
  "v_perm_b32 v163, v31, v27, s71\n"
  "v_mfma_f32_16x16x32_bf16 a[120:123], v[156:159], v[140:143], a[120:123]\n"
  "buffer_load_dwordx4 v[124:127], v245, s[40:43], s61 offen offset:3456\n"
  "v_mfma_f32_16x16x32_bf16 a[152:155], v[156:159], v[144:147], a[152:155]\n"
  "v_mfma_f32_16x16x32_bf16 a[184:187], v[156:159], v[148:151], a[184:187]\n"
  "v_mfma_f32_16x16x32_bf16 a[216:219], v[156:159], v[152:155], a[216:219]\n"
  "s_add_u32 m0, s72, 106240\n"
  "v_mfma_f32_16x16x32_bf16 a[28:31], v[160:163], v[128:131], a[28:31]\n"
  "buffer_load_dwordx4 v246, s[40:43], s64 offen lds\n"
  "v_mfma_f32_16x16x32_bf16 a[60:63], v[160:163], v[132:135], a[60:63]\n"
  "v_mfma_f32_16x16x32_bf16 a[92:95], v[160:163], v[136:139], a[92:95]\n"
  "s_waitcnt vmcnt(18)\n"
  "ds_read_b64_tr_b16 v[168:169], v247 offset:40192\n"
  "ds_read_b64_tr_b16 v[170:171], v247 offset:40704\n"
  "v_mfma_f32_16x16x32_bf16 a[124:127], v[160:163], v[140:143], a[124:127]\n"
  "v_mfma_f32_16x16x32_bf16 a[156:159], v[160:163], v[144:147], a[156:159]\n"
  "v_perm_b32 v156, v36, v32, s70\n"
  "v_perm_b32 v157, v44, v40, s70\n"
  "v_mfma_f32_16x16x32_bf16 a[188:191], v[160:163], v[148:151], a[188:191]\n"
  "v_perm_b32 v158, v52, v48, s70\n"
  "v_perm_b32 v159, v60, v56, s70\n"
  "v_mfma_f32_16x16x32_bf16 a[220:223], v[160:163], v[152:155], a[220:223]\n"
  "v_mfma_f32_16x16x32_bf16 a[224:227], v[164:167], v[128:131], a[224:227]\n"
  "ds_read_b128 v[128:131], v240 offset:0\n"
  "v_mfma_f32_16x16x32_bf16 a[228:231], v[164:167], v[132:135], a[228:231]\n"
  "ds_read_b128 v[132:135], v240 offset:1024\n"
  "v_mfma_f32_16x16x32_bf16 a[232:235], v[164:167], v[136:139], a[232:235]\n"
  "ds_read_b128 v[136:139], v240 offset:2048\n"
  "v_mfma_f32_16x16x32_bf16 a[236:239], v[164:167], v[140:143], a[236:239]\n"
  "ds_read_b128 v[140:143], v240 offset:3072\n"
  "v_mfma_f32_16x16x32_bf16 a[240:243], v[164:167], v[144:147], a[240:243]\n"
  "ds_read_b128 v[144:147], v240 offset:4096\n"
  "v_mfma_f32_16x16x32_bf16 a[244:247], v[164:167], v[148:151], a[244:247]\n"
  "ds_read_b128 v[148:151], v240 offset:5120\n"
  "v_mfma_f32_16x16x32_bf16 a[248:251], v[164:167], v[152:155], a[248:251]\n"
  "ds_read_b128 v[152:155], v240 offset:6144\n"
  "s_waitcnt lgkmcnt(6)\n"
  "s_cmp_eq_u32 s77, 2\n"
  "s_cbranch_scc1 L_ns1\n"
  "s_sleep 2\n"
  "L_ns1:\n"
  "v_mfma_f32_16x16x32_bf16 a[0:3], v[156:159], v[128:131], a[0:3]\n"
  "s_waitcnt lgkmcnt(5)\n"
  "v_mfma_f32_16x16x32_bf16 a[32:35], v[156:159], v[132:135], a[32:35]\n"
  "s_waitcnt lgkmcnt(4)\n"
  "v_perm_b32 v160, v36, v32, s71\n"
  "v_perm_b32 v161, v44, v40, s71\n"
  "v_mfma_f32_16x16x32_bf16 a[64:67], v[156:159], v[136:139], a[64:67]\n"
  "s_waitcnt lgkmcnt(3)\n"
  "v_perm_b32 v162, v52, v48, s71\n"
  "v_perm_b32 v163, v60, v56, s71\n"
  "v_mfma_f32_16x16x32_bf16 a[96:99], v[156:159], v[140:143], a[96:99]\n"
  "s_waitcnt lgkmcnt(2)\n"
  "v_mfma_f32_16x16x32_bf16 a[128:131], v[156:159], v[144:147], a[128:131]\n"
  "s_waitcnt lgkmcnt(1)\n"
  "v_mfma_f32_16x16x32_bf16 a[160:163], v[156:159], v[148:151], a[160:163]\n"
  "s_waitcnt lgkmcnt(0)\n"
  "v_mfma_f32_16x16x32_bf16 a[192:195], v[156:159], v[152:155], a[192:195]\n"
  "s_bitcmp1_b32 s60, 0\n"
  "s_cselect_b64 s[66:67], s[68:69], s[66:67]\n"
  "v_mfma_f32_16x16x32_bf16 a[4:7], v[160:163], v[128:131], a[4:7]\n"
  "s_add_u32 s62, s62, 1\n"
  "s_xor_b32 s64, s62, s79\n"
  "s_and_b32 s65, s64, 1\n"
  "v_mfma_f32_16x16x32_bf16 a[36:39], v[160:163], v[132:135], a[36:39]\n"
  "v_perm_b32 v156, v37, v33, s70\n"
  "v_perm_b32 v157, v45, v41, s70\n"
  "v_mfma_f32_16x16x32_bf16 a[68:71], v[160:163], v[136:139], a[68:71]\n"
  "v_perm_b32 v158, v53, v49, s70\n"
  "v_perm_b32 v159, v61, v57, s70\n"
  "v_mfma_f32_16x16x32_bf16 a[100:103], v[160:163], v[140:143], a[100:103]\n"
  "s_mul_i32 s65, s65, 147456\n"
  "s_bfe_u32 s61, s64, 0x20001\n"
  "s_mul_i32 s61, s61, 36864\n"
  "v_mfma_f32_16x16x32_bf16 a[132:135], v[160:163], v[144:147], a[132:135]\n"
  "s_add_u32 s65, s61, s65\n"
  "s_add_u32 s61, s65, s66\n"
  "s_add_u32 s64, s65, s67\n"
  "v_mfma_f32_16x16x32_bf16 a[164:167], v[160:163], v[148:151], a[164:167]\n"
  "buffer_load_dwordx4 v[0:3], v244, s[40:43], s61 offen offset:0\n"
  "v_mfma_f32_16x16x32_bf16 a[196:199], v[160:163], v[152:155], a[196:199]\n"
  "v_mfma_f32_16x16x32_bf16 a[8:11], v[156:159], v[128:131], a[8:11]\n"
  "v_mfma_f32_16x16x32_bf16 a[40:43], v[156:159], v[132:135], a[40:43]\n"
  "v_perm_b32 v160, v37, v33, s71\n"
  "v_perm_b32 v161, v45, v41, s71\n"
  "v_mfma_f32_16x16x32_bf16 a[72:75], v[156:159], v[136:139], a[72:75]\n"
  "v_perm_b32 v162, v53, v49, s71\n"
  "v_perm_b32 v163, v61, v57, s71\n"
  "v_mfma_f32_16x16x32_bf16 a[104:107], v[156:159], v[140:143], a[104:107]\n"
  "buffer_load_dwordx4 v[4:7], v244, s[40:43], s61 offen offset:1152\n"
  "v_mfma_f32_16x16x32_bf16 a[136:139], v[156:159], v[144:147], a[136:139]\n"
  "v_mfma_f32_16x16x32_bf16 a[168:171], v[156:159], v[148:151], a[168:171]\n"
  "v_mfma_f32_16x16x32_bf16 a[200:203], v[156:159], v[152:155], a[200:203]\n"
  "v_mfma_f32_16x16x32_bf16 a[12:15], v[160:163], v[128:131], a[12:15]\n"
  "buffer_load_dwordx4 v[8:11], v244, s[40:43], s61 offen offset:2304\n"
  "v_mfma_f32_16x16x32_bf16 a[44:47], v[160:163], v[132:135], a[44:47]\n"
  "v_perm_b32 v156, v38, v34, s70\n"
  "v_perm_b32 v157, v46, v42, s70\n"
  "v_mfma_f32_16x16x32_bf16 a[76:79], v[160:163], v[136:139], a[76:79]\n"
  "v_perm_b32 v158, v54, v50, s70\n"
  "v_perm_b32 v159, v62, v58, s70\n"
  "v_mfma_f32_16x16x32_bf16 a[108:111], v[160:163], v[140:143], a[108:111]\n"
  "v_mfma_f32_16x16x32_bf16 a[140:143], v[160:163], v[144:147], a[140:143]\n"
  "v_mfma_f32_16x16x32_bf16 a[172:175], v[160:163], v[148:151], a[172:175]\n"
  "buffer_load_dwordx4 v[12:15], v244, s[40:43], s61 offen offset:3456\n"
  "v_mfma_f32_16x16x32_bf16 a[204:207], v[160:163], v[152:155], a[204:207]\n"
  "v_mfma_f32_16x16x32_bf16 a[16:19], v[156:159], v[128:131], a[16:19]\n"
  "v_mfma_f32_16x16x32_bf16 a[48:51], v[156:159], v[132:135], a[48:51]\n"
  "v_perm_b32 v160, v38, v34, s71\n"
  "v_perm_b32 v161, v46, v42, s71\n"
  "v_mfma_f32_16x16x32_bf16 a[80:83], v[156:159], v[136:139], a[80:83]\n"
  "v_perm_b32 v162, v54, v50, s71\n"
  "v_perm_b32 v163, v62, v58, s71\n"
  "v_mfma_f32_16x16x32_bf16 a[112:115], v[156:159], v[140:143], a[112:115]\n"
  "buffer_load_dwordx4 v[16:19], v245, s[40:43], s61 offen offset:0\n"
  "v_mfma_f32_16x16x32_bf16 a[144:147], v[156:159], v[144:147], a[144:147]\n"
  "v_mfma_f32_16x16x32_bf16 a[176:179], v[156:159], v[148:151], a[176:179]\n"
  "v_mfma_f32_16x16x32_bf16 a[208:211], v[156:159], v[152:155], a[208:211]\n"
  "v_mfma_f32_16x16x32_bf16 a[20:23], v[160:163], v[128:131], a[20:23]\n"
  "buffer_load_dwordx4 v[20:23], v245, s[40:43], s61 offen offset:1152\n"
  "v_mfma_f32_16x16x32_bf16 a[52:55], v[160:163], v[132:135], a[52:55]\n"
  "v_perm_b32 v156, v39, v35, s70\n"
  "v_perm_b32 v157, v47, v43, s70\n"
  "v_mfma_f32_16x16x32_bf16 a[84:87], v[160:163], v[136:139], a[84:87]\n"
  "v_perm_b32 v158, v55, v51, s70\n"
  "v_perm_b32 v159, v63, v59, s70\n"
  "v_mfma_f32_16x16x32_bf16 a[116:119], v[160:163], v[140:143], a[116:119]\n"
  "s_add_u32 s63, s63, 1\n"
  "s_xor_b32 s87, s63, s79\n"
  "s_and_b32 s86, s87, 1\n"
  "v_mfma_f32_16x16x32_bf16 a[148:151], v[160:163], v[144:147], a[148:151]\n"
  "s_mul_i32 s86, s86, 8256\n"
  "s_bfe_u32 s87, s87, 0x20001\n"
  "s_mul_i32 s87, s87, 33024\n"
  "v_mfma_f32_16x16x32_bf16 a[180:183], v[160:163], v[148:151], a[180:183]\n"
  "buffer_load_dwordx4 v[24:27], v245, s[40:43], s61 offen offset:2304\n"
  "v_mfma_f32_16x16x32_bf16 a[212:215], v[160:163], v[152:155], a[212:215]\n"
  "s_add_u32 s87, s87, s86\n"
  "s_add_u32 s87, s87, s76\n"
  "v_add_u32 v240, s87, v243\n"
  "v_mfma_f32_16x16x32_bf16 a[24:27], v[156:159], v[128:131], a[24:27]\n"
  "v_mfma_f32_16x16x32_bf16 a[56:59], v[156:159], v[132:135], a[56:59]\n"
  "v_perm_b32 v160, v39, v35, s71\n"
  "v_perm_b32 v161, v47, v43, s71\n"
  "v_mfma_f32_16x16x32_bf16 a[88:91], v[156:159], v[136:139], a[88:91]\n"
  "v_perm_b32 v162, v55, v51, s71\n"
  "v_perm_b32 v163, v63, v59, s71\n"
  "v_mfma_f32_16x16x32_bf16 a[120:123], v[156:159], v[140:143], a[120:123]\n"
  "buffer_load_dwordx4 v[28:31], v245, s[40:43], s61 offen offset:3456\n"
  "v_mfma_f32_16x16x32_bf16 a[152:155], v[156:159], v[144:147], a[152:155]\n"
  "v_mfma_f32_16x16x32_bf16 a[184:187], v[156:159], v[148:151], a[184:187]\n"
  "v_mfma_f32_16x16x32_bf16 a[216:219], v[156:159], v[152:155], a[216:219]\n"
  "s_add_u32 m0, s72, 7168\n"
  "v_mfma_f32_16x16x32_bf16 a[28:31], v[160:163], v[128:131], a[28:31]\n"
  "buffer_load_dwordx4 v246, s[40:43], s64 offen lds\n"
  "v_mfma_f32_16x16x32_bf16 a[60:63], v[160:163], v[132:135], a[60:63]\n"
  "v_mfma_f32_16x16x32_bf16 a[92:95], v[160:163], v[136:139], a[92:95]\n"
  "s_waitcnt vmcnt(18)\n"
  "ds_read_b64_tr_b16 v[164:165], v253 offset:7168\n"
  "ds_read_b64_tr_b16 v[166:167], v253 offset:7680\n"
  "v_mfma_f32_16x16x32_bf16 a[124:127], v[160:163], v[140:143], a[124:127]\n"
  "v_mfma_f32_16x16x32_bf16 a[156:159], v[160:163], v[144:147], a[156:159]\n"
  "v_perm_b32 v156, v68, v64, s70\n"
  "v_perm_b32 v157, v76, v72, s70\n"
  "v_mfma_f32_16x16x32_bf16 a[188:191], v[160:163], v[148:151], a[188:191]\n"
  "v_perm_b32 v158, v84, v80, s70\n"
  "v_perm_b32 v159, v92, v88, s70\n"
  "v_mfma_f32_16x16x32_bf16 a[220:223], v[160:163], v[152:155], a[220:223]\n"
  "v_mfma_f32_16x16x32_bf16 a[224:227], v[168:171], v[128:131], a[224:227]\n"
  "ds_read_b128 v[128:131], v240 offset:0\n"
  "v_mfma_f32_16x16x32_bf16 a[228:231], v[168:171], v[132:135], a[228:231]\n"
  "ds_read_b128 v[132:135], v240 offset:1024\n"
  "v_mfma_f32_16x16x32_bf16 a[232:235], v[168:171], v[136:139], a[232:235]\n"
  "ds_read_b128 v[136:139], v240 offset:2048\n"
  "v_mfma_f32_16x16x32_bf16 a[236:239], v[168:171], v[140:143], a[236:239]\n"
  "ds_read_b128 v[140:143], v240 offset:3072\n"
  "v_mfma_f32_16x16x32_bf16 a[240:243], v[168:171], v[144:147], a[240:243]\n"
  "ds_read_b128 v[144:147], v240 offset:4096\n"
  "v_mfma_f32_16x16x32_bf16 a[244:247], v[168:171], v[148:151], a[244:247]\n"
  "ds_read_b128 v[148:151], v240 offset:5120\n"
  "v_mfma_f32_16x16x32_bf16 a[248:251], v[168:171], v[152:155], a[248:251]\n"
  "ds_read_b128 v[152:155], v240 offset:6144\n"
  "s_waitcnt lgkmcnt(6)\n"
  "s_cmp_eq_u32 s77, 2\n"
  "s_cbranch_scc1 L_ns2\n"
  "s_sleep 2\n"
  "L_ns2:\n"
  "v_mfma_f32_16x16x32_bf16 a[0:3], v[156:159], v[128:131], a[0:3]\n"
  "s_waitcnt lgkmcnt(5)\n"
  "v_mfma_f32_16x16x32_bf16 a[32:35], v[156:159], v[132:135], a[32:35]\n"
  "s_waitcnt lgkmcnt(4)\n"
  "v_perm_b32 v160, v68, v64, s71\n"
  "v_perm_b32 v161, v76, v72, s71\n"
  "v_mfma_f32_16x16x32_bf16 a[64:67], v[156:159], v[136:139], a[64:67]\n"
  "s_waitcnt lgkmcnt(3)\n"
  "v_perm_b32 v162, v84, v80, s71\n"
  "v_perm_b32 v163, v92, v88, s71\n"
  "v_mfma_f32_16x16x32_bf16 a[96:99], v[156:159], v[140:143], a[96:99]\n"
  "s_waitcnt lgkmcnt(2)\n"
  "v_mfma_f32_16x16x32_bf16 a[128:131], v[156:159], v[144:147], a[128:131]\n"
  "s_waitcnt lgkmcnt(1)\n"
  "v_mfma_f32_16x16x32_bf16 a[160:163], v[156:159], v[148:151], a[160:163]\n"
  "s_waitcnt lgkmcnt(0)\n"
  "v_mfma_f32_16x16x32_bf16 a[192:195], v[156:159], v[152:155], a[192:195]\n"
  "v_mfma_f32_16x16x32_bf16 a[4:7], v[160:163], v[128:131], a[4:7]\n"
  "s_add_u32 s62, s62, 1\n"
  "s_xor_b32 s64, s62, s79\n"
  "s_and_b32 s65, s64, 1\n"
  "v_mfma_f32_16x16x32_bf16 a[36:39], v[160:163], v[132:135], a[36:39]\n"
  "v_perm_b32 v156, v69, v65, s70\n"
  "v_perm_b32 v157, v77, v73, s70\n"
  "v_mfma_f32_16x16x32_bf16 a[68:71], v[160:163], v[136:139], a[68:71]\n"
  "v_perm_b32 v158, v85, v81, s70\n"
  "v_perm_b32 v159, v93, v89, s70\n"
  "v_mfma_f32_16x16x32_bf16 a[100:103], v[160:163], v[140:143], a[100:103]\n"
  "s_mul_i32 s65, s65, 147456\n"
  "s_bfe_u32 s61, s64, 0x20001\n"
  "s_mul_i32 s61, s61, 36864\n"
  "v_mfma_f32_16x16x32_bf16 a[132:135], v[160:163], v[144:147], a[132:135]\n"
  "s_add_u32 s65, s61, s65\n"
  "s_add_u32 s61, s65, s66\n"
  "s_add_u32 s64, s65, s67\n"
  "v_mfma_f32_16x16x32_bf16 a[164:167], v[160:163], v[148:151], a[164:167]\n"
  "buffer_load_dwordx4 v[32:35], v244, s[40:43], s61 offen offset:0\n"
  "v_mfma_f32_16x16x32_bf16 a[196:199], v[160:163], v[152:155], a[196:199]\n"
  "v_mfma_f32_16x16x32_bf16 a[8:11], v[156:159], v[128:131], a[8:11]\n"
  "v_mfma_f32_16x16x32_bf16 a[40:43], v[156:159], v[132:135], a[40:43]\n"
  "v_perm_b32 v160, v69, v65, s71\n"
  "v_perm_b32 v161, v77, v73, s71\n"
  "v_mfma_f32_16x16x32_bf16 a[72:75], v[156:159], v[136:139], a[72:75]\n"
  "v_perm_b32 v162, v85, v81, s71\n"
  "v_perm_b32 v163, v93, v89, s71\n"
  "v_mfma_f32_16x16x32_bf16 a[104:107], v[156:159], v[140:143], a[104:107]\n"
  "buffer_load_dwordx4 v[36:39], v244, s[40:43], s61 offen offset:1152\n"
  "v_mfma_f32_16x16x32_bf16 a[136:139], v[156:159], v[144:147], a[136:139]\n"
  "v_mfma_f32_16x16x32_bf16 a[168:171], v[156:159], v[148:151], a[168:171]\n"
  "v_mfma_f32_16x16x32_bf16 a[200:203], v[156:159], v[152:155], a[200:203]\n"
  "v_mfma_f32_16x16x32_bf16 a[12:15], v[160:163], v[128:131], a[12:15]\n"
  "buffer_load_dwordx4 v[40:43], v244, s[40:43], s61 offen offset:2304\n"
  "v_mfma_f32_16x16x32_bf16 a[44:47], v[160:163], v[132:135], a[44:47]\n"
  "v_perm_b32 v156, v70, v66, s70\n"
  "v_perm_b32 v157, v78, v74, s70\n"
  "v_mfma_f32_16x16x32_bf16 a[76:79], v[160:163], v[136:139], a[76:79]\n"
  "v_perm_b32 v158, v86, v82, s70\n"
  "v_perm_b32 v159, v94, v90, s70\n"
  "v_mfma_f32_16x16x32_bf16 a[108:111], v[160:163], v[140:143], a[108:111]\n"
  "v_mfma_f32_16x16x32_bf16 a[140:143], v[160:163], v[144:147], a[140:143]\n"
  "v_mfma_f32_16x16x32_bf16 a[172:175], v[160:163], v[148:151], a[172:175]\n"
  "buffer_load_dwordx4 v[44:47], v244, s[40:43], s61 offen offset:3456\n"
  "v_mfma_f32_16x16x32_bf16 a[204:207], v[160:163], v[152:155], a[204:207]\n"
  "v_mfma_f32_16x16x32_bf16 a[16:19], v[156:159], v[128:131], a[16:19]\n"
  "v_mfma_f32_16x16x32_bf16 a[48:51], v[156:159], v[132:135], a[48:51]\n"
  "v_perm_b32 v160, v70, v66, s71\n"
  "v_perm_b32 v161, v78, v74, s71\n"
  "v_mfma_f32_16x16x32_bf16 a[80:83], v[156:159], v[136:139], a[80:83]\n"
  "v_perm_b32 v162, v86, v82, s71\n"
  "v_perm_b32 v163, v94, v90, s71\n"
  "v_mfma_f32_16x16x32_bf16 a[112:115], v[156:159], v[140:143], a[112:115]\n"
  "buffer_load_dwordx4 v[48:51], v245, s[40:43], s61 offen offset:0\n"
  "v_mfma_f32_16x16x32_bf16 a[144:147], v[156:159], v[144:147], a[144:147]\n"
  "v_mfma_f32_16x16x32_bf16 a[176:179], v[156:159], v[148:151], a[176:179]\n"
  "v_mfma_f32_16x16x32_bf16 a[208:211], v[156:159], v[152:155], a[208:211]\n"
  "v_mfma_f32_16x16x32_bf16 a[20:23], v[160:163], v[128:131], a[20:23]\n"
  "buffer_load_dwordx4 v[52:55], v245, s[40:43], s61 offen offset:1152\n"
  "v_mfma_f32_16x16x32_bf16 a[52:55], v[160:163], v[132:135], a[52:55]\n"
  "v_perm_b32 v156, v71, v67, s70\n"
  "v_perm_b32 v157, v79, v75, s70\n"
  "v_mfma_f32_16x16x32_bf16 a[84:87], v[160:163], v[136:139], a[84:87]\n"
  "v_perm_b32 v158, v87, v83, s70\n"
  "v_perm_b32 v159, v95, v91, s70\n"
  "v_mfma_f32_16x16x32_bf16 a[116:119], v[160:163], v[140:143], a[116:119]\n"
  "s_add_u32 s63, s63, 1\n"
  "s_xor_b32 s87, s63, s79\n"
  "s_and_b32 s86, s87, 1\n"
  "v_mfma_f32_16x16x32_bf16 a[148:151], v[160:163], v[144:147], a[148:151]\n"
  "s_mul_i32 s86, s86, 8256\n"
  "s_bfe_u32 s87, s87, 0x20001\n"
  "s_mul_i32 s87, s87, 33024\n"
  "v_mfma_f32_16x16x32_bf16 a[180:183], v[160:163], v[148:151], a[180:183]\n"
  "buffer_load_dwordx4 v[56:59], v245, s[40:43], s61 offen offset:2304\n"
  "v_mfma_f32_16x16x32_bf16 a[212:215], v[160:163], v[152:155], a[212:215]\n"
  "s_add_u32 s87, s87, s86\n"
  "s_add_u32 s87, s87, s76\n"
  "v_add_u32 v240, s87, v243\n"
  "v_mfma_f32_16x16x32_bf16 a[24:27], v[156:159], v[128:131], a[24:27]\n"
  "v_mfma_f32_16x16x32_bf16 a[56:59], v[156:159], v[132:135], a[56:59]\n"
  "v_perm_b32 v160, v71, v67, s71\n"
  "v_perm_b32 v161, v79, v75, s71\n"
  "v_mfma_f32_16x16x32_bf16 a[88:91], v[156:159], v[136:139], a[88:91]\n"
  "v_perm_b32 v162, v87, v83, s71\n"
  "v_perm_b32 v163, v95, v91, s71\n"
  "v_mfma_f32_16x16x32_bf16 a[120:123], v[156:159], v[140:143], a[120:123]\n"
  "buffer_load_dwordx4 v[60:63], v245, s[40:43], s61 offen offset:3456\n"
  "v_mfma_f32_16x16x32_bf16 a[152:155], v[156:159], v[144:147], a[152:155]\n"
  "v_mfma_f32_16x16x32_bf16 a[184:187], v[156:159], v[148:151], a[184:187]\n"
  "v_mfma_f32_16x16x32_bf16 a[216:219], v[156:159], v[152:155], a[216:219]\n"
  "s_add_u32 m0, s72, 40192\n"
  "v_mfma_f32_16x16x32_bf16 a[28:31], v[160:163], v[128:131], a[28:31]\n"
  "buffer_load_dwordx4 v246, s[40:43], s64 offen lds\n"
  "v_mfma_f32_16x16x32_bf16 a[60:63], v[160:163], v[132:135], a[60:63]\n"
  "v_mfma_f32_16x16x32_bf16 a[92:95], v[160:163], v[136:139], a[92:95]\n"
  "s_waitcnt vmcnt(18)\n"
  "ds_read_b64_tr_b16 v[168:169], v253 offset:40192\n"
  "ds_read_b64_tr_b16 v[170:171], v253 offset:40704\n"
  "v_mfma_f32_16x16x32_bf16 a[124:127], v[160:163], v[140:143], a[124:127]\n"
  "v_mfma_f32_16x16x32_bf16 a[156:159], v[160:163], v[144:147], a[156:159]\n"
  "v_perm_b32 v156, v100, v96, s70\n"
  "v_perm_b32 v157, v108, v104, s70\n"
  "v_mfma_f32_16x16x32_bf16 a[188:191], v[160:163], v[148:151], a[188:191]\n"
  "v_perm_b32 v158, v116, v112, s70\n"
  "v_perm_b32 v159, v124, v120, s70\n"
  "v_mfma_f32_16x16x32_bf16 a[220:223], v[160:163], v[152:155], a[220:223]\n"
  "v_mfma_f32_16x16x32_bf16 a[224:227], v[164:167], v[128:131], a[224:227]\n"
  "ds_read_b128 v[128:131], v240 offset:0\n"
  "v_mfma_f32_16x16x32_bf16 a[228:231], v[164:167], v[132:135], a[228:231]\n"
  "ds_read_b128 v[132:135], v240 offset:1024\n"
  "v_mfma_f32_16x16x32_bf16 a[232:235], v[164:167], v[136:139], a[232:235]\n"
  "ds_read_b128 v[136:139], v240 offset:2048\n"
  "v_mfma_f32_16x16x32_bf16 a[236:239], v[164:167], v[140:143], a[236:239]\n"
  "ds_read_b128 v[140:143], v240 offset:3072\n"
  "v_mfma_f32_16x16x32_bf16 a[240:243], v[164:167], v[144:147], a[240:243]\n"
  "ds_read_b128 v[144:147], v240 offset:4096\n"
  "v_mfma_f32_16x16x32_bf16 a[244:247], v[164:167], v[148:151], a[244:247]\n"
  "ds_read_b128 v[148:151], v240 offset:5120\n"
  "v_mfma_f32_16x16x32_bf16 a[248:251], v[164:167], v[152:155], a[248:251]\n"
  "ds_read_b128 v[152:155], v240 offset:6144\n"
  "s_waitcnt lgkmcnt(6)\n"
  "s_cmp_eq_u32 s77, 2\n"
  "s_cbranch_scc1 L_ns3\n"
  "s_sleep 2\n"
  "L_ns3:\n"
  "v_mfma_f32_16x16x32_bf16 a[0:3], v[156:159], v[128:131], a[0:3]\n"
  "s_waitcnt lgkmcnt(5)\n"
  "v_mfma_f32_16x16x32_bf16 a[32:35], v[156:159], v[132:135], a[32:35]\n"
  "s_waitcnt lgkmcnt(4)\n"
  "v_perm_b32 v160, v100, v96, s71\n"
  "v_perm_b32 v161, v108, v104, s71\n"
  "v_mfma_f32_16x16x32_bf16 a[64:67], v[156:159], v[136:139], a[64:67]\n"
  "s_waitcnt lgkmcnt(3)\n"
  "v_perm_b32 v162, v116, v112, s71\n"
  "v_perm_b32 v163, v124, v120, s71\n"
  "v_mfma_f32_16x16x32_bf16 a[96:99], v[156:159], v[140:143], a[96:99]\n"
  "s_waitcnt lgkmcnt(2)\n"
  "v_mfma_f32_16x16x32_bf16 a[128:131], v[156:159], v[144:147], a[128:131]\n"
  "s_waitcnt lgkmcnt(1)\n"
  "v_mfma_f32_16x16x32_bf16 a[160:163], v[156:159], v[148:151], a[160:163]\n"
  "s_waitcnt lgkmcnt(0)\n"
  "v_mfma_f32_16x16x32_bf16 a[192:195], v[156:159], v[152:155], a[192:195]\n"
  "v_mfma_f32_16x16x32_bf16 a[4:7], v[160:163], v[128:131], a[4:7]\n"
  "s_add_u32 s62, s62, 1\n"
  "s_xor_b32 s64, s62, s79\n"
  "s_and_b32 s65, s64, 1\n"
  "v_mfma_f32_16x16x32_bf16 a[36:39], v[160:163], v[132:135], a[36:39]\n"
  "v_perm_b32 v156, v101, v97, s70\n"
  "v_perm_b32 v157, v109, v105, s70\n"
  "v_mfma_f32_16x16x32_bf16 a[68:71], v[160:163], v[136:139], a[68:71]\n"
  "v_perm_b32 v158, v117, v113, s70\n"
  "v_perm_b32 v159, v125, v121, s70\n"
  "v_mfma_f32_16x16x32_bf16 a[100:103], v[160:163], v[140:143], a[100:103]\n"
  "s_mul_i32 s65, s65, 147456\n"
  "s_bfe_u32 s61, s64, 0x20001\n"
  "s_mul_i32 s61, s61, 36864\n"
  "v_mfma_f32_16x16x32_bf16 a[132:135], v[160:163], v[144:147], a[132:135]\n"
  "s_add_u32 s65, s61, s65\n"
  "s_add_u32 s61, s65, s66\n"
  "s_add_u32 s64, s65, s67\n"
  "v_mfma_f32_16x16x32_bf16 a[164:167], v[160:163], v[148:151], a[164:167]\n"
  "buffer_load_dwordx4 v[64:67], v244, s[40:43], s61 offen offset:0\n"
  "v_mfma_f32_16x16x32_bf16 a[196:199], v[160:163], v[152:155], a[196:199]\n"
  "v_mfma_f32_16x16x32_bf16 a[8:11], v[156:159], v[128:131], a[8:11]\n"
  "v_mfma_f32_16x16x32_bf16 a[40:43], v[156:159], v[132:135], a[40:43]\n"
  "v_perm_b32 v160, v101, v97, s71\n"
  "v_perm_b32 v161, v109, v105, s71\n"
  "v_mfma_f32_16x16x32_bf16 a[72:75], v[156:159], v[136:139], a[72:75]\n"
  "v_perm_b32 v162, v117, v113, s71\n"
  "v_perm_b32 v163, v125, v121, s71\n"
  "v_mfma_f32_16x16x32_bf16 a[104:107], v[156:159], v[140:143], a[104:107]\n"
  "buffer_load_dwordx4 v[68:71], v244, s[40:43], s61 offen offset:1152\n"
  "v_mfma_f32_16x16x32_bf16 a[136:139], v[156:159], v[144:147], a[136:139]\n"
  "v_mfma_f32_16x16x32_bf16 a[168:171], v[156:159], v[148:151], a[168:171]\n"
  "v_mfma_f32_16x16x32_bf16 a[200:203], v[156:159], v[152:155], a[200:203]\n"
  "v_mfma_f32_16x16x32_bf16 a[12:15], v[160:163], v[128:131], a[12:15]\n"
  "buffer_load_dwordx4 v[72:75], v244, s[40:43], s61 offen offset:2304\n"
  "v_mfma_f32_16x16x32_bf16 a[44:47], v[160:163], v[132:135], a[44:47]\n"
  "v_perm_b32 v156, v102, v98, s70\n"
  "v_perm_b32 v157, v110, v106, s70\n"
  "v_mfma_f32_16x16x32_bf16 a[76:79], v[160:163], v[136:139], a[76:79]\n"
  "v_perm_b32 v158, v118, v114, s70\n"
  "v_perm_b32 v159, v126, v122, s70\n"
  "v_mfma_f32_16x16x32_bf16 a[108:111], v[160:163], v[140:143], a[108:111]\n"
  "v_mfma_f32_16x16x32_bf16 a[140:143], v[160:163], v[144:147], a[140:143]\n"
  "v_mfma_f32_16x16x32_bf16 a[172:175], v[160:163], v[148:151], a[172:175]\n"
  "buffer_load_dwordx4 v[76:79], v244, s[40:43], s61 offen offset:3456\n"
  "v_mfma_f32_16x16x32_bf16 a[204:207], v[160:163], v[152:155], a[204:207]\n"
  "v_mfma_f32_16x16x32_bf16 a[16:19], v[156:159], v[128:131], a[16:19]\n"
  "v_mfma_f32_16x16x32_bf16 a[48:51], v[156:159], v[132:135], a[48:51]\n"
  "v_perm_b32 v160, v102, v98, s71\n"
  "v_perm_b32 v161, v110, v106, s71\n"
  "v_mfma_f32_16x16x32_bf16 a[80:83], v[156:159], v[136:139], a[80:83]\n"
  "v_perm_b32 v162, v118, v114, s71\n"
  "v_perm_b32 v163, v126, v122, s71\n"
  "v_mfma_f32_16x16x32_bf16 a[112:115], v[156:159], v[140:143], a[112:115]\n"
  "buffer_load_dwordx4 v[80:83], v245, s[40:43], s61 offen offset:0\n"
  "v_mfma_f32_16x16x32_bf16 a[144:147], v[156:159], v[144:147], a[144:147]\n"
  "v_mfma_f32_16x16x32_bf16 a[176:179], v[156:159], v[148:151], a[176:179]\n"
  "v_mfma_f32_16x16x32_bf16 a[208:211], v[156:159], v[152:155], a[208:211]\n"
  "v_mfma_f32_16x16x32_bf16 a[20:23], v[160:163], v[128:131], a[20:23]\n"
  "buffer_load_dwordx4 v[84:87], v245, s[40:43], s61 offen offset:1152\n"
  "v_mfma_f32_16x16x32_bf16 a[52:55], v[160:163], v[132:135], a[52:55]\n"
  "v_perm_b32 v156, v103, v99, s70\n"
  "v_perm_b32 v157, v111, v107, s70\n"
  "v_mfma_f32_16x16x32_bf16 a[84:87], v[160:163], v[136:139], a[84:87]\n"
  "v_perm_b32 v158, v119, v115, s70\n"
  "v_perm_b32 v159, v127, v123, s70\n"
  "v_mfma_f32_16x16x32_bf16 a[116:119], v[160:163], v[140:143], a[116:119]\n"
  "s_add_u32 s63, s63, 1\n"
  "s_xor_b32 s87, s63, s79\n"
  "s_and_b32 s86, s87, 1\n"
  "v_mfma_f32_16x16x32_bf16 a[148:151], v[160:163], v[144:147], a[148:151]\n"
  "s_mul_i32 s86, s86, 8256\n"
  "s_bfe_u32 s87, s87, 0x20001\n"
  "s_mul_i32 s87, s87, 33024\n"
  "v_mfma_f32_16x16x32_bf16 a[180:183], v[160:163], v[148:151], a[180:183]\n"
  "buffer_load_dwordx4 v[88:91], v245, s[40:43], s61 offen offset:2304\n"
  "v_mfma_f32_16x16x32_bf16 a[212:215], v[160:163], v[152:155], a[212:215]\n"
  "s_add_u32 s87, s87, s86\n"
  "s_add_u32 s87, s87, s76\n"
  "v_add_u32 v240, s87, v243\n"
  "v_mfma_f32_16x16x32_bf16 a[24:27], v[156:159], v[128:131], a[24:27]\n"
  "v_mfma_f32_16x16x32_bf16 a[56:59], v[156:159], v[132:135], a[56:59]\n"
  "v_perm_b32 v160, v103, v99, s71\n"
  "v_perm_b32 v161, v111, v107, s71\n"
  "v_mfma_f32_16x16x32_bf16 a[88:91], v[156:159], v[136:139], a[88:91]\n"
  "v_perm_b32 v162, v119, v115, s71\n"
  "v_perm_b32 v163, v127, v123, s71\n"
  "v_mfma_f32_16x16x32_bf16 a[120:123], v[156:159], v[140:143], a[120:123]\n"
  "buffer_load_dwordx4 v[92:95], v245, s[40:43], s61 offen offset:3456\n"
  "v_mfma_f32_16x16x32_bf16 a[152:155], v[156:159], v[144:147], a[152:155]\n"
  "ds_read_b32 a252, v248 offset:8224\n"
  "ds_read_b32 a253, v248 offset:16480\n"
  "v_mfma_f32_16x16x32_bf16 a[184:187], v[156:159], v[148:151], a[184:187]\n"
  "ds_read_b32 a254, v248 offset:24736\n"
  "ds_read_b32 a255, v248 offset:32992\n"
  "v_mfma_f32_16x16x32_bf16 a[216:219], v[156:159], v[152:155], a[216:219]\n"
  "s_add_u32 m0, s72, 73216\n"
  "v_mfma_f32_16x16x32_bf16 a[28:31], v[160:163], v[128:131], a[28:31]\n"
  "buffer_load_dwordx4 v246, s[40:43], s64 offen lds\n"
  "v_mfma_f32_16x16x32_bf16 a[60:63], v[160:163], v[132:135], a[60:63]\n"
  "v_mfma_f32_16x16x32_bf16 a[92:95], v[160:163], v[136:139], a[92:95]\n"
  "s_waitcnt vmcnt(18)\n"
  "ds_read_b64_tr_b16 v[164:165], v247 offset:7168\n"
  "ds_read_b64_tr_b16 v[166:167], v247 offset:7680\n"
  "v_mfma_f32_16x16x32_bf16 a[124:127], v[160:163], v[140:143], a[124:127]\n"
  "v_mfma_f32_16x16x32_bf16 a[156:159], v[160:163], v[144:147], a[156:159]\n"
  "v_perm_b32 v156, v4, v0, s70\n"
  "v_perm_b32 v157, v12, v8, s70\n"
  "v_mfma_f32_16x16x32_bf16 a[188:191], v[160:163], v[148:151], a[188:191]\n"
  "v_perm_b32 v158, v20, v16, s70\n"
  "v_perm_b32 v159, v28, v24, s70\n"
  "v_mfma_f32_16x16x32_bf16 a[220:223], v[160:163], v[152:155], a[220:223]\n"
  "s_add_u32 s60, s60, 1\n"
  "s_cmp_lt_u32 s60, s78\n"
  "v_mfma_f32_16x16x32_bf16 a[224:227], v[168:171], v[128:131], a[224:227]\n"
  "ds_read_b128 v[128:131], v240 offset:0\n"
  "v_mfma_f32_16x16x32_bf16 a[228:231], v[168:171], v[132:135], a[228:231]\n"
  "ds_read_b128 v[132:135], v240 offset:1024\n"
  "v_mfma_f32_16x16x32_bf16 a[232:235], v[168:171], v[136:139], a[232:235]\n"
  "ds_read_b128 v[136:139], v240 offset:2048\n"
  "v_mfma_f32_16x16x32_bf16 a[236:239], v[168:171], v[140:143], a[236:239]\n"
  "ds_read_b128 v[140:143], v240 offset:3072\n"
  "v_mfma_f32_16x16x32_bf16 a[240:243], v[168:171], v[144:147], a[240:243]\n"
  "ds_read_b128 v[144:147], v240 offset:4096\n"
  "v_mfma_f32_16x16x32_bf16 a[244:247], v[168:171], v[148:151], a[244:247]\n"
  "ds_read_b128 v[148:151], v240 offset:5120\n"
  "v_mfma_f32_16x16x32_bf16 a[248:251], v[168:171], v[152:155], a[248:251]\n"
  "ds_read_b128 v[152:155], v240 offset:6144\n"
  "s_waitcnt lgkmcnt(6)\n"
  "s_cbranch_scc1 L_op\n"
  "s_waitcnt lgkmcnt(0)\n"
  "s_cmp_eq_u32 s77, 2\n"
  "s_cbranch_scc1 L_T2\n"
  "s_sub_u32 s63, s63, 1\n"
  "s_cmp_eq_u32 s77, 1\n"
  "s_cbranch_scc1 L_ET1\n"
  "s_mul_i32 s86, s74, 15360\n"
  "s_add_u32 s86, s86, 132096\n"
  "v_add_u32 v240, s86, v243\n"
  "v_accvgpr_read_b32 v128, a0\n"
  "v_accvgpr_read_b32 v129, a1\n"
  "v_accvgpr_read_b32 v130, a2\n"
  "v_accvgpr_read_b32 v131, a3\n"
  "v_accvgpr_read_b32 v132, a4\n"
  "v_accvgpr_read_b32 v133, a5\n"
  "v_accvgpr_read_b32 v134, a6\n"
  "v_accvgpr_read_b32 v135, a7\n"
  "v_cvt_pk_bf16_f32 v144, v128, v129\n"
  "v_cvt_pk_bf16_f32 v145, v130, v131\n"
  "v_cvt_pk_bf16_f32 v146, v132, v133\n"
  "v_cvt_pk_bf16_f32 v147, v134, v135\n"
  "ds_write_b128 v240, v[144:147] offset:0\n"
  "v_accvgpr_read_b32 v136, a8\n"
  "v_accvgpr_read_b32 v137, a9\n"
  "v_accvgpr_read_b32 v138, a10\n"
  "v_accvgpr_read_b32 v139, a11\n"
  "v_accvgpr_read_b32 v140, a12\n"
  "v_accvgpr_read_b32 v141, a13\n"
  "v_accvgpr_read_b32 v142, a14\n"
  "v_accvgpr_read_b32 v143, a15\n"
  "v_mfma_f32_32x32x16_bf16 a[0:15], v[248:251], v[248:251], 0\n"
  "v_cvt_pk_bf16_f32 v148, v136, v137\n"
  "v_cvt_pk_bf16_f32 v149, v138, v139\n"
  "v_cvt_pk_bf16_f32 v150, v140, v141\n"
  "v_cvt_pk_bf16_f32 v151, v142, v143\n"
  "ds_write_b128 v240, v[148:151] offset:1024\n"
  "v_accvgpr_read_b32 v128, a16\n"
  "v_accvgpr_read_b32 v129, a17\n"
  "v_accvgpr_read_b32 v130, a18\n"
  "v_accvgpr_read_b32 v131, a19\n"
  "v_accvgpr_read_b32 v132, a20\n"
  "v_accvgpr_read_b32 v133, a21\n"
  "v_accvgpr_read_b32 v134, a22\n"
  "v_accvgpr_read_b32 v135, a23\n"
  "v_cvt_pk_bf16_f32 v152, v128, v129\n"
  "v_cvt_pk_bf16_f32 v153, v130, v131\n"
  "v_cvt_pk_bf16_f32 v154, v132, v133\n"
  "v_cvt_pk_bf16_f32 v155, v134, v135\n"
  "ds_write_b128 v240, v[152:155] offset:2048\n"
  "v_accvgpr_read_b32 v136, a24\n"
  "v_accvgpr_read_b32 v137, a25\n"
  "v_accvgpr_read_b32 v138, a26\n"
  "v_accvgpr_read_b32 v139, a27\n"
  "v_accvgpr_read_b32 v140, a28\n"
  "v_accvgpr_read_b32 v141, a29\n"
  "v_accvgpr_read_b32 v142, a30\n"
  "v_accvgpr_read_b32 v143, a31\n"
  "v_mfma_f32_32x32x16_bf16 a[16:31], v[248:251], v[248:251], 0\n"
  "v_cvt_pk_bf16_f32 v144, v136, v137\n"
  "v_cvt_pk_bf16_f32 v145, v138, v139\n"
  "v_cvt_pk_bf16_f32 v146, v140, v141\n"
  "v_cvt_pk_bf16_f32 v147, v142, v143\n"
  "ds_write_b128 v240, v[144:147] offset:3072\n"
  "v_accvgpr_read_b32 v128, a32\n"
  "v_accvgpr_read_b32 v129, a33\n"
  "v_accvgpr_read_b32 v130, a34\n"
  "v_accvgpr_read_b32 v131, a35\n"
  "v_accvgpr_read_b32 v132, a36\n"
  "v_accvgpr_read_b32 v133, a37\n"
  "v_accvgpr_read_b32 v134, a38\n"
  "v_accvgpr_read_b32 v135, a39\n"
  "v_cvt_pk_bf16_f32 v148, v128, v129\n"
  "v_cvt_pk_bf16_f32 v149, v130, v131\n"
  "v_cvt_pk_bf16_f32 v150, v132, v133\n"
  "v_cvt_pk_bf16_f32 v151, v134, v135\n"
  "ds_write_b128 v240, v[148:151] offset:4096\n"
  "v_accvgpr_read_b32 v136, a40\n"
  "v_accvgpr_read_b32 v137, a41\n"
  "v_accvgpr_read_b32 v138, a42\n"
  "v_accvgpr_read_b32 v139, a43\n"
  "v_accvgpr_read_b32 v140, a44\n"
  "v_accvgpr_read_b32 v141, a45\n"
  "v_accvgpr_read_b32 v142, a46\n"
  "v_accvgpr_read_b32 v143, a47\n"
  "v_mfma_f32_32x32x16_bf16 a[32:47], v[248:251], v[248:251], 0\n"
  "v_cvt_pk_bf16_f32 v152, v136, v137\n"
  "v_cvt_pk_bf16_f32 v153, v138, v139\n"
  "v_cvt_pk_bf16_f32 v154, v140, v141\n"
  "v_cvt_pk_bf16_f32 v155, v142, v143\n"
  "ds_write_b128 v240, v[152:155] offset:5120\n"
  "v_accvgpr_read_b32 v128, a48\n"
  "v_accvgpr_read_b32 v129, a49\n"
  "v_accvgpr_read_b32 v130, a50\n"
  "v_accvgpr_read_b32 v131, a51\n"
  "v_accvgpr_read_b32 v132, a52\n"
  "v_accvgpr_read_b32 v133, a53\n"
  "v_accvgpr_read_b32 v134, a54\n"
  "v_accvgpr_read_b32 v135, a55\n"
  "v_cvt_pk_bf16_f32 v144, v128, v129\n"
  "v_cvt_pk_bf16_f32 v145, v130, v131\n"
  "v_cvt_pk_bf16_f32 v146, v132, v133\n"
  "v_cvt_pk_bf16_f32 v147, v134, v135\n"
  "ds_write_b128 v240, v[144:147] offset:6144\n"
  "v_accvgpr_read_b32 v136, a56\n"
  "v_accvgpr_read_b32 v137, a57\n"
  "v_accvgpr_read_b32 v138, a58\n"
  "v_accvgpr_read_b32 v139, a59\n"
  "v_accvgpr_read_b32 v140, a60\n"
  "v_accvgpr_read_b32 v141, a61\n"
  "v_accvgpr_read_b32 v142, a62\n"
  "v_accvgpr_read_b32 v143, a63\n"
  "v_mfma_f32_32x32x16_bf16 a[48:63], v[248:251], v[248:251], 0\n"
  "v_cvt_pk_bf16_f32 v148, v136, v137\n"
  "v_cvt_pk_bf16_f32 v149, v138, v139\n"
  "v_cvt_pk_bf16_f32 v150, v140, v141\n"
  "v_cvt_pk_bf16_f32 v151, v142, v143\n"
  "ds_write_b128 v240, v[148:151] offset:7168\n"
  "v_accvgpr_read_b32 v128, a64\n"
  "v_accvgpr_read_b32 v129, a65\n"
  "v_accvgpr_read_b32 v130, a66\n"
  "v_accvgpr_read_b32 v131, a67\n"
  "v_accvgpr_read_b32 v132, a68\n"
  "v_accvgpr_read_b32 v133, a69\n"
  "v_accvgpr_read_b32 v134, a70\n"
  "v_accvgpr_read_b32 v135, a71\n"
  "v_cvt_pk_bf16_f32 v152, v128, v129\n"
  "v_cvt_pk_bf16_f32 v153, v130, v131\n"
  "v_cvt_pk_bf16_f32 v154, v132, v133\n"
  "v_cvt_pk_bf16_f32 v155, v134, v135\n"
  "ds_write_b128 v240, v[152:155] offset:8192\n"
  "v_accvgpr_read_b32 v136, a72\n"
  "v_accvgpr_read_b32 v137, a73\n"
  "v_accvgpr_read_b32 v138, a74\n"
  "v_accvgpr_read_b32 v139, a75\n"
  "v_accvgpr_read_b32 v140, a76\n"
  "v_accvgpr_read_b32 v141, a77\n"
  "v_accvgpr_read_b32 v142, a78\n"
  "v_accvgpr_read_b32 v143, a79\n"
  "v_mfma_f32_32x32x16_bf16 a[64:79], v[248:251], v[248:251], 0\n"
  "v_cvt_pk_bf16_f32 v144, v136, v137\n"
  "v_cvt_pk_bf16_f32 v145, v138, v139\n"
  "v_cvt_pk_bf16_f32 v146, v140, v141\n"
  "v_cvt_pk_bf16_f32 v147, v142, v143\n"
  "ds_write_b128 v240, v[144:147] offset:9216\n"
  "v_accvgpr_read_b32 v128, a80\n"
  "v_accvgpr_read_b32 v129, a81\n"
  "v_accvgpr_read_b32 v130, a82\n"
  "v_accvgpr_read_b32 v131, a83\n"
  "v_accvgpr_read_b32 v132, a84\n"
  "v_accvgpr_read_b32 v133, a85\n"
  "v_accvgpr_read_b32 v134, a86\n"
  "v_accvgpr_read_b32 v135, a87\n"
  "v_cvt_pk_bf16_f32 v148, v128, v129\n"
  "v_cvt_pk_bf16_f32 v149, v130, v131\n"
  "v_cvt_pk_bf16_f32 v150, v132, v133\n"
  "v_cvt_pk_bf16_f32 v151, v134, v135\n"
  "ds_write_b128 v240, v[148:151] offset:10240\n"
  "v_accvgpr_read_b32 v136, a88\n"
  "v_accvgpr_read_b32 v137, a89\n"
  "v_accvgpr_read_b32 v138, a90\n"
  "v_accvgpr_read_b32 v139, a91\n"
  "v_accvgpr_read_b32 v140, a92\n"
  "v_accvgpr_read_b32 v141, a93\n"
  "v_accvgpr_read_b32 v142, a94\n"
  "v_accvgpr_read_b32 v143, a95\n"
  "v_mfma_f32_32x32x16_bf16 a[80:95], v[248:251], v[248:251], 0\n"
  "v_cvt_pk_bf16_f32 v152, v136, v137\n"
  "v_cvt_pk_bf16_f32 v153, v138, v139\n"
  "v_cvt_pk_bf16_f32 v154, v140, v141\n"
  "v_cvt_pk_bf16_f32 v155, v142, v143\n"
  "ds_write_b128 v240, v[152:155] offset:11264\n"
  "v_accvgpr_read_b32 v128, a96\n"
  "v_accvgpr_read_b32 v129, a97\n"
  "v_accvgpr_read_b32 v130, a98\n"
  "v_accvgpr_read_b32 v131, a99\n"
  "v_accvgpr_read_b32 v132, a100\n"
  "v_accvgpr_read_b32 v133, a101\n"
  "v_accvgpr_read_b32 v134, a102\n"
  "v_accvgpr_read_b32 v135, a103\n"
  "v_cvt_pk_bf16_f32 v144, v128, v129\n"
  "v_cvt_pk_bf16_f32 v145, v130, v131\n"
  "v_cvt_pk_bf16_f32 v146, v132, v133\n"
  "v_cvt_pk_bf16_f32 v147, v134, v135\n"
  "ds_write_b128 v240, v[144:147] offset:12288\n"
  "v_accvgpr_read_b32 v136, a104\n"
  "v_accvgpr_read_b32 v137, a105\n"
  "v_accvgpr_read_b32 v138, a106\n"
  "v_accvgpr_read_b32 v139, a107\n"
  "v_accvgpr_read_b32 v140, a108\n"
  "v_accvgpr_read_b32 v141, a109\n"
  "v_accvgpr_read_b32 v142, a110\n"
  "v_accvgpr_read_b32 v143, a111\n"
  "v_mfma_f32_32x32x16_bf16 a[96:111], v[248:251], v[248:251], 0\n"
  "v_cvt_pk_bf16_f32 v148, v136, v137\n"
  "v_cvt_pk_bf16_f32 v149, v138, v139\n"
  "v_cvt_pk_bf16_f32 v150, v140, v141\n"
  "v_cvt_pk_bf16_f32 v151, v142, v143\n"
  "ds_write_b128 v240, v[148:151] offset:13312\n"
  "v_accvgpr_read_b32 v128, a112\n"
  "v_accvgpr_read_b32 v129, a113\n"
  "v_accvgpr_read_b32 v130, a114\n"
  "v_accvgpr_read_b32 v131, a115\n"
  "v_accvgpr_read_b32 v132, a116\n"
  "v_accvgpr_read_b32 v133, a117\n"
  "v_accvgpr_read_b32 v134, a118\n"
  "v_accvgpr_read_b32 v135, a119\n"
  "v_cvt_pk_bf16_f32 v152, v128, v129\n"
  "v_cvt_pk_bf16_f32 v153, v130, v131\n"
  "v_cvt_pk_bf16_f32 v154, v132, v133\n"
  "v_cvt_pk_bf16_f32 v155, v134, v135\n"
  "ds_write_b128 v240, v[152:155] offset:14336\n"
  "v_accvgpr_read_b32 v136, a120\n"
  "v_accvgpr_read_b32 v137, a121\n"
  "v_accvgpr_read_b32 v138, a122\n"
  "v_accvgpr_read_b32 v139, a123\n"
  "v_accvgpr_read_b32 v140, a124\n"
  "v_accvgpr_read_b32 v141, a125\n"
  "v_accvgpr_read_b32 v142, a126\n"
  "v_accvgpr_read_b32 v143, a127\n"
  "v_mfma_f32_32x32x16_bf16 a[112:127], v[248:251], v[248:251], 0\n"
  "v_cvt_pk_bf16_f32 v172, v136, v137\n"
  "v_cvt_pk_bf16_f32 v173, v138, v139\n"
  "v_cvt_pk_bf16_f32 v174, v140, v141\n"
  "v_cvt_pk_bf16_f32 v175, v142, v143\n"
  "v_accvgpr_read_b32 v128, a128\n"
  "v_accvgpr_read_b32 v129, a129\n"
  "v_accvgpr_read_b32 v130, a130\n"
  "v_accvgpr_read_b32 v131, a131\n"
  "v_accvgpr_read_b32 v132, a132\n"
  "v_accvgpr_read_b32 v133, a133\n"
  "v_accvgpr_read_b32 v134, a134\n"
  "v_accvgpr_read_b32 v135, a135\n"
  "v_cvt_pk_bf16_f32 v176, v128, v129\n"
  "v_cvt_pk_bf16_f32 v177, v130, v131\n"
  "v_cvt_pk_bf16_f32 v178, v132, v133\n"
  "v_cvt_pk_bf16_f32 v179, v134, v135\n"
  "v_accvgpr_read_b32 v136, a136\n"
  "v_accvgpr_read_b32 v137, a137\n"
  "v_accvgpr_read_b32 v138, a138\n"
  "v_accvgpr_read_b32 v139, a139\n"
  "v_accvgpr_read_b32 v140, a140\n"
  "v_accvgpr_read_b32 v141, a141\n"
  "v_accvgpr_read_b32 v142, a142\n"
  "v_accvgpr_read_b32 v143, a143\n"
  "v_mfma_f32_32x32x16_bf16 a[128:143], v[248:251], v[248:251], 0\n"
  "v_cvt_pk_bf16_f32 v180, v136, v137\n"
  "v_cvt_pk_bf16_f32 v181, v138, v139\n"
  "v_cvt_pk_bf16_f32 v182, v140, v141\n"
  "v_cvt_pk_bf16_f32 v183, v142, v143\n"
  "v_accvgpr_read_b32 v128, a144\n"
  "v_accvgpr_read_b32 v129, a145\n"
  "v_accvgpr_read_b32 v130, a146\n"
  "v_accvgpr_read_b32 v131, a147\n"
  "v_accvgpr_read_b32 v132, a148\n"
  "v_accvgpr_read_b32 v133, a149\n"
  "v_accvgpr_read_b32 v134, a150\n"
  "v_accvgpr_read_b32 v135, a151\n"
  "v_cvt_pk_bf16_f32 v184, v128, v129\n"
  "v_cvt_pk_bf16_f32 v185, v130, v131\n"
  "v_cvt_pk_bf16_f32 v186, v132, v133\n"
  "v_cvt_pk_bf16_f32 v187, v134, v135\n"
  "v_accvgpr_read_b32 v136, a152\n"
  "v_accvgpr_read_b32 v137, a153\n"
  "v_accvgpr_read_b32 v138, a154\n"
  "v_accvgpr_read_b32 v139, a155\n"
  "v_accvgpr_read_b32 v140, a156\n"
  "v_accvgpr_read_b32 v141, a157\n"
  "v_accvgpr_read_b32 v142, a158\n"
  "v_accvgpr_read_b32 v143, a159\n"
  "v_mfma_f32_32x32x16_bf16 a[144:159], v[248:251], v[248:251], 0\n"
  "v_cvt_pk_bf16_f32 v188, v136, v137\n"
  "v_cvt_pk_bf16_f32 v189, v138, v139\n"
  "v_cvt_pk_bf16_f32 v190, v140, v141\n"
  "v_cvt_pk_bf16_f32 v191, v142, v143\n"
  "v_accvgpr_read_b32 v128, a160\n"
  "v_accvgpr_read_b32 v129, a161\n"
  "v_accvgpr_read_b32 v130, a162\n"
  "v_accvgpr_read_b32 v131, a163\n"
  "v_accvgpr_read_b32 v132, a164\n"
  "v_accvgpr_read_b32 v133, a165\n"
  "v_accvgpr_read_b32 v134, a166\n"
  "v_accvgpr_read_b32 v135, a167\n"
  "v_cvt_pk_bf16_f32 v192, v128, v129\n"
  "v_cvt_pk_bf16_f32 v193, v130, v131\n"
  "v_cvt_pk_bf16_f32 v194, v132, v133\n"
  "v_cvt_pk_bf16_f32 v195, v134, v135\n"
  "v_accvgpr_read_b32 v136, a168\n"
  "v_accvgpr_read_b32 v137, a169\n"
  "v_accvgpr_read_b32 v138, a170\n"
  "v_accvgpr_read_b32 v139, a171\n"
  "v_accvgpr_read_b32 v140, a172\n"
  "v_accvgpr_read_b32 v141, a173\n"
  "v_accvgpr_read_b32 v142, a174\n"
  "v_accvgpr_read_b32 v143, a175\n"
  "v_mfma_f32_32x32x16_bf16 a[160:175], v[248:251], v[248:251], 0\n"
  "v_cvt_pk_bf16_f32 v196, v136, v137\n"
  "v_cvt_pk_bf16_f32 v197, v138, v139\n"
  "v_cvt_pk_bf16_f32 v198, v140, v141\n"
  "v_cvt_pk_bf16_f32 v199, v142, v143\n"
  "v_accvgpr_read_b32 v128, a176\n"
  "v_accvgpr_read_b32 v129, a177\n"
  "v_accvgpr_read_b32 v130, a178\n"
  "v_accvgpr_read_b32 v131, a179\n"
  "v_accvgpr_read_b32 v132, a180\n"
  "v_accvgpr_read_b32 v133, a181\n"
  "v_accvgpr_read_b32 v134, a182\n"
  "v_accvgpr_read_b32 v135, a183\n"
  "v_cvt_pk_bf16_f32 v200, v128, v129\n"
  "v_cvt_pk_bf16_f32 v201, v130, v131\n"
  "v_cvt_pk_bf16_f32 v202, v132, v133\n"
  "v_cvt_pk_bf16_f32 v203, v134, v135\n"
  "v_accvgpr_read_b32 v136, a184\n"
  "v_accvgpr_read_b32 v137, a185\n"
  "v_accvgpr_read_b32 v138, a186\n"
  "v_accvgpr_read_b32 v139, a187\n"
  "v_accvgpr_read_b32 v140, a188\n"
  "v_accvgpr_read_b32 v141, a189\n"
  "v_accvgpr_read_b32 v142, a190\n"
  "v_accvgpr_read_b32 v143, a191\n"
  "v_mfma_f32_32x32x16_bf16 a[176:191], v[248:251], v[248:251], 0\n"
  "v_cvt_pk_bf16_f32 v204, v136, v137\n"
  "v_cvt_pk_bf16_f32 v205, v138, v139\n"
  "v_cvt_pk_bf16_f32 v206, v140, v141\n"
  "v_cvt_pk_bf16_f32 v207, v142, v143\n"
  "v_accvgpr_read_b32 v128, a192\n"
  "v_accvgpr_read_b32 v129, a193\n"
  "v_accvgpr_read_b32 v130, a194\n"
  "v_accvgpr_read_b32 v131, a195\n"
  "v_accvgpr_read_b32 v132, a196\n"
  "v_accvgpr_read_b32 v133, a197\n"
  "v_accvgpr_read_b32 v134, a198\n"
  "v_accvgpr_read_b32 v135, a199\n"
  "v_cvt_pk_bf16_f32 v208, v128, v129\n"
  "v_cvt_pk_bf16_f32 v209, v130, v131\n"
  "v_cvt_pk_bf16_f32 v210, v132, v133\n"
  "v_cvt_pk_bf16_f32 v211, v134, v135\n"
  "v_accvgpr_read_b32 v136, a200\n"
  "v_accvgpr_read_b32 v137, a201\n"
  "v_accvgpr_read_b32 v138, a202\n"
  "v_accvgpr_read_b32 v139, a203\n"
  "v_accvgpr_read_b32 v140, a204\n"
  "v_accvgpr_read_b32 v141, a205\n"
  "v_accvgpr_read_b32 v142, a206\n"
  "v_accvgpr_read_b32 v143, a207\n"
  "v_mfma_f32_32x32x16_bf16 a[192:207], v[248:251], v[248:251], 0\n"
  "v_cvt_pk_bf16_f32 v212, v136, v137\n"
  "v_cvt_pk_bf16_f32 v213, v138, v139\n"
  "v_cvt_pk_bf16_f32 v214, v140, v141\n"
  "v_cvt_pk_bf16_f32 v215, v142, v143\n"
  "v_accvgpr_read_b32 v128, a208\n"
  "v_accvgpr_read_b32 v129, a209\n"
  "v_accvgpr_read_b32 v130, a210\n"
  "v_accvgpr_read_b32 v131, a211\n"
  "v_accvgpr_read_b32 v132, a212\n"
  "v_accvgpr_read_b32 v133, a213\n"
  "v_accvgpr_read_b32 v134, a214\n"
  "v_accvgpr_read_b32 v135, a215\n"
  "v_cvt_pk_bf16_f32 v216, v128, v129\n"
  "v_cvt_pk_bf16_f32 v217, v130, v131\n"
  "v_cvt_pk_bf16_f32 v218, v132, v133\n"
  "v_cvt_pk_bf16_f32 v219, v134, v135\n"
  "v_accvgpr_read_b32 v136, a216\n"
  "v_accvgpr_read_b32 v137, a217\n"
  "v_accvgpr_read_b32 v138, a218\n"
  "v_accvgpr_read_b32 v139, a219\n"
  "v_accvgpr_read_b32 v140, a220\n"
  "v_accvgpr_read_b32 v141, a221\n"
  "v_accvgpr_read_b32 v142, a222\n"
  "v_accvgpr_read_b32 v143, a223\n"
  "v_mfma_f32_32x32x16_bf16 a[208:223], v[248:251], v[248:251], 0\n"
  "v_cvt_pk_bf16_f32 v220, v136, v137\n"
  "v_cvt_pk_bf16_f32 v221, v138, v139\n"
  "v_cvt_pk_bf16_f32 v222, v140, v141\n"
  "v_cvt_pk_bf16_f32 v223, v142, v143\n"
  "v_accvgpr_read_b32 v128, a224\n"
  "v_accvgpr_read_b32 v129, a225\n"
  "v_accvgpr_read_b32 v130, a226\n"
  "v_accvgpr_read_b32 v131, a227\n"
  "v_accvgpr_read_b32 v132, a228\n"
  "v_accvgpr_read_b32 v133, a229\n"
  "v_accvgpr_read_b32 v134, a230\n"
  "v_accvgpr_read_b32 v135, a231\n"
  "v_cvt_pk_bf16_f32 v224, v128, v129\n"
  "v_cvt_pk_bf16_f32 v225, v130, v131\n"
  "v_cvt_pk_bf16_f32 v226, v132, v133\n"
  "v_cvt_pk_bf16_f32 v227, v134, v135\n"
  "v_accvgpr_read_b32 v136, a232\n"
  "v_accvgpr_read_b32 v137, a233\n"
  "v_accvgpr_read_b32 v138, a234\n"
  "v_accvgpr_read_b32 v139, a235\n"
  "v_accvgpr_read_b32 v140, a236\n"
  "v_accvgpr_read_b32 v141, a237\n"
  "v_accvgpr_read_b32 v142, a238\n"
  "v_accvgpr_read_b32 v143, a239\n"
  "v_mfma_f32_32x32x16_bf16 a[224:239], v[248:251], v[248:251], 0\n"
  "v_cvt_pk_bf16_f32 v228, v136, v137\n"
  "v_cvt_pk_bf16_f32 v229, v138, v139\n"
  "v_cvt_pk_bf16_f32 v230, v140, v141\n"
  "v_cvt_pk_bf16_f32 v231, v142, v143\n"
  "v_accvgpr_read_b32 v128, a240\n"
  "v_accvgpr_read_b32 v129, a241\n"
  "v_accvgpr_read_b32 v130, a242\n"
  "v_accvgpr_read_b32 v131, a243\n"
  "v_accvgpr_read_b32 v132, a244\n"
  "v_accvgpr_read_b32 v133, a245\n"
  "v_accvgpr_read_b32 v134, a246\n"
  "v_accvgpr_read_b32 v135, a247\n"
  "v_cvt_pk_bf16_f32 v232, v128, v129\n"
  "v_cvt_pk_bf16_f32 v233, v130, v131\n"
  "v_cvt_pk_bf16_f32 v234, v132, v133\n"
  "v_cvt_pk_bf16_f32 v235, v134, v135\n"
  "v_accvgpr_read_b32 v136, a248\n"
  "v_accvgpr_read_b32 v137, a249\n"
  "v_accvgpr_read_b32 v138, a250\n"
  "v_accvgpr_read_b32 v139, a251\n"
  "v_accvgpr_read_b32 v140, a252\n"
  "v_accvgpr_read_b32 v141, a253\n"
  "v_accvgpr_read_b32 v142, a254\n"
  "v_accvgpr_read_b32 v143, a255\n"
  "v_mfma_f32_32x32x16_bf16 a[240:255], v[248:251], v[248:251], 0\n"
  "v_cvt_pk_bf16_f32 v236, v136, v137\n"
  "v_cvt_pk_bf16_f32 v237, v138, v139\n"
  "v_cvt_pk_bf16_f32 v238, v140, v141\n"
  "v_cvt_pk_bf16_f32 v239, v142, v143\n"
  "s_lshl_b32 s68, s74, 8\n"
  "s_add_u32 s68, s68, 294912\n"
  "s_lshl_b32 s69, s74, 5\n"
  "s_add_u32 s69, s69, 294912\n"
  "s_add_u32 s69, s69, 1024\n"
  "s_mov_b32 s77, 1\n"
  "s_waitcnt lgkmcnt(0)\n"
  "s_branch L_opstart\n"
  "L_ET1:\n"
  "v_mov_b32 v128, s72\n"
  "v_mov_b32 v129, 3\n"
  "ds_write_b32 v128, v129 offset:8224\n"
  "L_es2:\n"
  "ds_read_b32 v130, v248 offset:8224\n"
  "ds_read_b32 v131, v248 offset:16480\n"
  "s_waitcnt lgkmcnt(0)\n"
  "v_min_u32 v130, v130, v131\n"
  "v_cmp_gt_u32 vcc, 3, v130\n"
  "s_cbranch_vccnz L_es2\n"
  "ds_write_b128 v241, v[172:175] offset:0\n"
  "ds_write_b128 v241, v[176:179] offset:1024\n"
  "ds_write_b128 v241, v[180:183] offset:2048\n"
  "ds_write_b128 v241, v[184:187] offset:3072\n"
  "ds_write_b128 v241, v[188:191] offset:4096\n"
  "ds_write_b128 v241, v[192:195] offset:5120\n"
  "ds_write_b128 v241, v[196:199] offset:6144\n"
  "ds_write_b128 v241, v[200:203] offset:33024\n"
  "ds_write_b128 v241, v[204:207] offset:34048\n"
  "ds_write_b128 v241, v[208:211] offset:35072\n"
  "ds_write_b128 v241, v[212:215] offset:36096\n"
  "ds_write_b128 v241, v[216:219] offset:37120\n"
  "ds_write_b128 v241, v[220:223] offset:38144\n"
  "ds_write_b128 v241, v[224:227] offset:39168\n"
  "ds_write_b128 v242, v[228:231] offset:0\n"
  "ds_write_b128 v242, v[232:235] offset:1024\n"
  "ds_write_b128 v242, v[236:239] offset:2048\n"
  "v_mov_b32 v132, s72\n"
  "v_mov_b32 v133, 4\n"
  "ds_write_b32 v132, v133 offset:8224\n"
  "L_T1:\n"
  "L_tw:\n"
  "ds_read_b32 v134, v248 offset:24736\n"
  "ds_read_b32 v135, v248 offset:32992\n"
  "s_waitcnt lgkmcnt(0)\n"
  "v_min_u32 v134, v134, v135\n"
  "v_cmp_gt_u32 vcc, 1, v134\n"
  "s_cbranch_vccnz L_tw\n"
  "s_mov_b32 s77, 2\n"
  "s_mov_b32 s78, 1\n"
  "s_mov_b32 s76, 16512\n"
  "s_branch L_opstart\n"
  "L_T2:\n"
  "v_accvgpr_read_b32 v232, a252\n"
  "v_accvgpr_read_b32 v233, a253\n"
  "v_accvgpr_read_b32 v234, a254\n"
  "v_accvgpr_read_b32 v235, a255\n"
  "v_min_u32 v232, v232, v233\n"
  "v_lshrrev_b32 v232, 1, v232\n"
  "v_min3_u32 v232, v232, v234, v235\n"
  "v_cmp_gt_u32 vcc, 2, v232\n"
  "s_cbranch_vccnz L_T2s\n"
  "L_T2r:\n"
  "s_bitcmp1_b32 s74, 1\n"
  "s_cbranch_scc0 L_F\n"
  "s_mul_i32 s86, s74, 15360\n"
  "s_add_u32 s86, s86, 101376\n"
  "v_add_u32 v251, s86, v243\n"
  "v_subrev_u32 v249, 16512, v241\n"
  "v_subrev_u32 v250, 16512, v242\n"
  "ds_read_b128 v[172:175], v251 offset:0\n"
  "ds_read_b128 v[176:179], v251 offset:1024\n"
  "ds_read_b128 v[180:183], v251 offset:2048\n"
  "ds_read_b128 v[184:187], v251 offset:3072\n"
  "ds_read_b128 v[188:191], v251 offset:4096\n"
  "ds_read_b128 v[192:195], v251 offset:5120\n"
  "ds_read_b128 v[196:199], v251 offset:6144\n"
  "ds_read_b128 v[200:203], v251 offset:7168\n"
  "ds_read_b128 v[216:219], v249 offset:39168\n"
  "v_and_b32 v213, 1, v252\n"
  "v_lshlrev_b32 v213, 4, v213\n"
  "v_mov_b32 v214, 0x3f80\n"
  "v_lshlrev_b32 v213, v213, v214\n"
  "v_bfe_u32 v214, v252, 1, 3\n"
  "v_lshrrev_b32 v215, 4, v252\n"
  "v_lshlrev_b32 v215, 1, v215\n"
  "v_sub_u32 v214, v214, v215\n"
  "v_cmp_eq_u32 vcc, 0, v214\n"
  "v_mov_b32 v204, 0\n"
  "v_mov_b32 v205, 0\n"
  "v_cndmask_b32 v206, 0, v213, vcc\n"
  "v_cmp_eq_u32 vcc, 1, v214\n"
  "v_mov_b32 v208, 0\n"
  "v_mov_b32 v209, 0\n"
  "v_cndmask_b32 v207, 0, v213, vcc\n"
  "s_waitcnt lgkmcnt(8)\n"
  "s_waitcnt lgkmcnt(0)\n"
  "v_mfma_f32_16x16x32_bf16 a[0:3], v[206:209], v[172:175], a[0:3]\n"
  "v_mfma_f32_16x16x32_bf16 a[4:7], v[204:207], v[172:175], a[4:7]\n"
  "v_mfma_f32_16x16x32_bf16 a[8:11], v[206:209], v[176:179], a[8:11]\n"
  "v_mfma_f32_16x16x32_bf16 a[12:15], v[204:207], v[176:179], a[12:15]\n"
  "v_mfma_f32_16x16x32_bf16 a[16:19], v[206:209], v[180:183], a[16:19]\n"
  "v_mfma_f32_16x16x32_bf16 a[20:23], v[204:207], v[180:183], a[20:23]\n"
  "v_mfma_f32_16x16x32_bf16 a[24:27], v[206:209], v[184:187], a[24:27]\n"
  "v_mfma_f32_16x16x32_bf16 a[28:31], v[204:207], v[184:187], a[28:31]\n"
  "v_mfma_f32_16x16x32_bf16 a[32:35], v[206:209], v[188:191], a[32:35]\n"
  "v_mfma_f32_16x16x32_bf16 a[36:39], v[204:207], v[188:191], a[36:39]\n"
  "v_mfma_f32_16x16x32_bf16 a[40:43], v[206:209], v[192:195], a[40:43]\n"
  "v_mfma_f32_16x16x32_bf16 a[44:47], v[204:207], v[192:195], a[44:47]\n"
  "v_mfma_f32_16x16x32_bf16 a[48:51], v[206:209], v[196:199], a[48:51]\n"
  "v_mfma_f32_16x16x32_bf16 a[52:55], v[204:207], v[196:199], a[52:55]\n"
  "v_mfma_f32_16x16x32_bf16 a[56:59], v[206:209], v[200:203], a[56:59]\n"
  "v_mfma_f32_16x16x32_bf16 a[60:63], v[204:207], v[200:203], a[60:63]\n"
  "v_mfma_f32_16x16x32_bf16 a[224:227], v[206:209], v[216:219], a[224:227]\n"
  "v_mfma_f32_16x16x32_bf16 a[228:231], v[204:207], v[216:219], a[228:231]\n"
  "L_F:\n"
  "s_add_u32 s62, s62, 1\n"
  "s_xor_b32 s64, s62, s79\n"
  "s_and_b32 s65, s64, 1\n"
  "s_mul_i32 s65, s65, 147456\n"
  "s_bfe_u32 s61, s64, 0x20001\n"
  "s_mul_i32 s61, s61, 36864\n"
  "s_add_u32 s65, s61, s65\n"
  "s_add_u32 s61, s65, s66\n"
  "s_add_u32 s64, s65, s67\n"
  "buffer_load_dwordx4 v[96:99], v244, s[40:43], s61 offen offset:0\n"
  "buffer_load_dwordx4 v[100:103], v244, s[40:43], s61 offen offset:1152\n"
  "buffer_load_dwordx4 v[104:107], v244, s[40:43], s61 offen offset:2304\n"
  "buffer_load_dwordx4 v[108:111], v244, s[40:43], s61 offen offset:3456\n"
  "buffer_load_dwordx4 v[112:115], v245, s[40:43], s61 offen offset:0\n"
  "buffer_load_dwordx4 v[116:119], v245, s[40:43], s61 offen offset:1152\n"
  "buffer_load_dwordx4 v[120:123], v245, s[40:43], s61 offen offset:2304\n"
  "buffer_load_dwordx4 v[124:127], v245, s[40:43], s61 offen offset:3456\n"
  "s_add_u32 m0, s72, 106240\n"
  "s_nop 0\n"
  "buffer_load_dwordx4 v246, s[40:43], s64 offen lds\n"
  "v_perm_b32 v172, v4, v0, s70\n"
  "v_perm_b32 v173, v12, v8, s70\n"
  "v_perm_b32 v174, v20, v16, s70\n"
  "v_perm_b32 v175, v28, v24, s70\n"
  "s_add_u32 s63, s63, 1\n"
  "s_xor_b32 s87, s63, s79\n"
  "s_and_b32 s86, s87, 1\n"
  "s_mul_i32 s86, s86, 8256\n"
  "s_bfe_u32 s87, s87, 0x20001\n"
  "s_mul_i32 s87, s87, 33024\n"
  "s_add_u32 s87, s87, s86\n"
  "s_add_u32 s87, s87, s76\n"
  "v_add_u32 v240, s87, v243\n"
  "s_add_u32 s63, s63, 1\n"
  "s_xor_b32 s87, s63, s79\n"
  "s_and_b32 s86, s87, 1\n"
  "s_mul_i32 s86, s86, 8256\n"
  "s_bfe_u32 s87, s87, 0x20001\n"
  "s_mul_i32 s87, s87, 33024\n"
  "s_add_u32 s87, s87, s86\n"
  "s_add_u32 s87, s87, s76\n"
  "v_add_u32 v241, s87, v243\n"
  "s_add_u32 s63, s63, 1\n"
  "s_xor_b32 s87, s63, s79\n"
  "s_and_b32 s86, s87, 1\n"
  "s_mul_i32 s86, s86, 8256\n"
  "s_bfe_u32 s87, s87, 0x20001\n"
  "s_mul_i32 s87, s87, 33024\n"
  "s_add_u32 s87, s87, s86\n"
  "s_add_u32 s87, s87, s76\n"
  "v_add_u32 v242, s87, v243\n"
  "s_mul_i32 s86, s97, 4608\n"
  "v_mfma_f32_16x16x32_bf16 a[224:227], v[164:167], v[128:131], a[224:227]\n"
  "v_perm_b32 v176, v4, v0, s71\n"
  "v_perm_b32 v177, v12, v8, s71\n"
  "v_mfma_f32_16x16x32_bf16 a[228:231], v[164:167], v[132:135], a[228:231]\n"
  "v_perm_b32 v178, v20, v16, s71\n"
  "v_perm_b32 v179, v28, v24, s71\n"
  "v_mfma_f32_16x16x32_bf16 a[0:3], v[172:175], v[128:131], a[0:3]\n"
  "v_perm_b32 v180, v5, v1, s70\n"
  "v_perm_b32 v181, v13, v9, s70\n"
  "v_mfma_f32_16x16x32_bf16 a[32:35], v[172:175], v[132:135], a[32:35]\n"
  "v_perm_b32 v182, v21, v17, s70\n"
  "v_perm_b32 v183, v29, v25, s70\n"
  "v_mfma_f32_16x16x32_bf16 a[4:7], v[176:179], v[128:131], a[4:7]\n"
  "ds_read_b128 v[208:211], v240 offset:0\n"
  "ds_read_b128 v[220:223], v240 offset:1024\n"
  "v_perm_b32 v184, v5, v1, s71\n"
  "v_perm_b32 v185, v13, v9, s71\n"
  "v_mfma_f32_16x16x32_bf16 a[36:39], v[176:179], v[132:135], a[36:39]\n"
  "v_perm_b32 v186, v21, v17, s71\n"
  "v_perm_b32 v187, v29, v25, s71\n"
  "v_mfma_f32_16x16x32_bf16 a[8:11], v[180:183], v[128:131], a[8:11]\n"
  "v_perm_b32 v188, v6, v2, s70\n"
  "v_perm_b32 v189, v14, v10, s70\n"
  "v_mfma_f32_16x16x32_bf16 a[40:43], v[180:183], v[132:135], a[40:43]\n"
  "v_perm_b32 v190, v22, v18, s70\n"
  "v_perm_b32 v191, v30, v26, s70\n"
  "v_mfma_f32_16x16x32_bf16 a[12:15], v[184:187], v[128:131], a[12:15]\n"
  "v_perm_b32 v192, v6, v2, s71\n"
  "v_perm_b32 v193, v14, v10, s71\n"
  "v_mfma_f32_16x16x32_bf16 a[44:47], v[184:187], v[132:135], a[44:47]\n"
  "v_perm_b32 v194, v22, v18, s71\n"
  "v_perm_b32 v195, v30, v26, s71\n"
  "v_mfma_f32_16x16x32_bf16 a[16:19], v[188:191], v[128:131], a[16:19]\n"
  "v_perm_b32 v196, v7, v3, s70\n"
  "v_perm_b32 v197, v15, v11, s70\n"
  "v_mfma_f32_16x16x32_bf16 a[48:51], v[188:191], v[132:135], a[48:51]\n"
  "v_perm_b32 v198, v23, v19, s70\n"
  "v_perm_b32 v199, v31, v27, s70\n"
  "v_mfma_f32_16x16x32_bf16 a[20:23], v[192:195], v[128:131], a[20:23]\n"
  "v_perm_b32 v200, v7, v3, s71\n"
  "v_perm_b32 v201, v15, v11, s71\n"
  "v_mfma_f32_16x16x32_bf16 a[52:55], v[192:195], v[132:135], a[52:55]\n"
  "s_waitcnt vmcnt(18)\n"
  "ds_read_b64_tr_b16 v[168:169], v247 offset:40192\n"
  "ds_read_b64_tr_b16 v[170:171], v247 offset:40704\n"
  "v_perm_b32 v202, v23, v19, s71\n"
  "v_perm_b32 v203, v31, v27, s71\n"
  "v_mfma_f32_16x16x32_bf16 a[24:27], v[196:199], v[128:131], a[24:27]\n"
  "v_mfma_f32_16x16x32_bf16 a[56:59], v[196:199], v[132:135], a[56:59]\n"
  "v_perm_b32 v0, v36, v32, s70\n"
  "v_perm_b32 v1, v44, v40, s70\n"
  "v_mfma_f32_16x16x32_bf16 a[28:31], v[200:203], v[128:131], a[28:31]\n"
  "v_perm_b32 v2, v52, v48, s70\n"
  "v_perm_b32 v3, v60, v56, s70\n"
  "v_mfma_f32_16x16x32_bf16 a[60:63], v[200:203], v[132:135], a[60:63]\n"
  "s_waitcnt lgkmcnt(0)\n"
  "v_mfma_f32_16x16x32_bf16 a[224:227], v[168:171], v[208:211], a[224:227]\n"
  "v_perm_b32 v4, v36, v32, s71\n"
  "v_perm_b32 v5, v44, v40, s71\n"
  "v_mfma_f32_16x16x32_bf16 a[228:231], v[168:171], v[220:223], a[228:231]\n"
  "v_perm_b32 v6, v52, v48, s71\n"
  "v_perm_b32 v7, v60, v56, s71\n"
  "v_mfma_f32_16x16x32_bf16 a[0:3], v[0:3], v[208:211], a[0:3]\n"
  "v_perm_b32 v8, v37, v33, s70\n"
  "v_perm_b32 v9, v45, v41, s70\n"
  "v_mfma_f32_16x16x32_bf16 a[32:35], v[0:3], v[220:223], a[32:35]\n"
  "v_perm_b32 v10, v53, v49, s70\n"
  "v_perm_b32 v11, v61, v57, s70\n"
  "v_mfma_f32_16x16x32_bf16 a[4:7], v[4:7], v[208:211], a[4:7]\n"
  "ds_read_b128 v[212:215], v241 offset:0\n"
  "ds_read_b128 v[224:227], v241 offset:1024\n"
  "v_perm_b32 v12, v37, v33, s71\n"
  "v_perm_b32 v13, v45, v41, s71\n"
  "v_mfma_f32_16x16x32_bf16 a[36:39], v[4:7], v[220:223], a[36:39]\n"
  "v_perm_b32 v14, v53, v49, s71\n"
  "v_perm_b32 v15, v61, v57, s71\n"
  "v_mfma_f32_16x16x32_bf16 a[8:11], v[8:11], v[208:211], a[8:11]\n"
  "v_perm_b32 v16, v38, v34, s70\n"
  "v_perm_b32 v17, v46, v42, s70\n"
  "v_mfma_f32_16x16x32_bf16 a[40:43], v[8:11], v[220:223], a[40:43]\n"
  "v_perm_b32 v18, v54, v50, s70\n"
  "v_perm_b32 v19, v62, v58, s70\n"
  "v_mfma_f32_16x16x32_bf16 a[12:15], v[12:15], v[208:211], a[12:15]\n"
  "v_perm_b32 v20, v38, v34, s71\n"
  "v_perm_b32 v21, v46, v42, s71\n"
  "v_mfma_f32_16x16x32_bf16 a[44:47], v[12:15], v[220:223], a[44:47]\n"
  "v_perm_b32 v22, v54, v50, s71\n"
  "v_perm_b32 v23, v62, v58, s71\n"
  "v_mfma_f32_16x16x32_bf16 a[16:19], v[16:19], v[208:211], a[16:19]\n"
  "v_perm_b32 v24, v39, v35, s70\n"
  "v_perm_b32 v25, v47, v43, s70\n"
  "v_mfma_f32_16x16x32_bf16 a[48:51], v[16:19], v[220:223], a[48:51]\n"
  "v_perm_b32 v26, v55, v51, s70\n"
  "v_perm_b32 v27, v63, v59, s70\n"
  "v_mfma_f32_16x16x32_bf16 a[20:23], v[20:23], v[208:211], a[20:23]\n"
  "v_perm_b32 v28, v39, v35, s71\n"
  "v_perm_b32 v29, v47, v43, s71\n"
  "v_mfma_f32_16x16x32_bf16 a[52:55], v[20:23], v[220:223], a[52:55]\n"
  "s_waitcnt vmcnt(9)\n"
  "ds_read_b64_tr_b16 v[156:157], v253 offset:7168\n"
  "ds_read_b64_tr_b16 v[158:159], v253 offset:7680\n"
  "v_perm_b32 v30, v55, v51, s71\n"
  "v_perm_b32 v31, v63, v59, s71\n"
  "v_mfma_f32_16x16x32_bf16 a[24:27], v[24:27], v[208:211], a[24:27]\n"
  "v_mfma_f32_16x16x32_bf16 a[56:59], v[24:27], v[220:223], a[56:59]\n"
  "v_perm_b32 v32, v68, v64, s70\n"
  "v_perm_b32 v33, v76, v72, s70\n"
  "v_mfma_f32_16x16x32_bf16 a[28:31], v[28:31], v[208:211], a[28:31]\n"
  "v_perm_b32 v34, v84, v80, s70\n"
  "v_perm_b32 v35, v92, v88, s70\n"
  "v_mfma_f32_16x16x32_bf16 a[60:63], v[28:31], v[220:223], a[60:63]\n"
  "s_waitcnt lgkmcnt(0)\n"
  "v_mfma_f32_16x16x32_bf16 a[224:227], v[156:159], v[212:215], a[224:227]\n"
  "v_perm_b32 v36, v68, v64, s71\n"
  "v_perm_b32 v37, v76, v72, s71\n"
  "v_mfma_f32_16x16x32_bf16 a[228:231], v[156:159], v[224:227], a[228:231]\n"
  "v_perm_b32 v38, v84, v80, s71\n"
  "v_perm_b32 v39, v92, v88, s71\n"
  "v_mfma_f32_16x16x32_bf16 a[0:3], v[32:35], v[212:215], a[0:3]\n"
  "v_perm_b32 v40, v69, v65, s70\n"
  "v_perm_b32 v41, v77, v73, s70\n"
  "v_mfma_f32_16x16x32_bf16 a[32:35], v[32:35], v[224:227], a[32:35]\n"
  "v_perm_b32 v42, v85, v81, s70\n"
  "v_perm_b32 v43, v93, v89, s70\n"
  "v_mfma_f32_16x16x32_bf16 a[4:7], v[36:39], v[212:215], a[4:7]\n"
  "ds_read_b128 v[216:219], v242 offset:0\n"
  "ds_read_b128 v[228:231], v242 offset:1024\n"
  "v_perm_b32 v44, v69, v65, s71\n"
  "v_perm_b32 v45, v77, v73, s71\n"
  "v_mfma_f32_16x16x32_bf16 a[36:39], v[36:39], v[224:227], a[36:39]\n"
  "v_perm_b32 v46, v85, v81, s71\n"
  "v_perm_b32 v47, v93, v89, s71\n"
  "v_mfma_f32_16x16x32_bf16 a[8:11], v[40:43], v[212:215], a[8:11]\n"
  "v_perm_b32 v48, v70, v66, s70\n"
  "v_perm_b32 v49, v78, v74, s70\n"
  "v_mfma_f32_16x16x32_bf16 a[40:43], v[40:43], v[224:227], a[40:43]\n"
  "v_perm_b32 v50, v86, v82, s70\n"
  "v_perm_b32 v51, v94, v90, s70\n"
  "v_mfma_f32_16x16x32_bf16 a[12:15], v[44:47], v[212:215], a[12:15]\n"
  "v_perm_b32 v52, v70, v66, s71\n"
  "v_perm_b32 v53, v78, v74, s71\n"
  "v_mfma_f32_16x16x32_bf16 a[44:47], v[44:47], v[224:227], a[44:47]\n"
  "v_perm_b32 v54, v86, v82, s71\n"
  "v_perm_b32 v55, v94, v90, s71\n"
  "v_mfma_f32_16x16x32_bf16 a[16:19], v[48:51], v[212:215], a[16:19]\n"
  "v_perm_b32 v56, v71, v67, s70\n"
  "v_perm_b32 v57, v79, v75, s70\n"
  "v_mfma_f32_16x16x32_bf16 a[48:51], v[48:51], v[224:227], a[48:51]\n"
  "v_perm_b32 v58, v87, v83, s70\n"
  "v_perm_b32 v59, v95, v91, s70\n"
  "v_mfma_f32_16x16x32_bf16 a[20:23], v[52:55], v[212:215], a[20:23]\n"
  "v_perm_b32 v60, v71, v67, s71\n"
  "v_perm_b32 v61, v79, v75, s71\n"
  "v_mfma_f32_16x16x32_bf16 a[52:55], v[52:55], v[224:227], a[52:55]\n"
  "s_waitcnt vmcnt(0)\n"
  "ds_read_b64_tr_b16 v[160:161], v253 offset:40192\n"
  "ds_read_b64_tr_b16 v[162:163], v253 offset:40704\n"
  "v_perm_b32 v62, v87, v83, s71\n"
  "v_perm_b32 v63, v95, v91, s71\n"
  "v_mfma_f32_16x16x32_bf16 a[24:27], v[56:59], v[212:215], a[24:27]\n"
  "v_mfma_f32_16x16x32_bf16 a[56:59], v[56:59], v[224:227], a[56:59]\n"
  "v_perm_b32 v64, v100, v96, s70\n"
  "v_perm_b32 v65, v108, v104, s70\n"
  "v_mfma_f32_16x16x32_bf16 a[28:31], v[60:63], v[212:215], a[28:31]\n"
  "v_perm_b32 v66, v116, v112, s70\n"
  "v_perm_b32 v67, v124, v120, s70\n"
  "v_mfma_f32_16x16x32_bf16 a[60:63], v[60:63], v[224:227], a[60:63]\n"
  "s_waitcnt lgkmcnt(0)\n"
  "v_mfma_f32_16x16x32_bf16 a[224:227], v[160:163], v[216:219], a[224:227]\n"
  "v_perm_b32 v68, v100, v96, s71\n"
  "v_perm_b32 v69, v108, v104, s71\n"
  "v_mfma_f32_16x16x32_bf16 a[228:231], v[160:163], v[228:231], a[228:231]\n"
  "v_perm_b32 v70, v116, v112, s71\n"
  "v_perm_b32 v71, v124, v120, s71\n"
  "v_mfma_f32_16x16x32_bf16 a[0:3], v[64:67], v[216:219], a[0:3]\n"
  "v_perm_b32 v72, v101, v97, s70\n"
  "v_perm_b32 v73, v109, v105, s70\n"
  "v_mfma_f32_16x16x32_bf16 a[32:35], v[64:67], v[228:231], a[32:35]\n"
  "v_perm_b32 v74, v117, v113, s70\n"
  "v_perm_b32 v75, v125, v121, s70\n"
  "v_mfma_f32_16x16x32_bf16 a[4:7], v[68:71], v[216:219], a[4:7]\n"
  "v_perm_b32 v76, v101, v97, s71\n"
  "v_perm_b32 v77, v109, v105, s71\n"
  "v_mfma_f32_16x16x32_bf16 a[36:39], v[68:71], v[228:231], a[36:39]\n"
  "v_perm_b32 v78, v117, v113, s71\n"
  "v_perm_b32 v79, v125, v121, s71\n"
  "v_mfma_f32_16x16x32_bf16 a[8:11], v[72:75], v[216:219], a[8:11]\n"
  "v_perm_b32 v80, v102, v98, s70\n"
  "v_perm_b32 v81, v110, v106, s70\n"
  "v_mfma_f32_16x16x32_bf16 a[40:43], v[72:75], v[228:231], a[40:43]\n"
  "v_perm_b32 v82, v118, v114, s70\n"
  "v_perm_b32 v83, v126, v122, s70\n"
  "v_mfma_f32_16x16x32_bf16 a[12:15], v[76:79], v[216:219], a[12:15]\n"
  "v_perm_b32 v84, v102, v98, s71\n"
  "v_perm_b32 v85, v110, v106, s71\n"
  "v_mfma_f32_16x16x32_bf16 a[44:47], v[76:79], v[228:231], a[44:47]\n"
  "v_perm_b32 v86, v118, v114, s71\n"
  "v_perm_b32 v87, v126, v122, s71\n"
  "v_mfma_f32_16x16x32_bf16 a[16:19], v[80:83], v[216:219], a[16:19]\n"
  "v_perm_b32 v88, v103, v99, s70\n"
  "v_perm_b32 v89, v111, v107, s70\n"
  "v_mfma_f32_16x16x32_bf16 a[48:51], v[80:83], v[228:231], a[48:51]\n"
  "v_perm_b32 v90, v119, v115, s70\n"
  "v_perm_b32 v91, v127, v123, s70\n"
  "v_mfma_f32_16x16x32_bf16 a[20:23], v[84:87], v[216:219], a[20:23]\n"
  "v_perm_b32 v92, v103, v99, s71\n"
  "v_perm_b32 v93, v111, v107, s71\n"
  "v_mfma_f32_16x16x32_bf16 a[52:55], v[84:87], v[228:231], a[52:55]\n"
  "v_perm_b32 v94, v119, v115, s71\n"
  "v_perm_b32 v95, v127, v123, s71\n"
  "v_mfma_f32_16x16x32_bf16 a[24:27], v[88:91], v[216:219], a[24:27]\n"
  "v_mov_b32 v130, v206\n"
  "v_mov_b32 v131, v207\n"
  "v_mfma_f32_16x16x32_bf16 a[56:59], v[88:91], v[228:231], a[56:59]\n"
  "v_mov_b32 v128, 0\n"
  "v_mov_b32 v129, 0\n"
  "v_mfma_f32_16x16x32_bf16 a[28:31], v[92:95], v[216:219], a[28:31]\n"
  "v_lshlrev_b32 v248, 3, v252\n"
  "v_add_u32 v248, 4096, v248\n"
  "v_mfma_f32_16x16x32_bf16 a[60:63], v[92:95], v[228:231], a[60:63]\n"
  "v_mov_b32 v132, 0\n"
  "v_mov_b32 v133, 0\n"
  "s_cmp_lt_u32 s74, 2\n"
  "s_cbranch_scc1 L_fr2\n"
  "ds_read_b128 v[112:115], v251 offset:8192\n"
  "ds_read_b128 v[116:119], v251 offset:9216\n"
  "ds_read_b128 v[120:123], v251 offset:10240\n"
  "ds_read_b128 v[124:127], v251 offset:11264\n"
  "ds_read_b128 v[228:231], v250 offset:0\n"
  "L_fr2:\n"
  "v_mfma_f32_16x16x32_bf16 a[64:67], v[172:175], v[136:139], a[64:67]\n"
  "ds_read_b128 v[208:211], v240 offset:2048\n"
  "ds_read_b128 v[212:215], v241 offset:2048\n"
  "v_mfma_f32_16x16x32_bf16 a[68:71], v[176:179], v[136:139], a[68:71]\n"
  "ds_read_b128 v[216:219], v242 offset:2048\n"
  "v_mfma_f32_16x16x32_bf16 a[72:75], v[180:183], v[136:139], a[72:75]\n"
  "v_mfma_f32_16x16x32_bf16 a[76:79], v[184:187], v[136:139], a[76:79]\n"
  "v_accvgpr_read_b32 v232, a0\n"
  "v_accvgpr_read_b32 v233, a4\n"
  "v_mfma_f32_16x16x32_bf16 a[80:83], v[188:191], v[136:139], a[80:83]\n"
  "v_accvgpr_read_b32 v234, a8\n"
  "v_accvgpr_read_b32 v235, a12\n"
  "v_mfma_f32_16x16x32_bf16 a[84:87], v[192:195], v[136:139], a[84:87]\n"
  "v_accvgpr_read_b32 v236, a16\n"
  "v_accvgpr_read_b32 v237, a20\n"
  "v_mfma_f32_16x16x32_bf16 a[88:91], v[196:199], v[136:139], a[88:91]\n"
  "v_accvgpr_read_b32 v238, a24\n"
  "v_accvgpr_read_b32 v239, a28\n"
  "v_mfma_f32_16x16x32_bf16 a[92:95], v[200:203], v[136:139], a[92:95]\n"
  "v_cvt_pk_bf16_f32 v96, v232, v233\n"
  "v_cvt_pk_bf16_f32 v97, v234, v235\n"
  "v_mfma_f32_16x16x32_bf16 a[232:235], v[164:167], v[136:139], a[232:235]\n"
  "s_waitcnt lgkmcnt(0)\n"
  "v_cvt_pk_bf16_f32 v98, v236, v237\n"
  "v_cvt_pk_bf16_f32 v99, v238, v239\n"
  "v_mfma_f32_16x16x32_bf16 a[64:67], v[0:3], v[208:211], a[64:67]\n"
  "buffer_store_dwordx4 v[96:99], v243, s[52:55], s86 offen offset:0 sc1\n"
  "v_mfma_f32_16x16x32_bf16 a[68:71], v[4:7], v[208:211], a[68:71]\n"
  "v_accvgpr_read_b32 v220, a1\n"
  "v_accvgpr_read_b32 v221, a5\n"
  "v_mfma_f32_16x16x32_bf16 a[72:75], v[8:11], v[208:211], a[72:75]\n"
  "v_accvgpr_read_b32 v222, a9\n"
  "v_accvgpr_read_b32 v223, a13\n"
  "v_mfma_f32_16x16x32_bf16 a[76:79], v[12:15], v[208:211], a[76:79]\n"
  "v_accvgpr_read_b32 v224, a17\n"
  "v_accvgpr_read_b32 v225, a21\n"
  "v_mfma_f32_16x16x32_bf16 a[80:83], v[16:19], v[208:211], a[80:83]\n"
  "v_accvgpr_read_b32 v226, a25\n"
  "v_accvgpr_read_b32 v227, a29\n"
  "v_mfma_f32_16x16x32_bf16 a[84:87], v[20:23], v[208:211], a[84:87]\n"
  "v_cvt_pk_bf16_f32 v100, v220, v221\n"
  "v_cvt_pk_bf16_f32 v101, v222, v223\n"
  "v_mfma_f32_16x16x32_bf16 a[88:91], v[24:27], v[208:211], a[88:91]\n"
  "v_cvt_pk_bf16_f32 v102, v224, v225\n"
  "v_cvt_pk_bf16_f32 v103, v226, v227\n"
  "v_mfma_f32_16x16x32_bf16 a[92:95], v[28:31], v[208:211], a[92:95]\n"
  "buffer_store_dwordx4 v[100:103], v243, s[52:55], s86 offen offset:1024 sc1\n"
  "v_mfma_f32_16x16x32_bf16 a[232:235], v[168:171], v[208:211], a[232:235]\n"
  "v_accvgpr_read_b32 v232, a2\n"
  "v_accvgpr_read_b32 v233, a6\n"
  "v_mfma_f32_16x16x32_bf16 a[64:67], v[32:35], v[212:215], a[64:67]\n"
  "v_accvgpr_read_b32 v234, a10\n"
  "v_accvgpr_read_b32 v235, a14\n"
  "v_mfma_f32_16x16x32_bf16 a[68:71], v[36:39], v[212:215], a[68:71]\n"
  "v_accvgpr_read_b32 v236, a18\n"
  "v_accvgpr_read_b32 v237, a22\n"
  "v_mfma_f32_16x16x32_bf16 a[72:75], v[40:43], v[212:215], a[72:75]\n"
  "v_accvgpr_read_b32 v238, a26\n"
  "v_accvgpr_read_b32 v239, a30\n"
  "v_mfma_f32_16x16x32_bf16 a[76:79], v[44:47], v[212:215], a[76:79]\n"
  "v_cvt_pk_bf16_f32 v104, v232, v233\n"
  "v_cvt_pk_bf16_f32 v105, v234, v235\n"
  "v_mfma_f32_16x16x32_bf16 a[80:83], v[48:51], v[212:215], a[80:83]\n"
  "v_cvt_pk_bf16_f32 v106, v236, v237\n"
  "v_cvt_pk_bf16_f32 v107, v238, v239\n"
  "v_mfma_f32_16x16x32_bf16 a[84:87], v[52:55], v[212:215], a[84:87]\n"
  "buffer_store_dwordx4 v[104:107], v243, s[52:55], s86 offen offset:2048 sc1\n"
  "v_mfma_f32_16x16x32_bf16 a[88:91], v[56:59], v[212:215], a[88:91]\n"
  "v_accvgpr_read_b32 v220, a3\n"
  "v_accvgpr_read_b32 v221, a7\n"
  "v_mfma_f32_16x16x32_bf16 a[92:95], v[60:63], v[212:215], a[92:95]\n"
  "v_accvgpr_read_b32 v222, a11\n"
  "v_accvgpr_read_b32 v223, a15\n"
  "v_mfma_f32_16x16x32_bf16 a[232:235], v[156:159], v[212:215], a[232:235]\n"
  "v_accvgpr_read_b32 v224, a19\n"
  "v_accvgpr_read_b32 v225, a23\n"
  "s_cmp_lt_u32 s74, 2\n"
  "s_cbranch_scc1 L_fa2\n"
  "v_mfma_f32_16x16x32_bf16 a[64:67], v[130:133], v[112:115], a[64:67]\n"
  "v_mfma_f32_16x16x32_bf16 a[68:71], v[128:131], v[112:115], a[68:71]\n"
  "v_mfma_f32_16x16x32_bf16 a[72:75], v[130:133], v[116:119], a[72:75]\n"
  "v_mfma_f32_16x16x32_bf16 a[76:79], v[128:131], v[116:119], a[76:79]\n"
  "v_mfma_f32_16x16x32_bf16 a[80:83], v[130:133], v[120:123], a[80:83]\n"
  "v_mfma_f32_16x16x32_bf16 a[84:87], v[128:131], v[120:123], a[84:87]\n"
  "v_mfma_f32_16x16x32_bf16 a[88:91], v[130:133], v[124:127], a[88:91]\n"
  "v_mfma_f32_16x16x32_bf16 a[92:95], v[128:131], v[124:127], a[92:95]\n"
  "v_mfma_f32_16x16x32_bf16 a[232:235], v[130:133], v[228:231], a[232:235]\n"
  "L_fa2:\n"
  "v_mfma_f32_16x16x32_bf16 a[64:67], v[64:67], v[216:219], a[64:67]\n"
  "v_accvgpr_read_b32 v226, a27\n"
  "v_accvgpr_read_b32 v227, a31\n"
  "v_mfma_f32_16x16x32_bf16 a[68:71], v[68:71], v[216:219], a[68:71]\n"
  "v_cvt_pk_bf16_f32 v108, v220, v221\n"
  "v_cvt_pk_bf16_f32 v109, v222, v223\n"
  "v_mfma_f32_16x16x32_bf16 a[72:75], v[72:75], v[216:219], a[72:75]\n"
  "v_cvt_pk_bf16_f32 v110, v224, v225\n"
  "v_cvt_pk_bf16_f32 v111, v226, v227\n"
  "v_mfma_f32_16x16x32_bf16 a[76:79], v[76:79], v[216:219], a[76:79]\n"
  "buffer_store_dwordx4 v[108:111], v243, s[52:55], s86 offen offset:3072 sc1\n"
  "v_mfma_f32_16x16x32_bf16 a[80:83], v[80:83], v[216:219], a[80:83]\n"
  "v_accvgpr_read_b32 v232, a224\n"
  "v_accvgpr_read_b32 v233, a225\n"
  "v_mfma_f32_16x16x32_bf16 a[84:87], v[84:87], v[216:219], a[84:87]\n"
  "v_accvgpr_read_b32 v234, a226\n"
  "v_accvgpr_read_b32 v235, a227\n"
  "v_mfma_f32_16x16x32_bf16 a[88:91], v[88:91], v[216:219], a[88:91]\n"
  "v_cvt_pk_bf16_f32 v204, v232, v233\n"
  "v_cvt_pk_bf16_f32 v205, v234, v235\n"
  "v_mfma_f32_16x16x32_bf16 a[92:95], v[92:95], v[216:219], a[92:95]\n"
  "buffer_store_dwordx2 v[204:205], v248, s[52:55], s86 offen sc1\n"
  "v_mfma_f32_16x16x32_bf16 a[232:235], v[160:163], v[216:219], a[232:235]\n"
  "s_add_u32 s86, s86, 4608\n"
  "s_cmp_eq_u32 s86, 32256\n"
  "s_cselect_b32 s86, 0, s86\n"
  "s_cmp_lt_u32 s74, 2\n"
  "s_cbranch_scc1 L_fr3\n"
  "ds_read_b128 v[112:115], v251 offset:12288\n"
  "ds_read_b128 v[116:119], v251 offset:13312\n"
  "ds_read_b128 v[120:123], v251 offset:14336\n"
  "ds_read_b128 v[124:127], v249 offset:0\n"
  "ds_read_b128 v[228:231], v250 offset:0\n"
  "L_fr3:\n"
  "v_mfma_f32_16x16x32_bf16 a[96:99], v[172:175], v[140:143], a[96:99]\n"
  "ds_read_b128 v[208:211], v240 offset:3072\n"
  "ds_read_b128 v[212:215], v241 offset:3072\n"
  "v_mfma_f32_16x16x32_bf16 a[100:103], v[176:179], v[140:143], a[100:103]\n"
  "ds_read_b128 v[216:219], v242 offset:3072\n"
  "v_accvgpr_read_b32 v232, a32\n"
  "v_mfma_f32_16x16x32_bf16 a[104:107], v[180:183], v[140:143], a[104:107]\n"
  "v_accvgpr_read_b32 v233, a36\n"
  "v_accvgpr_read_b32 v234, a40\n"
  "v_mfma_f32_16x16x32_bf16 a[108:111], v[184:187], v[140:143], a[108:111]\n"
  "v_accvgpr_read_b32 v235, a44\n"
  "v_accvgpr_read_b32 v236, a48\n"
  "v_mfma_f32_16x16x32_bf16 a[112:115], v[188:191], v[140:143], a[112:115]\n"
  "v_accvgpr_read_b32 v237, a52\n"
  "v_accvgpr_read_b32 v238, a56\n"
  "v_mfma_f32_16x16x32_bf16 a[116:119], v[192:195], v[140:143], a[116:119]\n"
  "v_accvgpr_read_b32 v239, a60\n"
  "v_cvt_pk_bf16_f32 v96, v232, v233\n"
  "v_mfma_f32_16x16x32_bf16 a[120:123], v[196:199], v[140:143], a[120:123]\n"
  "v_cvt_pk_bf16_f32 v97, v234, v235\n"
  "v_cvt_pk_bf16_f32 v98, v236, v237\n"
  "v_mfma_f32_16x16x32_bf16 a[124:127], v[200:203], v[140:143], a[124:127]\n"
  "v_cvt_pk_bf16_f32 v99, v238, v239\n"
  "v_mfma_f32_16x16x32_bf16 a[236:239], v[164:167], v[140:143], a[236:239]\n"
  "s_waitcnt lgkmcnt(0)\n"
  "buffer_store_dwordx4 v[96:99], v243, s[52:55], s86 offen offset:0 sc1\n"
  "v_mfma_f32_16x16x32_bf16 a[96:99], v[0:3], v[208:211], a[96:99]\n"
  "v_accvgpr_read_b32 v220, a33\n"
  "v_accvgpr_read_b32 v221, a37\n"
  "v_mfma_f32_16x16x32_bf16 a[100:103], v[4:7], v[208:211], a[100:103]\n"
  "v_accvgpr_read_b32 v222, a41\n"
  "v_accvgpr_read_b32 v223, a45\n"
  "v_mfma_f32_16x16x32_bf16 a[104:107], v[8:11], v[208:211], a[104:107]\n"
  "v_accvgpr_read_b32 v224, a49\n"
  "v_accvgpr_read_b32 v225, a53\n"
  "v_mfma_f32_16x16x32_bf16 a[108:111], v[12:15], v[208:211], a[108:111]\n"
  "v_accvgpr_read_b32 v226, a57\n"
  "v_accvgpr_read_b32 v227, a61\n"
  "v_mfma_f32_16x16x32_bf16 a[112:115], v[16:19], v[208:211], a[112:115]\n"
  "v_cvt_pk_bf16_f32 v100, v220, v221\n"
  "v_cvt_pk_bf16_f32 v101, v222, v223\n"
  "v_mfma_f32_16x16x32_bf16 a[116:119], v[20:23], v[208:211], a[116:119]\n"
  "v_cvt_pk_bf16_f32 v102, v224, v225\n"
  "v_cvt_pk_bf16_f32 v103, v226, v227\n"
  "v_mfma_f32_16x16x32_bf16 a[120:123], v[24:27], v[208:211], a[120:123]\n"
  "buffer_store_dwordx4 v[100:103], v243, s[52:55], s86 offen offset:1024 sc1\n"
  "v_mfma_f32_16x16x32_bf16 a[124:127], v[28:31], v[208:211], a[124:127]\n"
  "v_accvgpr_read_b32 v232, a34\n"
  "v_accvgpr_read_b32 v233, a38\n"
  "v_mfma_f32_16x16x32_bf16 a[236:239], v[168:171], v[208:211], a[236:239]\n"
  "v_accvgpr_read_b32 v234, a42\n"
  "v_accvgpr_read_b32 v235, a46\n"
  "v_mfma_f32_16x16x32_bf16 a[96:99], v[32:35], v[212:215], a[96:99]\n"
  "v_accvgpr_read_b32 v236, a50\n"
  "v_accvgpr_read_b32 v237, a54\n"
  "v_mfma_f32_16x16x32_bf16 a[100:103], v[36:39], v[212:215], a[100:103]\n"
  "v_accvgpr_read_b32 v238, a58\n"
  "v_accvgpr_read_b32 v239, a62\n"
  "v_mfma_f32_16x16x32_bf16 a[104:107], v[40:43], v[212:215], a[104:107]\n"
  "v_cvt_pk_bf16_f32 v104, v232, v233\n"
  "v_cvt_pk_bf16_f32 v105, v234, v235\n"
  "v_mfma_f32_16x16x32_bf16 a[108:111], v[44:47], v[212:215], a[108:111]\n"
  "v_cvt_pk_bf16_f32 v106, v236, v237\n"
  "v_cvt_pk_bf16_f32 v107, v238, v239\n"
  "v_mfma_f32_16x16x32_bf16 a[112:115], v[48:51], v[212:215], a[112:115]\n"
  "buffer_store_dwordx4 v[104:107], v243, s[52:55], s86 offen offset:2048 sc1\n"
  "v_mfma_f32_16x16x32_bf16 a[116:119], v[52:55], v[212:215], a[116:119]\n"
  "v_accvgpr_read_b32 v220, a35\n"
  "v_accvgpr_read_b32 v221, a39\n"
  "v_mfma_f32_16x16x32_bf16 a[120:123], v[56:59], v[212:215], a[120:123]\n"
  "v_accvgpr_read_b32 v222, a43\n"
  "v_accvgpr_read_b32 v223, a47\n"
  "v_mfma_f32_16x16x32_bf16 a[124:127], v[60:63], v[212:215], a[124:127]\n"
  "v_accvgpr_read_b32 v224, a51\n"
  "v_accvgpr_read_b32 v225, a55\n"
  "v_mfma_f32_16x16x32_bf16 a[236:239], v[156:159], v[212:215], a[236:239]\n"
  "v_accvgpr_read_b32 v226, a59\n"
  "v_accvgpr_read_b32 v227, a63\n"
  "s_cmp_lt_u32 s74, 2\n"
  "s_cbranch_scc1 L_fa3\n"
  "v_mfma_f32_16x16x32_bf16 a[96:99], v[130:133], v[112:115], a[96:99]\n"
  "v_mfma_f32_16x16x32_bf16 a[100:103], v[128:131], v[112:115], a[100:103]\n"
  "v_mfma_f32_16x16x32_bf16 a[104:107], v[130:133], v[116:119], a[104:107]\n"
  "v_mfma_f32_16x16x32_bf16 a[108:111], v[128:131], v[116:119], a[108:111]\n"
  "v_mfma_f32_16x16x32_bf16 a[112:115], v[130:133], v[120:123], a[112:115]\n"
  "v_mfma_f32_16x16x32_bf16 a[116:119], v[128:131], v[120:123], a[116:119]\n"
  "v_mfma_f32_16x16x32_bf16 a[120:123], v[130:133], v[124:127], a[120:123]\n"
  "v_mfma_f32_16x16x32_bf16 a[124:127], v[128:131], v[124:127], a[124:127]\n"
  "v_mfma_f32_16x16x32_bf16 a[236:239], v[128:131], v[228:231], a[236:239]\n"
  "L_fa3:\n"
  "v_mfma_f32_16x16x32_bf16 a[96:99], v[64:67], v[216:219], a[96:99]\n"
  "v_cvt_pk_bf16_f32 v108, v220, v221\n"
  "v_cvt_pk_bf16_f32 v109, v222, v223\n"
  "v_mfma_f32_16x16x32_bf16 a[100:103], v[68:71], v[216:219], a[100:103]\n"
  "v_cvt_pk_bf16_f32 v110, v224, v225\n"
  "v_cvt_pk_bf16_f32 v111, v226, v227\n"
  "v_mfma_f32_16x16x32_bf16 a[104:107], v[72:75], v[216:219], a[104:107]\n"
  "buffer_store_dwordx4 v[108:111], v243, s[52:55], s86 offen offset:3072 sc1\n"
  "v_mfma_f32_16x16x32_bf16 a[108:111], v[76:79], v[216:219], a[108:111]\n"
  "v_accvgpr_read_b32 v232, a228\n"
  "v_accvgpr_read_b32 v233, a229\n"
  "v_mfma_f32_16x16x32_bf16 a[112:115], v[80:83], v[216:219], a[112:115]\n"
  "v_accvgpr_read_b32 v234, a230\n"
  "v_accvgpr_read_b32 v235, a231\n"
  "v_mfma_f32_16x16x32_bf16 a[116:119], v[84:87], v[216:219], a[116:119]\n"
  "v_cvt_pk_bf16_f32 v206, v232, v233\n"
  "v_cvt_pk_bf16_f32 v207, v234, v235\n"
  "v_mfma_f32_16x16x32_bf16 a[120:123], v[88:91], v[216:219], a[120:123]\n"
  "buffer_store_dwordx2 v[206:207], v248, s[52:55], s86 offen sc1\n"
  "v_mfma_f32_16x16x32_bf16 a[124:127], v[92:95], v[216:219], a[124:127]\n"
  "s_add_u32 s86, s86, 4608\n"
  "s_cmp_eq_u32 s86, 32256\n"
  "s_cselect_b32 s86, 0, s86\n"
  "v_mfma_f32_16x16x32_bf16 a[236:239], v[160:163], v[216:219], a[236:239]\n"
  "v_accvgpr_read_b32 v232, a64\n"
  "v_accvgpr_read_b32 v233, a68\n"
  "s_cmp_lt_u32 s74, 2\n"
  "s_cbranch_scc1 L_fr4\n"
  "ds_read_b128 v[112:115], v249 offset:1024\n"
  "ds_read_b128 v[116:119], v249 offset:2048\n"
  "ds_read_b128 v[120:123], v249 offset:3072\n"
  "ds_read_b128 v[124:127], v249 offset:4096\n"
  "ds_read_b128 v[228:231], v250 offset:1024\n"
  "L_fr4:\n"
  "v_mfma_f32_16x16x32_bf16 a[128:131], v[172:175], v[144:147], a[128:131]\n"
  "ds_read_b128 v[208:211], v240 offset:4096\n"
  "ds_read_b128 v[212:215], v241 offset:4096\n"
  "v_mfma_f32_16x16x32_bf16 a[132:135], v[176:179], v[144:147], a[132:135]\n"
  "ds_read_b128 v[216:219], v242 offset:4096\n"
  "v_accvgpr_read_b32 v234, a72\n"
  "v_mfma_f32_16x16x32_bf16 a[136:139], v[180:183], v[144:147], a[136:139]\n"
  "v_accvgpr_read_b32 v235, a76\n"
  "v_accvgpr_read_b32 v236, a80\n"
  "v_mfma_f32_16x16x32_bf16 a[140:143], v[184:187], v[144:147], a[140:143]\n"
  "v_accvgpr_read_b32 v237, a84\n"
  "v_accvgpr_read_b32 v238, a88\n"
  "v_mfma_f32_16x16x32_bf16 a[144:147], v[188:191], v[144:147], a[144:147]\n"
  "v_accvgpr_read_b32 v239, a92\n"
  "v_cvt_pk_bf16_f32 v96, v232, v233\n"
  "v_mfma_f32_16x16x32_bf16 a[148:151], v[192:195], v[144:147], a[148:151]\n"
  "v_cvt_pk_bf16_f32 v97, v234, v235\n"
  "v_cvt_pk_bf16_f32 v98, v236, v237\n"
  "v_mfma_f32_16x16x32_bf16 a[152:155], v[196:199], v[144:147], a[152:155]\n"
  "v_cvt_pk_bf16_f32 v99, v238, v239\n"
  "v_mfma_f32_16x16x32_bf16 a[156:159], v[200:203], v[144:147], a[156:159]\n"
  "buffer_store_dwordx4 v[96:99], v243, s[52:55], s86 offen offset:0 sc1\n"
  "v_mfma_f32_16x16x32_bf16 a[240:243], v[164:167], v[144:147], a[240:243]\n"
  "s_waitcnt lgkmcnt(0)\n"
  "v_accvgpr_read_b32 v220, a65\n"
  "v_accvgpr_read_b32 v221, a69\n"
  "v_mfma_f32_16x16x32_bf16 a[128:131], v[0:3], v[208:211], a[128:131]\n"
  "v_accvgpr_read_b32 v222, a73\n"
  "v_accvgpr_read_b32 v223, a77\n"
  "v_mfma_f32_16x16x32_bf16 a[132:135], v[4:7], v[208:211], a[132:135]\n"
  "v_accvgpr_read_b32 v224, a81\n"
  "v_accvgpr_read_b32 v225, a85\n"
  "v_mfma_f32_16x16x32_bf16 a[136:139], v[8:11], v[208:211], a[136:139]\n"
  "v_accvgpr_read_b32 v226, a89\n"
  "v_accvgpr_read_b32 v227, a93\n"
  "v_mfma_f32_16x16x32_bf16 a[140:143], v[12:15], v[208:211], a[140:143]\n"
  "v_cvt_pk_bf16_f32 v100, v220, v221\n"
  "v_cvt_pk_bf16_f32 v101, v222, v223\n"
  "v_mfma_f32_16x16x32_bf16 a[144:147], v[16:19], v[208:211], a[144:147]\n"
  "v_cvt_pk_bf16_f32 v102, v224, v225\n"
  "v_cvt_pk_bf16_f32 v103, v226, v227\n"
  "v_mfma_f32_16x16x32_bf16 a[148:151], v[20:23], v[208:211], a[148:151]\n"
  "buffer_store_dwordx4 v[100:103], v243, s[52:55], s86 offen offset:1024 sc1\n"
  "v_mfma_f32_16x16x32_bf16 a[152:155], v[24:27], v[208:211], a[152:155]\n"
  "v_accvgpr_read_b32 v232, a66\n"
  "v_accvgpr_read_b32 v233, a70\n"
  "v_mfma_f32_16x16x32_bf16 a[156:159], v[28:31], v[208:211], a[156:159]\n"
  "v_accvgpr_read_b32 v234, a74\n"
  "v_accvgpr_read_b32 v235, a78\n"
  "v_mfma_f32_16x16x32_bf16 a[240:243], v[168:171], v[208:211], a[240:243]\n"
  "v_accvgpr_read_b32 v236, a82\n"
  "v_accvgpr_read_b32 v237, a86\n"
  "v_mfma_f32_16x16x32_bf16 a[128:131], v[32:35], v[212:215], a[128:131]\n"
  "v_accvgpr_read_b32 v238, a90\n"
  "v_accvgpr_read_b32 v239, a94\n"
  "v_mfma_f32_16x16x32_bf16 a[132:135], v[36:39], v[212:215], a[132:135]\n"
  "v_cvt_pk_bf16_f32 v104, v232, v233\n"
  "v_cvt_pk_bf16_f32 v105, v234, v235\n"
  "v_mfma_f32_16x16x32_bf16 a[136:139], v[40:43], v[212:215], a[136:139]\n"
  "v_cvt_pk_bf16_f32 v106, v236, v237\n"
  "v_cvt_pk_bf16_f32 v107, v238, v239\n"
  "v_mfma_f32_16x16x32_bf16 a[140:143], v[44:47], v[212:215], a[140:143]\n"
  "buffer_store_dwordx4 v[104:107], v243, s[52:55], s86 offen offset:2048 sc1\n"
  "v_mfma_f32_16x16x32_bf16 a[144:147], v[48:51], v[212:215], a[144:147]\n"
  "v_accvgpr_read_b32 v220, a67\n"
  "v_accvgpr_read_b32 v221, a71\n"
  "v_mfma_f32_16x16x32_bf16 a[148:151], v[52:55], v[212:215], a[148:151]\n"
  "v_accvgpr_read_b32 v222, a75\n"
  "v_accvgpr_read_b32 v223, a79\n"
  "v_mfma_f32_16x16x32_bf16 a[152:155], v[56:59], v[212:215], a[152:155]\n"
  "v_accvgpr_read_b32 v224, a83\n"
  "v_accvgpr_read_b32 v225, a87\n"
  "v_mfma_f32_16x16x32_bf16 a[156:159], v[60:63], v[212:215], a[156:159]\n"
  "v_accvgpr_read_b32 v226, a91\n"
  "v_accvgpr_read_b32 v227, a95\n"
  "v_mfma_f32_16x16x32_bf16 a[240:243], v[156:159], v[212:215], a[240:243]\n"
  "v_cvt_pk_bf16_f32 v108, v220, v221\n"
  "v_cvt_pk_bf16_f32 v109, v222, v223\n"
  "s_cmp_lt_u32 s74, 2\n"
  "s_cbranch_scc1 L_fa4\n"
  "v_mfma_f32_16x16x32_bf16 a[128:131], v[130:133], v[112:115], a[128:131]\n"
  "v_mfma_f32_16x16x32_bf16 a[132:135], v[128:131], v[112:115], a[132:135]\n"
  "v_mfma_f32_16x16x32_bf16 a[136:139], v[130:133], v[116:119], a[136:139]\n"
  "v_mfma_f32_16x16x32_bf16 a[140:143], v[128:131], v[116:119], a[140:143]\n"
  "v_mfma_f32_16x16x32_bf16 a[144:147], v[130:133], v[120:123], a[144:147]\n"
  "v_mfma_f32_16x16x32_bf16 a[148:151], v[128:131], v[120:123], a[148:151]\n"
  "v_mfma_f32_16x16x32_bf16 a[152:155], v[130:133], v[124:127], a[152:155]\n"
  "v_mfma_f32_16x16x32_bf16 a[156:159], v[128:131], v[124:127], a[156:159]\n"
  "v_mfma_f32_16x16x32_bf16 a[240:243], v[130:133], v[228:231], a[240:243]\n"
  "L_fa4:\n"
  "v_mfma_f32_16x16x32_bf16 a[128:131], v[64:67], v[216:219], a[128:131]\n"
  "v_cvt_pk_bf16_f32 v110, v224, v225\n"
  "v_cvt_pk_bf16_f32 v111, v226, v227\n"
  "v_mfma_f32_16x16x32_bf16 a[132:135], v[68:71], v[216:219], a[132:135]\n"
  "buffer_store_dwordx4 v[108:111], v243, s[52:55], s86 offen offset:3072 sc1\n"
  "v_mfma_f32_16x16x32_bf16 a[136:139], v[72:75], v[216:219], a[136:139]\n"
  "v_accvgpr_read_b32 v232, a232\n"
  "v_accvgpr_read_b32 v233, a233\n"
  "v_mfma_f32_16x16x32_bf16 a[140:143], v[76:79], v[216:219], a[140:143]\n"
  "v_accvgpr_read_b32 v234, a234\n"
  "v_accvgpr_read_b32 v235, a235\n"
  "v_mfma_f32_16x16x32_bf16 a[144:147], v[80:83], v[216:219], a[144:147]\n"
  "v_cvt_pk_bf16_f32 v204, v232, v233\n"
  "v_cvt_pk_bf16_f32 v205, v234, v235\n"
  "v_mfma_f32_16x16x32_bf16 a[148:151], v[84:87], v[216:219], a[148:151]\n"
  "buffer_store_dwordx2 v[204:205], v248, s[52:55], s86 offen sc1\n"
  "v_mfma_f32_16x16x32_bf16 a[152:155], v[88:91], v[216:219], a[152:155]\n"
  "s_add_u32 s86, s86, 4608\n"
  "s_cmp_eq_u32 s86, 32256\n"
  "s_cselect_b32 s86, 0, s86\n"
  "v_mfma_f32_16x16x32_bf16 a[156:159], v[92:95], v[216:219], a[156:159]\n"
  "v_accvgpr_read_b32 v232, a96\n"
  "v_accvgpr_read_b32 v233, a100\n"
  "v_mfma_f32_16x16x32_bf16 a[240:243], v[160:163], v[216:219], a[240:243]\n"
  "v_accvgpr_read_b32 v234, a104\n"
  "v_accvgpr_read_b32 v235, a108\n"
  "s_cmp_lt_u32 s74, 2\n"
  "s_cbranch_scc1 L_fr5\n"
  "ds_read_b128 v[112:115], v249 offset:5120\n"
  "ds_read_b128 v[116:119], v249 offset:6144\n"
  "ds_read_b128 v[120:123], v249 offset:33024\n"
  "ds_read_b128 v[124:127], v249 offset:34048\n"
  "ds_read_b128 v[228:231], v250 offset:1024\n"
  "L_fr5:\n"
  "v_mfma_f32_16x16x32_bf16 a[160:163], v[172:175], v[148:151], a[160:163]\n"
  "ds_read_b128 v[208:211], v240 offset:5120\n"
  "ds_read_b128 v[212:215], v241 offset:5120\n"
  "v_mfma_f32_16x16x32_bf16 a[164:167], v[176:179], v[148:151], a[164:167]\n"
  "ds_read_b128 v[216:219], v242 offset:5120\n"
  "v_accvgpr_read_b32 v236, a112\n"
  "v_mfma_f32_16x16x32_bf16 a[168:171], v[180:183], v[148:151], a[168:171]\n"
  "v_accvgpr_read_b32 v237, a116\n"
  "v_accvgpr_read_b32 v238, a120\n"
  "v_mfma_f32_16x16x32_bf16 a[172:175], v[184:187], v[148:151], a[172:175]\n"
  "v_accvgpr_read_b32 v239, a124\n"
  "v_cvt_pk_bf16_f32 v96, v232, v233\n"
  "v_mfma_f32_16x16x32_bf16 a[176:179], v[188:191], v[148:151], a[176:179]\n"
  "v_cvt_pk_bf16_f32 v97, v234, v235\n"
  "v_cvt_pk_bf16_f32 v98, v236, v237\n"
  "v_mfma_f32_16x16x32_bf16 a[180:183], v[192:195], v[148:151], a[180:183]\n"
  "v_cvt_pk_bf16_f32 v99, v238, v239\n"
  "v_mfma_f32_16x16x32_bf16 a[184:187], v[196:199], v[148:151], a[184:187]\n"
  "buffer_store_dwordx4 v[96:99], v243, s[52:55], s86 offen offset:0 sc1\n"
  "v_mfma_f32_16x16x32_bf16 a[188:191], v[200:203], v[148:151], a[188:191]\n"
  "v_accvgpr_read_b32 v220, a97\n"
  "v_accvgpr_read_b32 v221, a101\n"
  "v_mfma_f32_16x16x32_bf16 a[244:247], v[164:167], v[148:151], a[244:247]\n"
  "s_waitcnt lgkmcnt(0)\n"
  "v_accvgpr_read_b32 v222, a105\n"
  "v_accvgpr_read_b32 v223, a109\n"
  "v_mfma_f32_16x16x32_bf16 a[160:163], v[0:3], v[208:211], a[160:163]\n"
  "v_accvgpr_read_b32 v224, a113\n"
  "v_accvgpr_read_b32 v225, a117\n"
  "v_mfma_f32_16x16x32_bf16 a[164:167], v[4:7], v[208:211], a[164:167]\n"
  "v_accvgpr_read_b32 v226, a121\n"
  "v_accvgpr_read_b32 v227, a125\n"
  "v_mfma_f32_16x16x32_bf16 a[168:171], v[8:11], v[208:211], a[168:171]\n"
  "v_cvt_pk_bf16_f32 v100, v220, v221\n"
  "v_cvt_pk_bf16_f32 v101, v222, v223\n"
  "v_mfma_f32_16x16x32_bf16 a[172:175], v[12:15], v[208:211], a[172:175]\n"
  "v_cvt_pk_bf16_f32 v102, v224, v225\n"
  "v_cvt_pk_bf16_f32 v103, v226, v227\n"
  "v_mfma_f32_16x16x32_bf16 a[176:179], v[16:19], v[208:211], a[176:179]\n"
  "buffer_store_dwordx4 v[100:103], v243, s[52:55], s86 offen offset:1024 sc1\n"
  "v_mfma_f32_16x16x32_bf16 a[180:183], v[20:23], v[208:211], a[180:183]\n"
  "v_accvgpr_read_b32 v232, a98\n"
  "v_accvgpr_read_b32 v233, a102\n"
  "v_mfma_f32_16x16x32_bf16 a[184:187], v[24:27], v[208:211], a[184:187]\n"
  "v_accvgpr_read_b32 v234, a106\n"
  "v_accvgpr_read_b32 v235, a110\n"
  "v_mfma_f32_16x16x32_bf16 a[188:191], v[28:31], v[208:211], a[188:191]\n"
  "v_accvgpr_read_b32 v236, a114\n"
  "v_accvgpr_read_b32 v237, a118\n"
  "v_mfma_f32_16x16x32_bf16 a[244:247], v[168:171], v[208:211], a[244:247]\n"
  "v_accvgpr_read_b32 v238, a122\n"
  "v_accvgpr_read_b32 v239, a126\n"
  "v_mfma_f32_16x16x32_bf16 a[160:163], v[32:35], v[212:215], a[160:163]\n"
  "v_cvt_pk_bf16_f32 v104, v232, v233\n"
  "v_cvt_pk_bf16_f32 v105, v234, v235\n"
  "v_mfma_f32_16x16x32_bf16 a[164:167], v[36:39], v[212:215], a[164:167]\n"
  "v_cvt_pk_bf16_f32 v106, v236, v237\n"
  "v_cvt_pk_bf16_f32 v107, v238, v239\n"
  "v_mfma_f32_16x16x32_bf16 a[168:171], v[40:43], v[212:215], a[168:171]\n"
  "buffer_store_dwordx4 v[104:107], v243, s[52:55], s86 offen offset:2048 sc1\n"
  "v_mfma_f32_16x16x32_bf16 a[172:175], v[44:47], v[212:215], a[172:175]\n"
  "v_accvgpr_read_b32 v220, a99\n"
  "v_accvgpr_read_b32 v221, a103\n"
  "v_mfma_f32_16x16x32_bf16 a[176:179], v[48:51], v[212:215], a[176:179]\n"
  "v_accvgpr_read_b32 v222, a107\n"
  "v_accvgpr_read_b32 v223, a111\n"
  "v_mfma_f32_16x16x32_bf16 a[180:183], v[52:55], v[212:215], a[180:183]\n"
  "v_accvgpr_read_b32 v224, a115\n"
  "v_accvgpr_read_b32 v225, a119\n"
  "v_mfma_f32_16x16x32_bf16 a[184:187], v[56:59], v[212:215], a[184:187]\n"
  "v_accvgpr_read_b32 v226, a123\n"
  "v_accvgpr_read_b32 v227, a127\n"
  "v_mfma_f32_16x16x32_bf16 a[188:191], v[60:63], v[212:215], a[188:191]\n"
  "v_cvt_pk_bf16_f32 v108, v220, v221\n"
  "v_cvt_pk_bf16_f32 v109, v222, v223\n"
  "v_mfma_f32_16x16x32_bf16 a[244:247], v[156:159], v[212:215], a[244:247]\n"
  "v_cvt_pk_bf16_f32 v110, v224, v225\n"
  "v_cvt_pk_bf16_f32 v111, v226, v227\n"
  "s_cmp_lt_u32 s74, 2\n"
  "s_cbranch_scc1 L_fa5\n"
  "v_mfma_f32_16x16x32_bf16 a[160:163], v[130:133], v[112:115], a[160:163]\n"
  "v_mfma_f32_16x16x32_bf16 a[164:167], v[128:131], v[112:115], a[164:167]\n"
  "v_mfma_f32_16x16x32_bf16 a[168:171], v[130:133], v[116:119], a[168:171]\n"
  "v_mfma_f32_16x16x32_bf16 a[172:175], v[128:131], v[116:119], a[172:175]\n"
  "v_mfma_f32_16x16x32_bf16 a[176:179], v[130:133], v[120:123], a[176:179]\n"
  "v_mfma_f32_16x16x32_bf16 a[180:183], v[128:131], v[120:123], a[180:183]\n"
  "v_mfma_f32_16x16x32_bf16 a[184:187], v[130:133], v[124:127], a[184:187]\n"
  "v_mfma_f32_16x16x32_bf16 a[188:191], v[128:131], v[124:127], a[188:191]\n"
  "v_mfma_f32_16x16x32_bf16 a[244:247], v[128:131], v[228:231], a[244:247]\n"
  "L_fa5:\n"
  "v_mfma_f32_16x16x32_bf16 a[160:163], v[64:67], v[216:219], a[160:163]\n"
  "buffer_store_dwordx4 v[108:111], v243, s[52:55], s86 offen offset:3072 sc1\n"
  "v_mfma_f32_16x16x32_bf16 a[164:167], v[68:71], v[216:219], a[164:167]\n"
  "v_accvgpr_read_b32 v232, a236\n"
  "v_accvgpr_read_b32 v233, a237\n"
  "v_mfma_f32_16x16x32_bf16 a[168:171], v[72:75], v[216:219], a[168:171]\n"
  "v_accvgpr_read_b32 v234, a238\n"
  "v_accvgpr_read_b32 v235, a239\n"
  "v_mfma_f32_16x16x32_bf16 a[172:175], v[76:79], v[216:219], a[172:175]\n"
  "v_cvt_pk_bf16_f32 v206, v232, v233\n"
  "v_cvt_pk_bf16_f32 v207, v234, v235\n"
  "v_mfma_f32_16x16x32_bf16 a[176:179], v[80:83], v[216:219], a[176:179]\n"
  "buffer_store_dwordx2 v[206:207], v248, s[52:55], s86 offen sc1\n"
  "v_mfma_f32_16x16x32_bf16 a[180:183], v[84:87], v[216:219], a[180:183]\n"
  "s_add_u32 s86, s86, 4608\n"
  "s_cmp_eq_u32 s86, 32256\n"
  "s_cselect_b32 s86, 0, s86\n"
  "v_mfma_f32_16x16x32_bf16 a[184:187], v[88:91], v[216:219], a[184:187]\n"
  "v_accvgpr_read_b32 v232, a128\n"
  "v_accvgpr_read_b32 v233, a132\n"
  "v_mfma_f32_16x16x32_bf16 a[188:191], v[92:95], v[216:219], a[188:191]\n"
  "v_accvgpr_read_b32 v234, a136\n"
  "v_accvgpr_read_b32 v235, a140\n"
  "v_mfma_f32_16x16x32_bf16 a[244:247], v[160:163], v[216:219], a[244:247]\n"
  "v_accvgpr_read_b32 v236, a144\n"
  "v_accvgpr_read_b32 v237, a148\n"
  "s_cmp_lt_u32 s74, 2\n"
  "s_cbranch_scc1 L_fr6\n"
  "ds_read_b128 v[112:115], v249 offset:35072\n"
  "ds_read_b128 v[116:119], v249 offset:36096\n"
  "ds_read_b128 v[120:123], v249 offset:37120\n"
  "ds_read_b128 v[124:127], v249 offset:38144\n"
  "ds_read_b128 v[228:231], v250 offset:2048\n"
  "L_fr6:\n"
  "v_mfma_f32_16x16x32_bf16 a[192:195], v[172:175], v[152:155], a[192:195]\n"
  "ds_read_b128 v[208:211], v240 offset:6144\n"
  "ds_read_b128 v[212:215], v241 offset:6144\n"
  "v_mfma_f32_16x16x32_bf16 a[196:199], v[176:179], v[152:155], a[196:199]\n"
  "ds_read_b128 v[216:219], v242 offset:6144\n"
  "v_accvgpr_read_b32 v238, a152\n"
  "v_mfma_f32_16x16x32_bf16 a[200:203], v[180:183], v[152:155], a[200:203]\n"
  "v_accvgpr_read_b32 v239, a156\n"
  "v_cvt_pk_bf16_f32 v96, v232, v233\n"
  "v_mfma_f32_16x16x32_bf16 a[204:207], v[184:187], v[152:155], a[204:207]\n"
  "v_cvt_pk_bf16_f32 v97, v234, v235\n"
  "v_cvt_pk_bf16_f32 v98, v236, v237\n"
  "v_mfma_f32_16x16x32_bf16 a[208:211], v[188:191], v[152:155], a[208:211]\n"
  "v_cvt_pk_bf16_f32 v99, v238, v239\n"
  "v_mfma_f32_16x16x32_bf16 a[212:215], v[192:195], v[152:155], a[212:215]\n"
  "buffer_store_dwordx4 v[96:99], v243, s[52:55], s86 offen offset:0 sc1\n"
  "v_mfma_f32_16x16x32_bf16 a[216:219], v[196:199], v[152:155], a[216:219]\n"
  "v_accvgpr_read_b32 v220, a129\n"
  "v_accvgpr_read_b32 v221, a133\n"
  "v_mfma_f32_16x16x32_bf16 a[220:223], v[200:203], v[152:155], a[220:223]\n"
  "v_accvgpr_read_b32 v222, a137\n"
  "v_accvgpr_read_b32 v223, a141\n"
  "v_mfma_f32_16x16x32_bf16 a[248:251], v[164:167], v[152:155], a[248:251]\n"
  "s_waitcnt lgkmcnt(0)\n"
  "v_accvgpr_read_b32 v224, a145\n"
  "v_accvgpr_read_b32 v225, a149\n"
  "v_mfma_f32_16x16x32_bf16 a[192:195], v[0:3], v[208:211], a[192:195]\n"
  "v_accvgpr_read_b32 v226, a153\n"
  "v_accvgpr_read_b32 v227, a157\n"
  "v_mfma_f32_16x16x32_bf16 a[196:199], v[4:7], v[208:211], a[196:199]\n"
  "v_cvt_pk_bf16_f32 v100, v220, v221\n"
  "v_cvt_pk_bf16_f32 v101, v222, v223\n"
  "v_mfma_f32_16x16x32_bf16 a[200:203], v[8:11], v[208:211], a[200:203]\n"
  "v_cvt_pk_bf16_f32 v102, v224, v225\n"
  "v_cvt_pk_bf16_f32 v103, v226, v227\n"
  "v_mfma_f32_16x16x32_bf16 a[204:207], v[12:15], v[208:211], a[204:207]\n"
  "buffer_store_dwordx4 v[100:103], v243, s[52:55], s86 offen offset:1024 sc1\n"
  "v_mfma_f32_16x16x32_bf16 a[208:211], v[16:19], v[208:211], a[208:211]\n"
  "v_accvgpr_read_b32 v232, a130\n"
  "v_accvgpr_read_b32 v233, a134\n"
  "v_mfma_f32_16x16x32_bf16 a[212:215], v[20:23], v[208:211], a[212:215]\n"
  "v_accvgpr_read_b32 v234, a138\n"
  "v_accvgpr_read_b32 v235, a142\n"
  "v_mfma_f32_16x16x32_bf16 a[216:219], v[24:27], v[208:211], a[216:219]\n"
  "v_accvgpr_read_b32 v236, a146\n"
  "v_accvgpr_read_b32 v237, a150\n"
  "v_mfma_f32_16x16x32_bf16 a[220:223], v[28:31], v[208:211], a[220:223]\n"
  "v_accvgpr_read_b32 v238, a154\n"
  "v_accvgpr_read_b32 v239, a158\n"
  "v_mfma_f32_16x16x32_bf16 a[248:251], v[168:171], v[208:211], a[248:251]\n"
  "v_cvt_pk_bf16_f32 v104, v232, v233\n"
  "v_cvt_pk_bf16_f32 v105, v234, v235\n"
  "v_mfma_f32_16x16x32_bf16 a[192:195], v[32:35], v[212:215], a[192:195]\n"
  "v_cvt_pk_bf16_f32 v106, v236, v237\n"
  "v_cvt_pk_bf16_f32 v107, v238, v239\n"
  "v_mfma_f32_16x16x32_bf16 a[196:199], v[36:39], v[212:215], a[196:199]\n"
  "buffer_store_dwordx4 v[104:107], v243, s[52:55], s86 offen offset:2048 sc1\n"
  "v_mfma_f32_16x16x32_bf16 a[200:203], v[40:43], v[212:215], a[200:203]\n"
  "v_accvgpr_read_b32 v220, a131\n"
  "v_accvgpr_read_b32 v221, a135\n"
  "v_mfma_f32_16x16x32_bf16 a[204:207], v[44:47], v[212:215], a[204:207]\n"
  "v_accvgpr_read_b32 v222, a139\n"
  "v_accvgpr_read_b32 v223, a143\n"
  "v_mfma_f32_16x16x32_bf16 a[208:211], v[48:51], v[212:215], a[208:211]\n"
  "v_accvgpr_read_b32 v224, a147\n"
  "v_accvgpr_read_b32 v225, a151\n"
  "v_mfma_f32_16x16x32_bf16 a[212:215], v[52:55], v[212:215], a[212:215]\n"
  "v_accvgpr_read_b32 v226, a155\n"
  "v_accvgpr_read_b32 v227, a159\n"
  "v_mfma_f32_16x16x32_bf16 a[216:219], v[56:59], v[212:215], a[216:219]\n"
  "v_cvt_pk_bf16_f32 v108, v220, v221\n"
  "v_cvt_pk_bf16_f32 v109, v222, v223\n"
  "v_mfma_f32_16x16x32_bf16 a[220:223], v[60:63], v[212:215], a[220:223]\n"
  "v_cvt_pk_bf16_f32 v110, v224, v225\n"
  "v_cvt_pk_bf16_f32 v111, v226, v227\n"
  "v_mfma_f32_16x16x32_bf16 a[248:251], v[156:159], v[212:215], a[248:251]\n"
  "buffer_store_dwordx4 v[108:111], v243, s[52:55], s86 offen offset:3072 sc1\n"
  "s_cmp_lt_u32 s74, 2\n"
  "s_cbranch_scc1 L_fa6\n"
  "v_mfma_f32_16x16x32_bf16 a[192:195], v[130:133], v[112:115], a[192:195]\n"
  "v_mfma_f32_16x16x32_bf16 a[196:199], v[128:131], v[112:115], a[196:199]\n"
  "v_mfma_f32_16x16x32_bf16 a[200:203], v[130:133], v[116:119], a[200:203]\n"
  "v_mfma_f32_16x16x32_bf16 a[204:207], v[128:131], v[116:119], a[204:207]\n"
  "v_mfma_f32_16x16x32_bf16 a[208:211], v[130:133], v[120:123], a[208:211]\n"
  "v_mfma_f32_16x16x32_bf16 a[212:215], v[128:131], v[120:123], a[212:215]\n"
  "v_mfma_f32_16x16x32_bf16 a[216:219], v[130:133], v[124:127], a[216:219]\n"
  "v_mfma_f32_16x16x32_bf16 a[220:223], v[128:131], v[124:127], a[220:223]\n"
  "v_mfma_f32_16x16x32_bf16 a[248:251], v[130:133], v[228:231], a[248:251]\n"
  "L_fa6:\n"
  "v_mfma_f32_16x16x32_bf16 a[192:195], v[64:67], v[216:219], a[192:195]\n"
  "v_accvgpr_read_b32 v232, a240\n"
  "v_accvgpr_read_b32 v233, a241\n"
  "v_mfma_f32_16x16x32_bf16 a[196:199], v[68:71], v[216:219], a[196:199]\n"
  "v_accvgpr_read_b32 v234, a242\n"
  "v_accvgpr_read_b32 v235, a243\n"
  "v_mfma_f32_16x16x32_bf16 a[200:203], v[72:75], v[216:219], a[200:203]\n"
  "v_cvt_pk_bf16_f32 v204, v232, v233\n"
  "v_cvt_pk_bf16_f32 v205, v234, v235\n"
  "v_mfma_f32_16x16x32_bf16 a[204:207], v[76:79], v[216:219], a[204:207]\n"
  "buffer_store_dwordx2 v[204:205], v248, s[52:55], s86 offen sc1\n"
  "v_mfma_f32_16x16x32_bf16 a[208:211], v[80:83], v[216:219], a[208:211]\n"
  "s_add_u32 s86, s86, 4608\n"
  "s_cmp_eq_u32 s86, 32256\n"
  "s_cselect_b32 s86, 0, s86\n"
  "v_mfma_f32_16x16x32_bf16 a[212:215], v[84:87], v[216:219], a[212:215]\n"
  "v_accvgpr_read_b32 v232, a160\n"
  "v_accvgpr_read_b32 v233, a164\n"
  "v_mfma_f32_16x16x32_bf16 a[216:219], v[88:91], v[216:219], a[216:219]\n"
  "v_accvgpr_read_b32 v234, a168\n"
  "v_accvgpr_read_b32 v235, a172\n"
  "v_mfma_f32_16x16x32_bf16 a[220:223], v[92:95], v[216:219], a[220:223]\n"
  "v_accvgpr_read_b32 v236, a176\n"
  "v_accvgpr_read_b32 v237, a180\n"
  "v_mfma_f32_16x16x32_bf16 a[248:251], v[160:163], v[216:219], a[248:251]\n"
  "v_accvgpr_read_b32 v238, a184\n"
  "v_accvgpr_read_b32 v239, a188\n"
  "v_cvt_pk_bf16_f32 v96, v232, v233\n"
  "v_cvt_pk_bf16_f32 v97, v234, v235\n"
  "v_cvt_pk_bf16_f32 v98, v236, v237\n"
  "v_cvt_pk_bf16_f32 v99, v238, v239\n"
  "buffer_store_dwordx4 v[96:99], v243, s[52:55], s86 offen offset:0 sc1\n"
  "v_accvgpr_read_b32 v220, a161\n"
  "v_accvgpr_read_b32 v221, a165\n"
  "v_accvgpr_read_b32 v222, a169\n"
  "v_accvgpr_read_b32 v223, a173\n"
  "v_accvgpr_read_b32 v224, a177\n"
  "v_accvgpr_read_b32 v225, a181\n"
  "v_accvgpr_read_b32 v226, a185\n"
  "v_accvgpr_read_b32 v227, a189\n"
  "v_cvt_pk_bf16_f32 v100, v220, v221\n"
  "v_cvt_pk_bf16_f32 v101, v222, v223\n"
  "v_cvt_pk_bf16_f32 v102, v224, v225\n"
  "v_cvt_pk_bf16_f32 v103, v226, v227\n"
  "buffer_store_dwordx4 v[100:103], v243, s[52:55], s86 offen offset:1024 sc1\n"
  "v_accvgpr_read_b32 v232, a162\n"
  "v_accvgpr_read_b32 v233, a166\n"
  "v_accvgpr_read_b32 v234, a170\n"
  "v_accvgpr_read_b32 v235, a174\n"
  "v_accvgpr_read_b32 v236, a178\n"
  "v_accvgpr_read_b32 v237, a182\n"
  "v_accvgpr_read_b32 v238, a186\n"
  "v_accvgpr_read_b32 v239, a190\n"
  "v_cvt_pk_bf16_f32 v104, v232, v233\n"
  "v_cvt_pk_bf16_f32 v105, v234, v235\n"
  "v_cvt_pk_bf16_f32 v106, v236, v237\n"
  "v_cvt_pk_bf16_f32 v107, v238, v239\n"
  "buffer_store_dwordx4 v[104:107], v243, s[52:55], s86 offen offset:2048 sc1\n"
  "v_accvgpr_read_b32 v220, a163\n"
  "v_accvgpr_read_b32 v221, a167\n"
  "v_accvgpr_read_b32 v222, a171\n"
  "v_accvgpr_read_b32 v223, a175\n"
  "v_accvgpr_read_b32 v224, a179\n"
  "v_accvgpr_read_b32 v225, a183\n"
  "v_accvgpr_read_b32 v226, a187\n"
  "v_accvgpr_read_b32 v227, a191\n"
  "v_cvt_pk_bf16_f32 v108, v220, v221\n"
  "v_cvt_pk_bf16_f32 v109, v222, v223\n"
  "v_cvt_pk_bf16_f32 v110, v224, v225\n"
  "v_cvt_pk_bf16_f32 v111, v226, v227\n"
  "buffer_store_dwordx4 v[108:111], v243, s[52:55], s86 offen offset:3072 sc1\n"
  "v_accvgpr_read_b32 v232, a244\n"
  "v_accvgpr_read_b32 v233, a245\n"
  "v_accvgpr_read_b32 v234, a246\n"
  "v_accvgpr_read_b32 v235, a247\n"
  "v_cvt_pk_bf16_f32 v206, v232, v233\n"
  "v_cvt_pk_bf16_f32 v207, v234, v235\n"
  "buffer_store_dwordx2 v[206:207], v248, s[52:55], s86 offen sc1\n"
  "s_cmp_eq_u32 s92, 7\n"
  "s_cbranch_scc0 L_keep\n"
  "s_nop 7\n"
  "s_add_u32 s86, s86, 4608\n"
  "s_cmp_eq_u32 s86, 32256\n"
  "s_cselect_b32 s86, 0, s86\n"
  "v_accvgpr_read_b32 v232, a192\n"
  "v_accvgpr_read_b32 v233, a196\n"
  "v_accvgpr_read_b32 v234, a200\n"
  "v_accvgpr_read_b32 v235, a204\n"
  "v_accvgpr_read_b32 v236, a208\n"
  "v_accvgpr_read_b32 v237, a212\n"
  "v_accvgpr_read_b32 v238, a216\n"
  "v_accvgpr_read_b32 v239, a220\n"
  "v_cvt_pk_bf16_f32 v96, v232, v233\n"
  "v_cvt_pk_bf16_f32 v97, v234, v235\n"
  "v_cvt_pk_bf16_f32 v98, v236, v237\n"
  "v_cvt_pk_bf16_f32 v99, v238, v239\n"
  "buffer_store_dwordx4 v[96:99], v243, s[52:55], s86 offen offset:0 sc1\n"
  "v_accvgpr_read_b32 v220, a193\n"
  "v_accvgpr_read_b32 v221, a197\n"
  "v_accvgpr_read_b32 v222, a201\n"
  "v_accvgpr_read_b32 v223, a205\n"
  "v_accvgpr_read_b32 v224, a209\n"
  "v_accvgpr_read_b32 v225, a213\n"
  "v_accvgpr_read_b32 v226, a217\n"
  "v_accvgpr_read_b32 v227, a221\n"
  "v_cvt_pk_bf16_f32 v100, v220, v221\n"
  "v_cvt_pk_bf16_f32 v101, v222, v223\n"
  "v_cvt_pk_bf16_f32 v102, v224, v225\n"
  "v_cvt_pk_bf16_f32 v103, v226, v227\n"
  "buffer_store_dwordx4 v[100:103], v243, s[52:55], s86 offen offset:1024 sc1\n"
  "v_accvgpr_read_b32 v232, a194\n"
  "v_accvgpr_read_b32 v233, a198\n"
  "v_accvgpr_read_b32 v234, a202\n"
  "v_accvgpr_read_b32 v235, a206\n"
  "v_accvgpr_read_b32 v236, a210\n"
  "v_accvgpr_read_b32 v237, a214\n"
  "v_accvgpr_read_b32 v238, a218\n"
  "v_accvgpr_read_b32 v239, a222\n"
  "v_cvt_pk_bf16_f32 v104, v232, v233\n"
  "v_cvt_pk_bf16_f32 v105, v234, v235\n"
  "v_cvt_pk_bf16_f32 v106, v236, v237\n"
  "v_cvt_pk_bf16_f32 v107, v238, v239\n"
  "buffer_store_dwordx4 v[104:107], v243, s[52:55], s86 offen offset:2048 sc1\n"
  "v_accvgpr_read_b32 v220, a195\n"
  "v_accvgpr_read_b32 v221, a199\n"
  "v_accvgpr_read_b32 v222, a203\n"
  "v_accvgpr_read_b32 v223, a207\n"
  "v_accvgpr_read_b32 v224, a211\n"
  "v_accvgpr_read_b32 v225, a215\n"
  "v_accvgpr_read_b32 v226, a219\n"
  "v_accvgpr_read_b32 v227, a223\n"
  "v_cvt_pk_bf16_f32 v108, v220, v221\n"
  "v_cvt_pk_bf16_f32 v109, v222, v223\n"
  "v_cvt_pk_bf16_f32 v110, v224, v225\n"
  "v_cvt_pk_bf16_f32 v111, v226, v227\n"
  "buffer_store_dwordx4 v[108:111], v243, s[52:55], s86 offen offset:3072 sc1\n"
  "v_accvgpr_read_b32 v232, a248\n"
  "v_accvgpr_read_b32 v233, a249\n"
  "v_accvgpr_read_b32 v234, a250\n"
  "v_accvgpr_read_b32 v235, a251\n"
  "v_cvt_pk_bf16_f32 v204, v232, v233\n"
  "v_cvt_pk_bf16_f32 v205, v234, v235\n"
  "buffer_store_dwordx2 v[204:205], v248, s[52:55], s86 offen sc1\n"
  "L_keep:\n"
  "s_lshl_b32 s86, s92, 2\n"
  "v_lshl_add_u32 v240, v252, 8, s86\n"
  "s_lshl_b32 s86, s92, 8\n"
  "v_lshrrev_b32 v241, 3, v252\n"
  "v_lshl_add_u32 v241, v241, 9, v252\n"
  "v_lshl_add_u32 v241, v241, 2, s86\n"
  "s_mul_i32 s86, s92, 126464\n"
  "s_sub_u32 s44, s52, s86\n"
  "s_subb_u32 s45, s53, 0\n"
  "s_mov_b32 s46, 922112\n"
  "s_mov_b64 s[48:49], %6\n"
  "s_and_b32 s49, s49, 0xffff\n"
  "s_mov_b32 s50, 256896\n"
  "s_mov_b32 s51, 0x20000\n"
  "s_mov_b32 s93, 0\n"
  "v_accvgpr_read_b32 v144, a192\n"
  "v_accvgpr_read_b32 v145, a196\n"
  "v_accvgpr_read_b32 v146, a200\n"
  "v_accvgpr_read_b32 v147, a204\n"
  "v_accvgpr_read_b32 v148, a208\n"
  "v_accvgpr_read_b32 v149, a212\n"
  "v_accvgpr_read_b32 v150, a216\n"
  "v_accvgpr_read_b32 v151, a220\n"
  "v_accvgpr_read_b32 v152, a193\n"
  "v_accvgpr_read_b32 v153, a197\n"
  "v_accvgpr_read_b32 v154, a201\n"
  "v_accvgpr_read_b32 v155, a205\n"
  "v_accvgpr_read_b32 v156, a209\n"
  "v_accvgpr_read_b32 v157, a213\n"
  "v_accvgpr_read_b32 v158, a217\n"
  "v_accvgpr_read_b32 v159, a221\n"
  "v_accvgpr_read_b32 v160, a194\n"
  "v_accvgpr_read_b32 v161, a198\n"
  "v_accvgpr_read_b32 v162, a202\n"
  "v_accvgpr_read_b32 v163, a206\n"
  "v_accvgpr_read_b32 v164, a210\n"
  "v_accvgpr_read_b32 v165, a214\n"
  "v_accvgpr_read_b32 v166, a218\n"
  "v_accvgpr_read_b32 v167, a222\n"
  "v_accvgpr_read_b32 v168, a195\n"
  "v_accvgpr_read_b32 v169, a199\n"
  "v_accvgpr_read_b32 v170, a203\n"
  "v_accvgpr_read_b32 v171, a207\n"
  "v_accvgpr_read_b32 v172, a211\n"
  "v_accvgpr_read_b32 v173, a215\n"
  "v_accvgpr_read_b32 v174, a219\n"
  "v_accvgpr_read_b32 v175, a223\n"
  "v_accvgpr_read_b32 v176, a248\n"
  "v_accvgpr_read_b32 v177, a249\n"
  "v_accvgpr_read_b32 v178, a250\n"
  "v_accvgpr_read_b32 v179, a251\n"
  "v_lshrrev_b32 v246, 4, v252\n"
  "v_and_b32 v247, 15, v252\n"
  "v_mul_u32_u24 v247, 2304, v247\n"
  "s_mov_b32 s65, 0xffff0000\n"
  "v_add_u32 v220, s72, v243\n"
  "v_add_u32 v221, 66048, v220\n"
  "v_and_b32 v222, 3, v252\n"
  "v_mul_u32_u24 v222, 33024, v222\n"
  "v_bfe_u32 v223, v252, 2, 2\n"
  "v_lshl_add_u32 v222, v223, 8, v222\n"
  "v_lshl_add_u32 v222, v246, 4, v222\n"
  "s_add_u32 s86, s72, 7168\n"
  "v_add_u32 v222, s86, v222\n"
  "v_mul_u32_u24 v223, 2304, v246\n"
  "v_and_b32 v224, 15, v252\n"
  "v_lshl_add_u32 v223, v224, 4, v223\n"
  "s_lshl_b32 s86, s92, 17\n"
  "s_waitcnt vmcnt(0)\n"
  "buffer_store_dword v255, v240, s[88:91], 0 offen offset:128 sc1\n"
  "L_pollA:\n"
  "buffer_load_dword v242, v241, s[88:91], 0 offen offset:128 sc1\n"
  "buffer_load_dword v244, v240, s[88:91], 0 offen sc1\n"
  "s_add_u32 s93, s93, 1\n"
  "s_waitcnt vmcnt(0)\n"
  "v_cmp_ne_u32 vcc, v244, v255\n"
  "s_cbranch_vccz L_rfA\n"
  "v_mov_b32 v255, v244\n"
  "buffer_store_dword v255, v240, s[88:91], 0 offen offset:128 sc1\n"
  "L_rfA:\n"
  "v_cmp_ne_u32 vcc, v242, v254\n"
  "s_nop 0\n"
  "s_and_b64 vcc, vcc, 0x7f\n"
  "s_cbranch_scc0 L_goA\n"
  "s_cmp_lt_u32 s93, 0x80000\n"
  "s_cbranch_scc1 L_pollA\n"
  "L_goA:\n"
  "s_cmp_eq_u32 s92, 7\n"
  "s_cbranch_scc1 L_last\n"
  "s_add_u32 s86, s86, 0x20000\n"
  "s_cmp_eq_u32 s86, 0xe0000\n"
  "s_cselect_b32 s86, 0, s86\n"
  "buffer_load_dwordx4 v[0:3], v243, s[44:47], s86 offen offset:0 sc1\n"
  "buffer_load_dwordx4 v[4:7], v243, s[44:47], s86 offen offset:1024 sc1\n"
  "buffer_load_dwordx4 v[8:11], v243, s[44:47], s86 offen offset:2048 sc1\n"
  "buffer_load_dwordx4 v[12:15], v243, s[44:47], s86 offen offset:3072 sc1\n"
  "buffer_load_dwordx2 v[16:17], v248, s[44:47], s86 offen sc1\n"
  "s_add_u32 s86, s86, 0x20000\n"
  "s_cmp_eq_u32 s86, 0xe0000\n"
  "s_cselect_b32 s86, 0, s86\n"
  "buffer_load_dwordx4 v[20:23], v243, s[44:47], s86 offen offset:0 sc1\n"
  "buffer_load_dwordx4 v[24:27], v243, s[44:47], s86 offen offset:1024 sc1\n"
  "buffer_load_dwordx4 v[28:31], v243, s[44:47], s86 offen offset:2048 sc1\n"
  "buffer_load_dwordx4 v[32:35], v243, s[44:47], s86 offen offset:3072 sc1\n"
  "buffer_load_dwordx2 v[36:37], v248, s[44:47], s86 offen sc1\n"
  "buffer_load_dword v242, v241, s[88:91], 0 offen offset:128 sc1\n"
  "s_add_u32 s86, s86, 0x20000\n"
  "s_cmp_eq_u32 s86, 0xe0000\n"
  "s_cselect_b32 s86, 0, s86\n"
  "buffer_load_dwordx4 v[40:43], v243, s[44:47], s86 offen offset:0 sc1\n"
  "buffer_load_dwordx4 v[44:47], v243, s[44:47], s86 offen offset:1024 sc1\n"
  "buffer_load_dwordx4 v[48:51], v243, s[44:47], s86 offen offset:2048 sc1\n"
  "buffer_load_dwordx4 v[52:55], v243, s[44:47], s86 offen offset:3072 sc1\n"
  "buffer_load_dwordx2 v[56:57], v248, s[44:47], s86 offen sc1\n"
  "s_add_u32 s86, s86, 0x20000\n"
  "s_cmp_eq_u32 s86, 0xe0000\n"
  "s_cselect_b32 s86, 0, s86\n"
  "buffer_load_dwordx4 v[60:63], v243, s[44:47], s86 offen offset:0 sc1\n"
  "buffer_load_dwordx4 v[64:67], v243, s[44:47], s86 offen offset:1024 sc1\n"
  "buffer_load_dwordx4 v[68:71], v243, s[44:47], s86 offen offset:2048 sc1\n"
  "buffer_load_dwordx4 v[72:75], v243, s[44:47], s86 offen offset:3072 sc1\n"
  "buffer_load_dwordx2 v[76:77], v248, s[44:47], s86 offen sc1\n"
  "s_add_u32 s86, s86, 0x20000\n"
  "s_cmp_eq_u32 s86, 0xe0000\n"
  "s_cselect_b32 s86, 0, s86\n"
  "buffer_load_dwordx4 v[80:83], v243, s[44:47], s86 offen offset:0 sc1\n"
  "buffer_load_dwordx4 v[84:87], v243, s[44:47], s86 offen offset:1024 sc1\n"
  "buffer_load_dwordx4 v[88:91], v243, s[44:47], s86 offen offset:2048 sc1\n"
  "buffer_load_dwordx4 v[92:95], v243, s[44:47], s86 offen offset:3072 sc1\n"
  "buffer_load_dwordx2 v[96:97], v248, s[44:47], s86 offen sc1\n"
  "s_add_u32 s86, s86, 0x20000\n"
  "s_cmp_eq_u32 s86, 0xe0000\n"
  "s_cselect_b32 s86, 0, s86\n"
  "buffer_load_dwordx4 v[100:103], v243, s[44:47], s86 offen offset:0 sc1\n"
  "buffer_load_dwordx4 v[104:107], v243, s[44:47], s86 offen offset:1024 sc1\n"
  "buffer_load_dwordx4 v[108:111], v243, s[44:47], s86 offen offset:2048 sc1\n"
  "buffer_load_dwordx4 v[112:115], v243, s[44:47], s86 offen offset:3072 sc1\n"
  "buffer_load_dwordx2 v[116:117], v248, s[44:47], s86 offen sc1\n"
  "s_waitcnt vmcnt(20)\n"
  "v_cmp_ne_u32 vcc, v242, v254\n"
  "s_nop 0\n"
  "s_and_b64 vcc, vcc, 0x80\n"
  "s_cbranch_scc0 L_goB\n"
  "L_pollB:\n"
  "buffer_load_dword v242, v241, s[88:91], 0 offen offset:128 sc1\n"
  "buffer_load_dword v244, v240, s[88:91], 0 offen sc1\n"
  "s_add_u32 s93, s93, 1\n"
  "s_waitcnt vmcnt(0)\n"
  "v_cmp_ne_u32 vcc, v244, v255\n"
  "s_cbranch_vccz L_rfB\n"
  "v_mov_b32 v255, v244\n"
  "buffer_store_dword v255, v240, s[88:91], 0 offen offset:128 sc1\n"
  "L_rfB:\n"
  "v_cmp_ne_u32 vcc, v242, v254\n"
  "s_nop 0\n"
  "s_and_b64 vcc, vcc, 0x80\n"
  "s_cbranch_scc0 L_goB\n"
  "s_cmp_lt_u32 s93, 0x80000\n"
  "s_cbranch_scc1 L_pollB\n"
  "L_goB:\n"
  "s_mov_b32 s86, 0xe0000\n"
  "buffer_load_dwordx4 v[120:123], v243, s[44:47], s86 offen offset:0 sc1\n"
  "buffer_load_dwordx4 v[124:127], v243, s[44:47], s86 offen offset:1024 sc1\n"
  "buffer_load_dwordx4 v[128:131], v243, s[44:47], s86 offen offset:2048 sc1\n"
  "buffer_load_dwordx4 v[132:135], v243, s[44:47], s86 offen offset:3072 sc1\n"
  "buffer_load_dwordx2 v[136:137], v248, s[44:47], s86 offen sc1\n"
  "buffer_load_dword v244, v240, s[88:91], 0 offen sc1\n"
  "s_waitcnt vmcnt(32)\n"
  "v_lshlrev_b32 v180, 16, v0\n"
  "v_and_b32 v181, s65, v0\n"
  "v_lshlrev_b32 v182, 16, v1\n"
  "v_and_b32 v183, s65, v1\n"
  "v_lshlrev_b32 v184, 16, v2\n"
  "v_and_b32 v185, s65, v2\n"
  "v_lshlrev_b32 v186, 16, v3\n"
  "v_and_b32 v187, s65, v3\n"
  "v_lshlrev_b32 v188, 16, v4\n"
  "v_and_b32 v189, s65, v4\n"
  "v_lshlrev_b32 v190, 16, v5\n"
  "v_and_b32 v191, s65, v5\n"
  "v_lshlrev_b32 v192, 16, v6\n"
  "v_and_b32 v193, s65, v6\n"
  "v_lshlrev_b32 v194, 16, v7\n"
  "v_and_b32 v195, s65, v7\n"
  "v_lshlrev_b32 v196, 16, v8\n"
  "v_and_b32 v197, s65, v8\n"
  "v_lshlrev_b32 v198, 16, v9\n"
  "v_and_b32 v199, s65, v9\n"
  "v_lshlrev_b32 v200, 16, v10\n"
  "v_and_b32 v201, s65, v10\n"
  "v_lshlrev_b32 v202, 16, v11\n"
  "v_and_b32 v203, s65, v11\n"
  "v_lshlrev_b32 v204, 16, v12\n"
  "v_and_b32 v205, s65, v12\n"
  "v_lshlrev_b32 v206, 16, v13\n"
  "v_and_b32 v207, s65, v13\n"
  "v_lshlrev_b32 v208, 16, v14\n"
  "v_and_b32 v209, s65, v14\n"
  "v_lshlrev_b32 v210, 16, v15\n"
  "v_and_b32 v211, s65, v15\n"
  "v_lshlrev_b32 v212, 16, v16\n"
  "v_and_b32 v213, s65, v16\n"
  "v_lshlrev_b32 v214, 16, v17\n"
  "v_and_b32 v215, s65, v17\n"
  "v_pk_add_f32 v[144:145], v[144:145], v[180:181]\n"
  "v_pk_add_f32 v[146:147], v[146:147], v[182:183]\n"
  "v_pk_add_f32 v[148:149], v[148:149], v[184:185]\n"
  "v_pk_add_f32 v[150:151], v[150:151], v[186:187]\n"
  "v_pk_add_f32 v[152:153], v[152:153], v[188:189]\n"
  "v_pk_add_f32 v[154:155], v[154:155], v[190:191]\n"
  "v_pk_add_f32 v[156:157], v[156:157], v[192:193]\n"
  "v_pk_add_f32 v[158:159], v[158:159], v[194:195]\n"
  "v_pk_add_f32 v[160:161], v[160:161], v[196:197]\n"
  "v_pk_add_f32 v[162:163], v[162:163], v[198:199]\n"
  "v_pk_add_f32 v[164:165], v[164:165], v[200:201]\n"
  "v_pk_add_f32 v[166:167], v[166:167], v[202:203]\n"
  "v_pk_add_f32 v[168:169], v[168:169], v[204:205]\n"
  "v_pk_add_f32 v[170:171], v[170:171], v[206:207]\n"
  "v_pk_add_f32 v[172:173], v[172:173], v[208:209]\n"
  "v_pk_add_f32 v[174:175], v[174:175], v[210:211]\n"
  "v_pk_add_f32 v[176:177], v[176:177], v[212:213]\n"
  "v_pk_add_f32 v[178:179], v[178:179], v[214:215]\n"
  "s_waitcnt vmcnt(27)\n"
  "v_lshlrev_b32 v180, 16, v20\n"
  "v_and_b32 v181, s65, v20\n"
  "v_lshlrev_b32 v182, 16, v21\n"
  "v_and_b32 v183, s65, v21\n"
  "v_lshlrev_b32 v184, 16, v22\n"
  "v_and_b32 v185, s65, v22\n"
  "v_lshlrev_b32 v186, 16, v23\n"
  "v_and_b32 v187, s65, v23\n"
  "v_lshlrev_b32 v188, 16, v24\n"
  "v_and_b32 v189, s65, v24\n"
  "v_lshlrev_b32 v190, 16, v25\n"
  "v_and_b32 v191, s65, v25\n"
  "v_lshlrev_b32 v192, 16, v26\n"
  "v_and_b32 v193, s65, v26\n"
  "v_lshlrev_b32 v194, 16, v27\n"
  "v_and_b32 v195, s65, v27\n"
  "v_lshlrev_b32 v196, 16, v28\n"
  "v_and_b32 v197, s65, v28\n"
  "v_lshlrev_b32 v198, 16, v29\n"
  "v_and_b32 v199, s65, v29\n"
  "v_lshlrev_b32 v200, 16, v30\n"
  "v_and_b32 v201, s65, v30\n"
  "v_lshlrev_b32 v202, 16, v31\n"
  "v_and_b32 v203, s65, v31\n"
  "v_lshlrev_b32 v204, 16, v32\n"
  "v_and_b32 v205, s65, v32\n"
  "v_lshlrev_b32 v206, 16, v33\n"
  "v_and_b32 v207, s65, v33\n"
  "v_lshlrev_b32 v208, 16, v34\n"
  "v_and_b32 v209, s65, v34\n"
  "v_lshlrev_b32 v210, 16, v35\n"
  "v_and_b32 v211, s65, v35\n"
  "v_lshlrev_b32 v212, 16, v36\n"
  "v_and_b32 v213, s65, v36\n"
  "v_lshlrev_b32 v214, 16, v37\n"
  "v_and_b32 v215, s65, v37\n"
  "v_pk_add_f32 v[144:145], v[144:145], v[180:181]\n"
  "v_pk_add_f32 v[146:147], v[146:147], v[182:183]\n"
  "v_pk_add_f32 v[148:149], v[148:149], v[184:185]\n"
  "v_pk_add_f32 v[150:151], v[150:151], v[186:187]\n"
  "v_pk_add_f32 v[152:153], v[152:153], v[188:189]\n"
  "v_pk_add_f32 v[154:155], v[154:155], v[190:191]\n"
  "v_pk_add_f32 v[156:157], v[156:157], v[192:193]\n"
  "v_pk_add_f32 v[158:159], v[158:159], v[194:195]\n"
  "v_pk_add_f32 v[160:161], v[160:161], v[196:197]\n"
  "v_pk_add_f32 v[162:163], v[162:163], v[198:199]\n"
  "v_pk_add_f32 v[164:165], v[164:165], v[200:201]\n"
  "v_pk_add_f32 v[166:167], v[166:167], v[202:203]\n"
  "v_pk_add_f32 v[168:169], v[168:169], v[204:205]\n"
  "v_pk_add_f32 v[170:171], v[170:171], v[206:207]\n"
  "v_pk_add_f32 v[172:173], v[172:173], v[208:209]\n"
  "v_pk_add_f32 v[174:175], v[174:175], v[210:211]\n"
  "v_pk_add_f32 v[176:177], v[176:177], v[212:213]\n"
  "v_pk_add_f32 v[178:179], v[178:179], v[214:215]\n"
  "s_waitcnt vmcnt(21)\n"
  "v_lshlrev_b32 v180, 16, v40\n"
  "v_and_b32 v181, s65, v40\n"
  "v_lshlrev_b32 v182, 16, v41\n"
  "v_and_b32 v183, s65, v41\n"
  "v_lshlrev_b32 v184, 16, v42\n"
  "v_and_b32 v185, s65, v42\n"
  "v_lshlrev_b32 v186, 16, v43\n"
  "v_and_b32 v187, s65, v43\n"
  "v_lshlrev_b32 v188, 16, v44\n"
  "v_and_b32 v189, s65, v44\n"
  "v_lshlrev_b32 v190, 16, v45\n"
  "v_and_b32 v191, s65, v45\n"
  "v_lshlrev_b32 v192, 16, v46\n"
  "v_and_b32 v193, s65, v46\n"
  "v_lshlrev_b32 v194, 16, v47\n"
  "v_and_b32 v195, s65, v47\n"
  "v_lshlrev_b32 v196, 16, v48\n"
  "v_and_b32 v197, s65, v48\n"
  "v_lshlrev_b32 v198, 16, v49\n"
  "v_and_b32 v199, s65, v49\n"
  "v_lshlrev_b32 v200, 16, v50\n"
  "v_and_b32 v201, s65, v50\n"
  "v_lshlrev_b32 v202, 16, v51\n"
  "v_and_b32 v203, s65, v51\n"
  "v_lshlrev_b32 v204, 16, v52\n"
  "v_and_b32 v205, s65, v52\n"
  "v_lshlrev_b32 v206, 16, v53\n"
  "v_and_b32 v207, s65, v53\n"
  "v_lshlrev_b32 v208, 16, v54\n"
  "v_and_b32 v209, s65, v54\n"
  "v_lshlrev_b32 v210, 16, v55\n"
  "v_and_b32 v211, s65, v55\n"
  "v_lshlrev_b32 v212, 16, v56\n"
  "v_and_b32 v213, s65, v56\n"
  "v_lshlrev_b32 v214, 16, v57\n"
  "v_and_b32 v215, s65, v57\n"
  "v_pk_add_f32 v[144:145], v[144:145], v[180:181]\n"
  "v_pk_add_f32 v[146:147], v[146:147], v[182:183]\n"
  "v_pk_add_f32 v[148:149], v[148:149], v[184:185]\n"
  "v_pk_add_f32 v[150:151], v[150:151], v[186:187]\n"
  "v_pk_add_f32 v[152:153], v[152:153], v[188:189]\n"
  "v_pk_add_f32 v[154:155], v[154:155], v[190:191]\n"
  "v_pk_add_f32 v[156:157], v[156:157], v[192:193]\n"
  "v_pk_add_f32 v[158:159], v[158:159], v[194:195]\n"
  "v_pk_add_f32 v[160:161], v[160:161], v[196:197]\n"
  "v_pk_add_f32 v[162:163], v[162:163], v[198:199]\n"
  "v_pk_add_f32 v[164:165], v[164:165], v[200:201]\n"
  "v_pk_add_f32 v[166:167], v[166:167], v[202:203]\n"
  "v_pk_add_f32 v[168:169], v[168:169], v[204:205]\n"
  "v_pk_add_f32 v[170:171], v[170:171], v[206:207]\n"
  "v_pk_add_f32 v[172:173], v[172:173], v[208:209]\n"
  "v_pk_add_f32 v[174:175], v[174:175], v[210:211]\n"
  "v_pk_add_f32 v[176:177], v[176:177], v[212:213]\n"
  "v_pk_add_f32 v[178:179], v[178:179], v[214:215]\n"
  "s_waitcnt vmcnt(16)\n"
  "v_lshlrev_b32 v180, 16, v60\n"
  "v_and_b32 v181, s65, v60\n"
  "v_lshlrev_b32 v182, 16, v61\n"
  "v_and_b32 v183, s65, v61\n"
  "v_lshlrev_b32 v184, 16, v62\n"
  "v_and_b32 v185, s65, v62\n"
  "v_lshlrev_b32 v186, 16, v63\n"
  "v_and_b32 v187, s65, v63\n"
  "v_lshlrev_b32 v188, 16, v64\n"
  "v_and_b32 v189, s65, v64\n"
  "v_lshlrev_b32 v190, 16, v65\n"
  "v_and_b32 v191, s65, v65\n"
  "v_lshlrev_b32 v192, 16, v66\n"
  "v_and_b32 v193, s65, v66\n"
  "v_lshlrev_b32 v194, 16, v67\n"
  "v_and_b32 v195, s65, v67\n"
  "v_lshlrev_b32 v196, 16, v68\n"
  "v_and_b32 v197, s65, v68\n"
  "v_lshlrev_b32 v198, 16, v69\n"
  "v_and_b32 v199, s65, v69\n"
  "v_lshlrev_b32 v200, 16, v70\n"
  "v_and_b32 v201, s65, v70\n"
  "v_lshlrev_b32 v202, 16, v71\n"
  "v_and_b32 v203, s65, v71\n"
  "v_lshlrev_b32 v204, 16, v72\n"
  "v_and_b32 v205, s65, v72\n"
  "v_lshlrev_b32 v206, 16, v73\n"
  "v_and_b32 v207, s65, v73\n"
  "v_lshlrev_b32 v208, 16, v74\n"
  "v_and_b32 v209, s65, v74\n"
  "v_lshlrev_b32 v210, 16, v75\n"
  "v_and_b32 v211, s65, v75\n"
  "v_lshlrev_b32 v212, 16, v76\n"
  "v_and_b32 v213, s65, v76\n"
  "v_lshlrev_b32 v214, 16, v77\n"
  "v_and_b32 v215, s65, v77\n"
  "v_pk_add_f32 v[144:145], v[144:145], v[180:181]\n"
  "v_pk_add_f32 v[146:147], v[146:147], v[182:183]\n"
  "v_pk_add_f32 v[148:149], v[148:149], v[184:185]\n"
  "v_pk_add_f32 v[150:151], v[150:151], v[186:187]\n"
  "v_pk_add_f32 v[152:153], v[152:153], v[188:189]\n"
  "v_pk_add_f32 v[154:155], v[154:155], v[190:191]\n"
  "v_pk_add_f32 v[156:157], v[156:157], v[192:193]\n"
  "v_pk_add_f32 v[158:159], v[158:159], v[194:195]\n"
  "v_pk_add_f32 v[160:161], v[160:161], v[196:197]\n"
  "v_pk_add_f32 v[162:163], v[162:163], v[198:199]\n"
  "v_pk_add_f32 v[164:165], v[164:165], v[200:201]\n"
  "v_pk_add_f32 v[166:167], v[166:167], v[202:203]\n"
  "v_pk_add_f32 v[168:169], v[168:169], v[204:205]\n"
  "v_pk_add_f32 v[170:171], v[170:171], v[206:207]\n"
  "v_pk_add_f32 v[172:173], v[172:173], v[208:209]\n"
  "v_pk_add_f32 v[174:175], v[174:175], v[210:211]\n"
  "v_pk_add_f32 v[176:177], v[176:177], v[212:213]\n"
  "v_pk_add_f32 v[178:179], v[178:179], v[214:215]\n"
  "s_waitcnt vmcnt(11)\n"
  "v_lshlrev_b32 v180, 16, v80\n"
  "v_and_b32 v181, s65, v80\n"
  "v_lshlrev_b32 v182, 16, v81\n"
  "v_and_b32 v183, s65, v81\n"
  "v_lshlrev_b32 v184, 16, v82\n"
  "v_and_b32 v185, s65, v82\n"
  "v_lshlrev_b32 v186, 16, v83\n"
  "v_and_b32 v187, s65, v83\n"
  "v_lshlrev_b32 v188, 16, v84\n"
  "v_and_b32 v189, s65, v84\n"
  "v_lshlrev_b32 v190, 16, v85\n"
  "v_and_b32 v191, s65, v85\n"
  "v_lshlrev_b32 v192, 16, v86\n"
  "v_and_b32 v193, s65, v86\n"
  "v_lshlrev_b32 v194, 16, v87\n"
  "v_and_b32 v195, s65, v87\n"
  "v_lshlrev_b32 v196, 16, v88\n"
  "v_and_b32 v197, s65, v88\n"
  "v_lshlrev_b32 v198, 16, v89\n"
  "v_and_b32 v199, s65, v89\n"
  "v_lshlrev_b32 v200, 16, v90\n"
  "v_and_b32 v201, s65, v90\n"
  "v_lshlrev_b32 v202, 16, v91\n"
  "v_and_b32 v203, s65, v91\n"
  "v_lshlrev_b32 v204, 16, v92\n"
  "v_and_b32 v205, s65, v92\n"
  "v_lshlrev_b32 v206, 16, v93\n"
  "v_and_b32 v207, s65, v93\n"
  "v_lshlrev_b32 v208, 16, v94\n"
  "v_and_b32 v209, s65, v94\n"
  "v_lshlrev_b32 v210, 16, v95\n"
  "v_and_b32 v211, s65, v95\n"
  "v_lshlrev_b32 v212, 16, v96\n"
  "v_and_b32 v213, s65, v96\n"
  "v_lshlrev_b32 v214, 16, v97\n"
  "v_and_b32 v215, s65, v97\n"
  "v_pk_add_f32 v[144:145], v[144:145], v[180:181]\n"
  "v_pk_add_f32 v[146:147], v[146:147], v[182:183]\n"
  "v_pk_add_f32 v[148:149], v[148:149], v[184:185]\n"
  "v_pk_add_f32 v[150:151], v[150:151], v[186:187]\n"
  "v_pk_add_f32 v[152:153], v[152:153], v[188:189]\n"
  "v_pk_add_f32 v[154:155], v[154:155], v[190:191]\n"
  "v_pk_add_f32 v[156:157], v[156:157], v[192:193]\n"
  "v_pk_add_f32 v[158:159], v[158:159], v[194:195]\n"
  "v_pk_add_f32 v[160:161], v[160:161], v[196:197]\n"
  "v_pk_add_f32 v[162:163], v[162:163], v[198:199]\n"
  "v_pk_add_f32 v[164:165], v[164:165], v[200:201]\n"
  "v_pk_add_f32 v[166:167], v[166:167], v[202:203]\n"
  "v_pk_add_f32 v[168:169], v[168:169], v[204:205]\n"
  "v_pk_add_f32 v[170:171], v[170:171], v[206:207]\n"
  "v_pk_add_f32 v[172:173], v[172:173], v[208:209]\n"
  "v_pk_add_f32 v[174:175], v[174:175], v[210:211]\n"
  "v_pk_add_f32 v[176:177], v[176:177], v[212:213]\n"
  "v_pk_add_f32 v[178:179], v[178:179], v[214:215]\n"
  "s_waitcnt vmcnt(6)\n"
  "v_lshlrev_b32 v180, 16, v100\n"
  "v_and_b32 v181, s65, v100\n"
  "v_lshlrev_b32 v182, 16, v101\n"
  "v_and_b32 v183, s65, v101\n"
  "v_lshlrev_b32 v184, 16, v102\n"
  "v_and_b32 v185, s65, v102\n"
  "v_lshlrev_b32 v186, 16, v103\n"
  "v_and_b32 v187, s65, v103\n"
  "v_lshlrev_b32 v188, 16, v104\n"
  "v_and_b32 v189, s65, v104\n"
  "v_lshlrev_b32 v190, 16, v105\n"
  "v_and_b32 v191, s65, v105\n"
  "v_lshlrev_b32 v192, 16, v106\n"
  "v_and_b32 v193, s65, v106\n"
  "v_lshlrev_b32 v194, 16, v107\n"
  "v_and_b32 v195, s65, v107\n"
  "v_lshlrev_b32 v196, 16, v108\n"
  "v_and_b32 v197, s65, v108\n"
  "v_lshlrev_b32 v198, 16, v109\n"
  "v_and_b32 v199, s65, v109\n"
  "v_lshlrev_b32 v200, 16, v110\n"
  "v_and_b32 v201, s65, v110\n"
  "v_lshlrev_b32 v202, 16, v111\n"
  "v_and_b32 v203, s65, v111\n"
  "v_lshlrev_b32 v204, 16, v112\n"
  "v_and_b32 v205, s65, v112\n"
  "v_lshlrev_b32 v206, 16, v113\n"
  "v_and_b32 v207, s65, v113\n"
  "v_lshlrev_b32 v208, 16, v114\n"
  "v_and_b32 v209, s65, v114\n"
  "v_lshlrev_b32 v210, 16, v115\n"
  "v_and_b32 v211, s65, v115\n"
  "v_lshlrev_b32 v212, 16, v116\n"
  "v_and_b32 v213, s65, v116\n"
  "v_lshlrev_b32 v214, 16, v117\n"
  "v_and_b32 v215, s65, v117\n"
  "v_pk_add_f32 v[144:145], v[144:145], v[180:181]\n"
  "v_pk_add_f32 v[146:147], v[146:147], v[182:183]\n"
  "v_pk_add_f32 v[148:149], v[148:149], v[184:185]\n"
  "v_pk_add_f32 v[150:151], v[150:151], v[186:187]\n"
  "v_pk_add_f32 v[152:153], v[152:153], v[188:189]\n"
  "v_pk_add_f32 v[154:155], v[154:155], v[190:191]\n"
  "v_pk_add_f32 v[156:157], v[156:157], v[192:193]\n"
  "v_pk_add_f32 v[158:159], v[158:159], v[194:195]\n"
  "v_pk_add_f32 v[160:161], v[160:161], v[196:197]\n"
  "v_pk_add_f32 v[162:163], v[162:163], v[198:199]\n"
  "v_pk_add_f32 v[164:165], v[164:165], v[200:201]\n"
  "v_pk_add_f32 v[166:167], v[166:167], v[202:203]\n"
  "v_pk_add_f32 v[168:169], v[168:169], v[204:205]\n"
  "v_pk_add_f32 v[170:171], v[170:171], v[206:207]\n"
  "v_pk_add_f32 v[172:173], v[172:173], v[208:209]\n"
  "v_pk_add_f32 v[174:175], v[174:175], v[210:211]\n"
  "v_pk_add_f32 v[176:177], v[176:177], v[212:213]\n"
  "v_pk_add_f32 v[178:179], v[178:179], v[214:215]\n"
  "s_waitcnt vmcnt(1)\n"
  "v_lshlrev_b32 v180, 16, v120\n"
  "v_and_b32 v181, s65, v120\n"
  "v_lshlrev_b32 v182, 16, v121\n"
  "v_and_b32 v183, s65, v121\n"
  "v_lshlrev_b32 v184, 16, v122\n"
  "v_and_b32 v185, s65, v122\n"
  "v_lshlrev_b32 v186, 16, v123\n"
  "v_and_b32 v187, s65, v123\n"
  "v_lshlrev_b32 v188, 16, v124\n"
  "v_and_b32 v189, s65, v124\n"
  "v_lshlrev_b32 v190, 16, v125\n"
  "v_and_b32 v191, s65, v125\n"
  "v_lshlrev_b32 v192, 16, v126\n"
  "v_and_b32 v193, s65, v126\n"
  "v_lshlrev_b32 v194, 16, v127\n"
  "v_and_b32 v195, s65, v127\n"
  "v_lshlrev_b32 v196, 16, v128\n"
  "v_and_b32 v197, s65, v128\n"
  "v_lshlrev_b32 v198, 16, v129\n"
  "v_and_b32 v199, s65, v129\n"
  "v_lshlrev_b32 v200, 16, v130\n"
  "v_and_b32 v201, s65, v130\n"
  "v_lshlrev_b32 v202, 16, v131\n"
  "v_and_b32 v203, s65, v131\n"
  "v_lshlrev_b32 v204, 16, v132\n"
  "v_and_b32 v205, s65, v132\n"
  "v_lshlrev_b32 v206, 16, v133\n"
  "v_and_b32 v207, s65, v133\n"
  "v_lshlrev_b32 v208, 16, v134\n"
  "v_and_b32 v209, s65, v134\n"
  "v_lshlrev_b32 v210, 16, v135\n"
  "v_and_b32 v211, s65, v135\n"
  "v_lshlrev_b32 v212, 16, v136\n"
  "v_and_b32 v213, s65, v136\n"
  "v_lshlrev_b32 v214, 16, v137\n"
  "v_and_b32 v215, s65, v137\n"
  "v_pk_add_f32 v[144:145], v[144:145], v[180:181]\n"
  "v_pk_add_f32 v[146:147], v[146:147], v[182:183]\n"
  "v_pk_add_f32 v[148:149], v[148:149], v[184:185]\n"
  "v_pk_add_f32 v[150:151], v[150:151], v[186:187]\n"
  "v_pk_add_f32 v[152:153], v[152:153], v[188:189]\n"
  "v_pk_add_f32 v[154:155], v[154:155], v[190:191]\n"
  "v_pk_add_f32 v[156:157], v[156:157], v[192:193]\n"
  "v_pk_add_f32 v[158:159], v[158:159], v[194:195]\n"
  "v_pk_add_f32 v[160:161], v[160:161], v[196:197]\n"
  "v_pk_add_f32 v[162:163], v[162:163], v[198:199]\n"
  "v_pk_add_f32 v[164:165], v[164:165], v[200:201]\n"
  "v_pk_add_f32 v[166:167], v[166:167], v[202:203]\n"
  "v_pk_add_f32 v[168:169], v[168:169], v[204:205]\n"
  "v_pk_add_f32 v[170:171], v[170:171], v[206:207]\n"
  "v_pk_add_f32 v[172:173], v[172:173], v[208:209]\n"
  "v_pk_add_f32 v[174:175], v[174:175], v[210:211]\n"
  "v_pk_add_f32 v[176:177], v[176:177], v[212:213]\n"
  "v_pk_add_f32 v[178:179], v[178:179], v[214:215]\n"
  "v_cvt_pk_bf16_f32 v0, v144, v145\n"
  "v_cvt_pk_bf16_f32 v1, v146, v147\n"
  "v_cvt_pk_bf16_f32 v2, v148, v149\n"
  "v_cvt_pk_bf16_f32 v3, v150, v151\n"
  "v_cvt_pk_bf16_f32 v4, v152, v153\n"
  "v_cvt_pk_bf16_f32 v5, v154, v155\n"
  "v_cvt_pk_bf16_f32 v6, v156, v157\n"
  "v_cvt_pk_bf16_f32 v7, v158, v159\n"
  "v_cvt_pk_bf16_f32 v8, v160, v161\n"
  "v_cvt_pk_bf16_f32 v9, v162, v163\n"
  "v_cvt_pk_bf16_f32 v10, v164, v165\n"
  "v_cvt_pk_bf16_f32 v11, v166, v167\n"
  "v_cvt_pk_bf16_f32 v12, v168, v169\n"
  "v_cvt_pk_bf16_f32 v13, v170, v171\n"
  "v_cvt_pk_bf16_f32 v14, v172, v173\n"
  "v_cvt_pk_bf16_f32 v15, v174, v175\n"
  "v_cvt_pk_bf16_f32 v16, v176, v177\n"
  "v_cvt_pk_bf16_f32 v17, v178, v179\n"
  "v_lshl_add_u32 v180, v246, 6, v247\n"
  "v_lshl_add_u32 v181, v246, 3, v247\n"
  "s_mul_i32 s86, s92, 36864\n"
  "s_lshl_b32 s87, s74, 8\n"
  "s_add_u32 s87, s86, s87\n"
  "ds_write_b128 v220, v[0:3] offset:7168\n"
  "ds_write_b128 v220, v[4:7] offset:40192\n"
  "ds_write_b128 v221, v[8:11] offset:7168\n"
  "ds_write_b128 v221, v[12:15] offset:40192\n"
  "ds_read_b128 v[20:23], v222 offset:0\n"
  "ds_read_b128 v[24:27], v222 offset:64\n"
  "ds_read_b128 v[28:31], v222 offset:128\n"
  "ds_read_b128 v[32:35], v222 offset:192\n"
  "s_waitcnt lgkmcnt(3)\n"
  "buffer_store_dwordx4 v[20:23], v223, s[48:51], s87 offen sc1\n"
  "s_add_u32 s87, s87, 9216\n"
  "s_waitcnt lgkmcnt(2)\n"
  "buffer_store_dwordx4 v[24:27], v223, s[48:51], s87 offen sc1\n"
  "s_add_u32 s87, s87, 9216\n"
  "s_waitcnt lgkmcnt(1)\n"
  "buffer_store_dwordx4 v[28:31], v223, s[48:51], s87 offen sc1\n"
  "s_add_u32 s87, s87, 9216\n"
  "s_waitcnt lgkmcnt(0)\n"
  "buffer_store_dwordx4 v[32:35], v223, s[48:51], s87 offen sc1\n"
  "s_lshl_b32 s87, s74, 5\n"
  "s_add_u32 s87, s86, s87\n"
  "buffer_store_dwordx2 v[16:17], v181, s[48:51], s87 offen offset:1024\n"
  "s_waitcnt vmcnt(5)\n"
  "s_branch L_done\n"
  "L_last:\n"
  "buffer_load_dword v244, v240, s[88:91], 0 offen sc1\n"
  "s_waitcnt vmcnt(0)\n"
  "L_done:\n"
  "v_cmp_ne_u32 vcc, v244, v255\n"
  "s_cbranch_vccz L_rf2\n"
  "v_mov_b32 v255, v244\n"
  "buffer_store_dword v255, v240, s[88:91], 0 offen offset:128 sc1\n"
  "L_rf2:\n"
  "s_branch L_end\n"
  "L_T2s:\n"
  "L_tw2:\n"
  "ds_read_b32 v232, v248 offset:24736\n"
  "ds_read_b32 v233, v248 offset:32992\n"
  "s_waitcnt lgkmcnt(0)\n"
  "v_min_u32 v232, v232, v233\n"
  "v_cmp_gt_u32 vcc, 2, v232\n"
  "s_cbranch_vccnz L_tw2\n"
  "L_tw3:\n"
  "ds_read_b32 v232, v248 offset:8224\n"
  "ds_read_b32 v233, v248 offset:16480\n"
  "s_waitcnt lgkmcnt(0)\n"
  "v_min_u32 v232, v232, v233\n"
  "v_cmp_gt_u32 vcc, 4, v232\n"
  "s_cbranch_vccnz L_tw3\n"
  "ds_read_b128 v[128:131], v240 offset:0\n"
  "ds_read_b128 v[132:135], v240 offset:1024\n"
  "ds_read_b128 v[136:139], v240 offset:2048\n"
  "ds_read_b128 v[140:143], v240 offset:3072\n"
  "ds_read_b128 v[144:147], v240 offset:4096\n"
  "ds_read_b128 v[148:151], v240 offset:5120\n"
  "ds_read_b128 v[152:155], v240 offset:6144\n"
  "s_waitcnt lgkmcnt(0)\n"
  "s_branch L_T2r\n"
  "L_rare_even:\n"
  "s_nop 7\n"
  "s_cselect_b32 s87, 1, 0\n"
  "v_bfe_u32 v240, v252, 2, 1\n"
  "v_lshlrev_b32 v240, 12, v240\n"
  "v_bfe_u32 v241, v252, 3, 1\n"
  "v_lshl_add_u32 v240, v241, 11, v240\n"
  "v_and_b32 v241, 3, v252\n"
  "v_lshl_add_u32 v240, v241, 8, v240\n"
  "v_lshrrev_b32 v241, 4, v252\n"
  "v_lshl_add_u32 v240, v241, 6, v240\n"
  "buffer_load_dwordx4 v[168:171], v240, s[56:59], s78 offen offset:0\n"
  "buffer_load_dwordx4 v[172:175], v240, s[56:59], s78 offen offset:16\n"
  "buffer_load_dwordx4 v[176:179], v240, s[56:59], s78 offen offset:32\n"
  "buffer_load_dwordx4 v[180:183], v240, s[56:59], s78 offen offset:48\n"
  "s_waitcnt vmcnt(0)\n"
  "v_mfma_f32_16x16x32_bf16 v[112:115], v[168:171], v[0:3], v[196:199]\n"
  "v_mfma_f32_16x16x32_bf16 v[116:119], v[168:171], v[16:19], v[200:203]\n"
  "v_mfma_f32_16x16x32_bf16 v[120:123], v[168:171], v[32:35], v[204:207]\n"
  "v_mfma_f32_16x16x32_bf16 v[124:127], v[168:171], v[48:51], v[208:211]\n"
  "v_mfma_f32_16x16x32_bf16 v[128:131], v[168:171], v[64:67], v[212:215]\n"
  "v_mfma_f32_16x16x32_bf16 v[132:135], v[168:171], v[80:83], v[216:219]\n"
  "v_mfma_f32_16x16x32_bf16 v[136:139], v[168:171], v[96:99], v[220:223]\n"
  "v_mfma_f32_16x16x32_bf16 v[112:115], v[172:175], v[4:7], v[112:115]\n"
  "v_mfma_f32_16x16x32_bf16 v[116:119], v[172:175], v[20:23], v[116:119]\n"
  "v_mfma_f32_16x16x32_bf16 v[120:123], v[172:175], v[36:39], v[120:123]\n"
  "v_mfma_f32_16x16x32_bf16 v[124:127], v[172:175], v[52:55], v[124:127]\n"
  "v_mfma_f32_16x16x32_bf16 v[128:131], v[172:175], v[68:71], v[128:131]\n"
  "v_mfma_f32_16x16x32_bf16 v[132:135], v[172:175], v[84:87], v[132:135]\n"
  "v_mfma_f32_16x16x32_bf16 v[136:139], v[172:175], v[100:103], v[136:139]\n"
  "v_mfma_f32_16x16x32_bf16 v[112:115], v[176:179], v[8:11], v[112:115]\n"
  "v_mfma_f32_16x16x32_bf16 v[116:119], v[176:179], v[24:27], v[116:119]\n"
  "v_mfma_f32_16x16x32_bf16 v[120:123], v[176:179], v[40:43], v[120:123]\n"
  "v_mfma_f32_16x16x32_bf16 v[124:127], v[176:179], v[56:59], v[124:127]\n"
  "v_mfma_f32_16x16x32_bf16 v[128:131], v[176:179], v[72:75], v[128:131]\n"
  "v_mfma_f32_16x16x32_bf16 v[132:135], v[176:179], v[88:91], v[132:135]\n"
  "v_mfma_f32_16x16x32_bf16 v[136:139], v[176:179], v[104:107], v[136:139]\n"
  "v_mfma_f32_16x16x32_bf16 v[112:115], v[180:183], v[12:15], v[112:115]\n"
  "v_mfma_f32_16x16x32_bf16 v[116:119], v[180:183], v[28:31], v[116:119]\n"
  "v_mfma_f32_16x16x32_bf16 v[120:123], v[180:183], v[44:47], v[120:123]\n"
  "v_mfma_f32_16x16x32_bf16 v[124:127], v[180:183], v[60:63], v[124:127]\n"
  "v_mfma_f32_16x16x32_bf16 v[128:131], v[180:183], v[76:79], v[128:131]\n"
  "v_mfma_f32_16x16x32_bf16 v[132:135], v[180:183], v[92:95], v[132:135]\n"
  "v_mfma_f32_16x16x32_bf16 v[136:139], v[180:183], v[108:111], v[136:139]\n"
  "s_nop 7\n"
  "buffer_load_dwordx4 v[168:171], v240, s[56:59], s78 offen offset:1024\n"
  "buffer_load_dwordx4 v[172:175], v240, s[56:59], s78 offen offset:1040\n"
  "buffer_load_dwordx4 v[176:179], v240, s[56:59], s78 offen offset:1056\n"
  "buffer_load_dwordx4 v[180:183], v240, s[56:59], s78 offen offset:1072\n"
  "s_waitcnt vmcnt(0)\n"
  "v_mfma_f32_16x16x32_bf16 v[140:143], v[168:171], v[0:3], v[196:199]\n"
  "v_mfma_f32_16x16x32_bf16 v[144:147], v[168:171], v[16:19], v[200:203]\n"
  "v_mfma_f32_16x16x32_bf16 v[148:151], v[168:171], v[32:35], v[204:207]\n"
  "v_mfma_f32_16x16x32_bf16 v[152:155], v[168:171], v[48:51], v[208:211]\n"
  "v_mfma_f32_16x16x32_bf16 v[156:159], v[168:171], v[64:67], v[212:215]\n"
  "v_mfma_f32_16x16x32_bf16 v[160:163], v[168:171], v[80:83], v[216:219]\n"
  "v_mfma_f32_16x16x32_bf16 v[164:167], v[168:171], v[96:99], v[220:223]\n"
  "v_mfma_f32_16x16x32_bf16 v[140:143], v[172:175], v[4:7], v[140:143]\n"
  "v_mfma_f32_16x16x32_bf16 v[144:147], v[172:175], v[20:23], v[144:147]\n"
  "v_mfma_f32_16x16x32_bf16 v[148:151], v[172:175], v[36:39], v[148:151]\n"
  "v_mfma_f32_16x16x32_bf16 v[152:155], v[172:175], v[52:55], v[152:155]\n"
  "v_mfma_f32_16x16x32_bf16 v[156:159], v[172:175], v[68:71], v[156:159]\n"
  "v_mfma_f32_16x16x32_bf16 v[160:163], v[172:175], v[84:87], v[160:163]\n"
  "v_mfma_f32_16x16x32_bf16 v[164:167], v[172:175], v[100:103], v[164:167]\n"
  "v_mfma_f32_16x16x32_bf16 v[140:143], v[176:179], v[8:11], v[140:143]\n"
  "v_mfma_f32_16x16x32_bf16 v[144:147], v[176:179], v[24:27], v[144:147]\n"
  "v_mfma_f32_16x16x32_bf16 v[148:151], v[176:179], v[40:43], v[148:151]\n"
  "v_mfma_f32_16x16x32_bf16 v[152:155], v[176:179], v[56:59], v[152:155]\n"
  "v_mfma_f32_16x16x32_bf16 v[156:159], v[176:179], v[72:75], v[156:159]\n"
  "v_mfma_f32_16x16x32_bf16 v[160:163], v[176:179], v[88:91], v[160:163]\n"
  "v_mfma_f32_16x16x32_bf16 v[164:167], v[176:179], v[104:107], v[164:167]\n"
  "v_mfma_f32_16x16x32_bf16 v[140:143], v[180:183], v[12:15], v[140:143]\n"
  "v_mfma_f32_16x16x32_bf16 v[144:147], v[180:183], v[28:31], v[144:147]\n"
  "v_mfma_f32_16x16x32_bf16 v[148:151], v[180:183], v[44:47], v[148:151]\n"
  "v_mfma_f32_16x16x32_bf16 v[152:155], v[180:183], v[60:63], v[152:155]\n"
  "v_mfma_f32_16x16x32_bf16 v[156:159], v[180:183], v[76:79], v[156:159]\n"
  "v_mfma_f32_16x16x32_bf16 v[160:163], v[180:183], v[92:95], v[160:163]\n"
  "v_mfma_f32_16x16x32_bf16 v[164:167], v[180:183], v[108:111], v[164:167]\n"
  "s_nop 7\n"
  "v_max3_f32 v168, v112, v113, v114\n"
  "v_max3_f32 v168, v168, v115, v140\n"
  "v_max3_f32 v168, v168, v141, v142\n"
  "v_max_f32 v168, v168, v143\n"
  "v_max3_f32 v169, v116, v117, v118\n"
  "v_max3_f32 v169, v169, v119, v144\n"
  "v_max3_f32 v169, v169, v145, v146\n"
  "v_max_f32 v169, v169, v147\n"
  "v_max3_f32 v170, v120, v121, v122\n"
  "v_max3_f32 v170, v170, v123, v148\n"
  "v_max3_f32 v170, v170, v149, v150\n"
  "v_max_f32 v170, v170, v151\n"
  "v_max3_f32 v171, v124, v125, v126\n"
  "v_max3_f32 v171, v171, v127, v152\n"
  "v_max3_f32 v171, v171, v153, v154\n"
  "v_max_f32 v171, v171, v155\n"
  "v_max3_f32 v172, v128, v129, v130\n"
  "v_max3_f32 v172, v172, v131, v156\n"
  "v_max3_f32 v172, v172, v157, v158\n"
  "v_max_f32 v172, v172, v159\n"
  "v_max3_f32 v173, v132, v133, v134\n"
  "v_max3_f32 v173, v173, v135, v160\n"
  "v_max3_f32 v173, v173, v161, v162\n"
  "v_max_f32 v173, v173, v163\n"
  "v_max3_f32 v174, v136, v137, v138\n"
  "v_max3_f32 v174, v174, v139, v164\n"
  "v_max3_f32 v174, v174, v165, v166\n"
  "v_max_f32 v174, v174, v167\n"
  "v_mov_b32 v176, v168\n"
  "v_mov_b32 v177, v169\n"
  "v_mov_b32 v178, v170\n"
  "v_mov_b32 v179, v171\n"
  "v_mov_b32 v180, v172\n"
  "v_mov_b32 v181, v173\n"
  "v_mov_b32 v182, v174\n"
  "v_permlane16_swap_b32 v168, v176\n"
  "v_permlane16_swap_b32 v169, v177\n"
  "v_permlane16_swap_b32 v170, v178\n"
  "v_permlane16_swap_b32 v171, v179\n"
  "v_permlane16_swap_b32 v172, v180\n"
  "v_permlane16_swap_b32 v173, v181\n"
  "v_permlane16_swap_b32 v174, v182\n"
  "v_max_f32 v168, v168, v176\n"
  "v_max_f32 v169, v169, v177\n"
  "v_max_f32 v170, v170, v178\n"
  "v_max_f32 v171, v171, v179\n"
  "v_max_f32 v172, v172, v180\n"
  "v_max_f32 v173, v173, v181\n"
  "v_max_f32 v174, v174, v182\n"
  "v_mov_b32 v176, v168\n"
  "v_mov_b32 v177, v169\n"
  "v_mov_b32 v178, v170\n"
  "v_mov_b32 v179, v171\n"
  "v_mov_b32 v180, v172\n"
  "v_mov_b32 v181, v173\n"
  "v_mov_b32 v182, v174\n"
  "v_permlane32_swap_b32 v168, v176\n"
  "v_permlane32_swap_b32 v169, v177\n"
  "v_permlane32_swap_b32 v170, v178\n"
  "v_permlane32_swap_b32 v171, v179\n"
  "v_permlane32_swap_b32 v172, v180\n"
  "v_permlane32_swap_b32 v173, v181\n"
  "v_permlane32_swap_b32 v174, v182\n"
  "v_max_f32 v168, v168, v176\n"
  "v_max_f32 v169, v169, v177\n"
  "v_max_f32 v170, v170, v178\n"
  "v_max_f32 v171, v171, v179\n"
  "v_max_f32 v172, v172, v180\n"
  "v_max_f32 v173, v173, v181\n"
  "v_max_f32 v174, v174, v182\n"
  "v_add_f32 v168, 0x41800000, v168\n"
  "v_add_f32 v169, 0x41800000, v169\n"
  "v_add_f32 v170, 0x41800000, v170\n"
  "v_add_f32 v171, 0x41800000, v171\n"
  "v_add_f32 v172, 0x41800000, v172\n"
  "v_add_f32 v173, 0x41800000, v173\n"
  "v_add_f32 v174, 0x41800000, v174\n"
  "v_max_f32 v168, 0, v168\n"
  "v_max_f32 v169, 0, v169\n"
  "v_max_f32 v170, 0, v170\n"
  "v_max_f32 v171, 0, v171\n"
  "v_max_f32 v172, 0, v172\n"
  "v_max_f32 v173, 0, v173\n"
  "v_max_f32 v174, 0, v174\n"
  "v_sub_f32 v112, v112, v168\n"
  "v_sub_f32 v113, v113, v168\n"
  "v_sub_f32 v114, v114, v168\n"
  "v_sub_f32 v115, v115, v168\n"
  "v_sub_f32 v140, v140, v168\n"
  "v_sub_f32 v141, v141, v168\n"
  "v_sub_f32 v142, v142, v168\n"
  "v_sub_f32 v143, v143, v168\n"
  "v_sub_f32 v196, v196, v168\n"
  "v_sub_f32 v197, v197, v168\n"
  "v_sub_f32 v198, v198, v168\n"
  "v_sub_f32 v199, v199, v168\n"
  "v_sub_f32 v116, v116, v169\n"
  "v_sub_f32 v117, v117, v169\n"
  "v_sub_f32 v118, v118, v169\n"
  "v_sub_f32 v119, v119, v169\n"
  "v_sub_f32 v144, v144, v169\n"
  "v_sub_f32 v145, v145, v169\n"
  "v_sub_f32 v146, v146, v169\n"
  "v_sub_f32 v147, v147, v169\n"
  "v_sub_f32 v200, v200, v169\n"
  "v_sub_f32 v201, v201, v169\n"
  "v_sub_f32 v202, v202, v169\n"
  "v_sub_f32 v203, v203, v169\n"
  "v_sub_f32 v120, v120, v170\n"
  "v_sub_f32 v121, v121, v170\n"
  "v_sub_f32 v122, v122, v170\n"
  "v_sub_f32 v123, v123, v170\n"
  "v_sub_f32 v148, v148, v170\n"
  "v_sub_f32 v149, v149, v170\n"
  "v_sub_f32 v150, v150, v170\n"
  "v_sub_f32 v151, v151, v170\n"
  "v_sub_f32 v204, v204, v170\n"
  "v_sub_f32 v205, v205, v170\n"
  "v_sub_f32 v206, v206, v170\n"
  "v_sub_f32 v207, v207, v170\n"
  "v_sub_f32 v124, v124, v171\n"
  "v_sub_f32 v125, v125, v171\n"
  "v_sub_f32 v126, v126, v171\n"
  "v_sub_f32 v127, v127, v171\n"
  "v_sub_f32 v152, v152, v171\n"
  "v_sub_f32 v153, v153, v171\n"
  "v_sub_f32 v154, v154, v171\n"
  "v_sub_f32 v155, v155, v171\n"
  "v_sub_f32 v208, v208, v171\n"
  "v_sub_f32 v209, v209, v171\n"
  "v_sub_f32 v210, v210, v171\n"
  "v_sub_f32 v211, v211, v171\n"
  "v_sub_f32 v128, v128, v172\n"
  "v_sub_f32 v129, v129, v172\n"
  "v_sub_f32 v130, v130, v172\n"
  "v_sub_f32 v131, v131, v172\n"
  "v_sub_f32 v156, v156, v172\n"
  "v_sub_f32 v157, v157, v172\n"
  "v_sub_f32 v158, v158, v172\n"
  "v_sub_f32 v159, v159, v172\n"
  "v_sub_f32 v212, v212, v172\n"
  "v_sub_f32 v213, v213, v172\n"
  "v_sub_f32 v214, v214, v172\n"
  "v_sub_f32 v215, v215, v172\n"
  "v_sub_f32 v132, v132, v173\n"
  "v_sub_f32 v133, v133, v173\n"
  "v_sub_f32 v134, v134, v173\n"
  "v_sub_f32 v135, v135, v173\n"
  "v_sub_f32 v160, v160, v173\n"
  "v_sub_f32 v161, v161, v173\n"
  "v_sub_f32 v162, v162, v173\n"
  "v_sub_f32 v163, v163, v173\n"
  "v_sub_f32 v216, v216, v173\n"
  "v_sub_f32 v217, v217, v173\n"
  "v_sub_f32 v218, v218, v173\n"
  "v_sub_f32 v219, v219, v173\n"
  "v_sub_f32 v136, v136, v174\n"
  "v_sub_f32 v137, v137, v174\n"
  "v_sub_f32 v138, v138, v174\n"
  "v_sub_f32 v139, v139, v174\n"
  "v_sub_f32 v164, v164, v174\n"
  "v_sub_f32 v165, v165, v174\n"
  "v_sub_f32 v166, v166, v174\n"
  "v_sub_f32 v167, v167, v174\n"
  "v_sub_f32 v220, v220, v174\n"
  "v_sub_f32 v221, v221, v174\n"
  "v_sub_f32 v222, v222, v174\n"
  "v_sub_f32 v223, v223, v174\n"
  "v_exp_f32 v112, v112\n"
  "v_exp_f32 v113, v113\n"
  "v_exp_f32 v114, v114\n"
  "v_exp_f32 v115, v115\n"
  "v_exp_f32 v140, v140\n"
  "v_exp_f32 v141, v141\n"
  "v_exp_f32 v142, v142\n"
  "v_exp_f32 v143, v143\n"
  "v_cvt_pk_bf16_f32 v112, v112, v113\n"
  "v_cvt_pk_bf16_f32 v113, v114, v115\n"
  "v_cvt_pk_bf16_f32 v114, v140, v141\n"
  "v_cvt_pk_bf16_f32 v115, v142, v143\n"
  "v_exp_f32 v116, v116\n"
  "v_exp_f32 v117, v117\n"
  "v_exp_f32 v118, v118\n"
  "v_exp_f32 v119, v119\n"
  "v_exp_f32 v144, v144\n"
  "v_exp_f32 v145, v145\n"
  "v_exp_f32 v146, v146\n"
  "v_exp_f32 v147, v147\n"
  "v_cvt_pk_bf16_f32 v116, v116, v117\n"
  "v_cvt_pk_bf16_f32 v117, v118, v119\n"
  "v_cvt_pk_bf16_f32 v118, v144, v145\n"
  "v_cvt_pk_bf16_f32 v119, v146, v147\n"
  "v_exp_f32 v120, v120\n"
  "v_exp_f32 v121, v121\n"
  "v_exp_f32 v122, v122\n"
  "v_exp_f32 v123, v123\n"
  "v_exp_f32 v148, v148\n"
  "v_exp_f32 v149, v149\n"
  "v_exp_f32 v150, v150\n"
  "v_exp_f32 v151, v151\n"
  "v_cvt_pk_bf16_f32 v120, v120, v121\n"
  "v_cvt_pk_bf16_f32 v121, v122, v123\n"
  "v_cvt_pk_bf16_f32 v122, v148, v149\n"
  "v_cvt_pk_bf16_f32 v123, v150, v151\n"
  "v_exp_f32 v124, v124\n"
  "v_exp_f32 v125, v125\n"
  "v_exp_f32 v126, v126\n"
  "v_exp_f32 v127, v127\n"
  "v_exp_f32 v152, v152\n"
  "v_exp_f32 v153, v153\n"
  "v_exp_f32 v154, v154\n"
  "v_exp_f32 v155, v155\n"
  "v_cvt_pk_bf16_f32 v124, v124, v125\n"
  "v_cvt_pk_bf16_f32 v125, v126, v127\n"
  "v_cvt_pk_bf16_f32 v126, v152, v153\n"
  "v_cvt_pk_bf16_f32 v127, v154, v155\n"
  "v_exp_f32 v128, v128\n"
  "v_exp_f32 v129, v129\n"
  "v_exp_f32 v130, v130\n"
  "v_exp_f32 v131, v131\n"
  "v_exp_f32 v156, v156\n"
  "v_exp_f32 v157, v157\n"
  "v_exp_f32 v158, v158\n"
  "v_exp_f32 v159, v159\n"
  "v_cvt_pk_bf16_f32 v128, v128, v129\n"
  "v_cvt_pk_bf16_f32 v129, v130, v131\n"
  "v_cvt_pk_bf16_f32 v130, v156, v157\n"
  "v_cvt_pk_bf16_f32 v131, v158, v159\n"
  "v_exp_f32 v132, v132\n"
  "v_exp_f32 v133, v133\n"
  "v_exp_f32 v134, v134\n"
  "v_exp_f32 v135, v135\n"
  "v_exp_f32 v160, v160\n"
  "v_exp_f32 v161, v161\n"
  "v_exp_f32 v162, v162\n"
  "v_exp_f32 v163, v163\n"
  "v_cvt_pk_bf16_f32 v132, v132, v133\n"
  "v_cvt_pk_bf16_f32 v133, v134, v135\n"
  "v_cvt_pk_bf16_f32 v134, v160, v161\n"
  "v_cvt_pk_bf16_f32 v135, v162, v163\n"
  "v_exp_f32 v136, v136\n"
  "v_exp_f32 v137, v137\n"
  "v_exp_f32 v138, v138\n"
  "v_exp_f32 v139, v139\n"
  "v_exp_f32 v164, v164\n"
  "v_exp_f32 v165, v165\n"
  "v_exp_f32 v166, v166\n"
  "v_exp_f32 v167, v167\n"
  "v_cvt_pk_bf16_f32 v136, v136, v137\n"
  "v_cvt_pk_bf16_f32 v137, v138, v139\n"
  "v_cvt_pk_bf16_f32 v138, v164, v165\n"
  "v_cvt_pk_bf16_f32 v139, v166, v167\n"
  "v_exp_f32 v140, -v168\n"
  "v_exp_f32 v141, -v169\n"
  "v_exp_f32 v142, -v170\n"
  "v_exp_f32 v143, -v171\n"
  "v_exp_f32 v144, -v172\n"
  "v_exp_f32 v145, -v173\n"
  "v_exp_f32 v146, -v174\n"
  "s_call_b64 s[84:85], L_rescale\n"
  "s_cmp_eq_u32 s87, 1\n"
  "s_nop 1\n"
  "s_branch L_ret_even\n"
  "L_rare_odd:\n"
  "s_nop 7\n"
  "s_cselect_b32 s87, 1, 0\n"
  "v_bfe_u32 v240, v252, 2, 1\n"
  "v_lshlrev_b32 v240, 12, v240\n"
  "v_bfe_u32 v241, v252, 3, 1\n"
  "v_lshl_add_u32 v240, v241, 11, v240\n"
  "v_and_b32 v241, 3, v252\n"
  "v_lshl_add_u32 v240, v241, 8, v240\n"
  "v_lshrrev_b32 v241, 4, v252\n"
  "v_lshl_add_u32 v240, v241, 6, v240\n"
  "buffer_load_dwordx4 v[112:115], v240, s[56:59], s78 offen offset:0\n"
  "buffer_load_dwordx4 v[116:119], v240, s[56:59], s78 offen offset:16\n"
  "buffer_load_dwordx4 v[120:123], v240, s[56:59], s78 offen offset:32\n"
  "buffer_load_dwordx4 v[124:127], v240, s[56:59], s78 offen offset:48\n"
  "s_waitcnt vmcnt(0)\n"
  "v_mfma_f32_16x16x32_bf16 v[168:171], v[112:115], v[0:3], v[196:199]\n"
  "v_mfma_f32_16x16x32_bf16 v[172:175], v[112:115], v[16:19], v[200:203]\n"
  "v_mfma_f32_16x16x32_bf16 v[176:179], v[112:115], v[32:35], v[204:207]\n"
  "v_mfma_f32_16x16x32_bf16 v[180:183], v[112:115], v[48:51], v[208:211]\n"
  "v_mfma_f32_16x16x32_bf16 v[184:187], v[112:115], v[64:67], v[212:215]\n"
  "v_mfma_f32_16x16x32_bf16 v[188:191], v[112:115], v[80:83], v[216:219]\n"
  "v_mfma_f32_16x16x32_bf16 v[192:195], v[112:115], v[96:99], v[220:223]\n"
  "v_mfma_f32_16x16x32_bf16 v[168:171], v[116:119], v[4:7], v[168:171]\n"
  "v_mfma_f32_16x16x32_bf16 v[172:175], v[116:119], v[20:23], v[172:175]\n"
  "v_mfma_f32_16x16x32_bf16 v[176:179], v[116:119], v[36:39], v[176:179]\n"
  "v_mfma_f32_16x16x32_bf16 v[180:183], v[116:119], v[52:55], v[180:183]\n"
  "v_mfma_f32_16x16x32_bf16 v[184:187], v[116:119], v[68:71], v[184:187]\n"
  "v_mfma_f32_16x16x32_bf16 v[188:191], v[116:119], v[84:87], v[188:191]\n"
  "v_mfma_f32_16x16x32_bf16 v[192:195], v[116:119], v[100:103], v[192:195]\n"
  "v_mfma_f32_16x16x32_bf16 v[168:171], v[120:123], v[8:11], v[168:171]\n"
  "v_mfma_f32_16x16x32_bf16 v[172:175], v[120:123], v[24:27], v[172:175]\n"
  "v_mfma_f32_16x16x32_bf16 v[176:179], v[120:123], v[40:43], v[176:179]\n"
  "v_mfma_f32_16x16x32_bf16 v[180:183], v[120:123], v[56:59], v[180:183]\n"
  "v_mfma_f32_16x16x32_bf16 v[184:187], v[120:123], v[72:75], v[184:187]\n"
  "v_mfma_f32_16x16x32_bf16 v[188:191], v[120:123], v[88:91], v[188:191]\n"
  "v_mfma_f32_16x16x32_bf16 v[192:195], v[120:123], v[104:107], v[192:195]\n"
  "v_mfma_f32_16x16x32_bf16 v[168:171], v[124:127], v[12:15], v[168:171]\n"
  "v_mfma_f32_16x16x32_bf16 v[172:175], v[124:127], v[28:31], v[172:175]\n"
  "v_mfma_f32_16x16x32_bf16 v[176:179], v[124:127], v[44:47], v[176:179]\n"
  "v_mfma_f32_16x16x32_bf16 v[180:183], v[124:127], v[60:63], v[180:183]\n"
  "v_mfma_f32_16x16x32_bf16 v[184:187], v[124:127], v[76:79], v[184:187]\n"
  "v_mfma_f32_16x16x32_bf16 v[188:191], v[124:127], v[92:95], v[188:191]\n"
  "v_mfma_f32_16x16x32_bf16 v[192:195], v[124:127], v[108:111], v[192:195]\n"
  "s_nop 7\n"
  "buffer_load_dwordx4 v[112:115], v240, s[56:59], s78 offen offset:1024\n"
  "buffer_load_dwordx4 v[116:119], v240, s[56:59], s78 offen offset:1040\n"
  "buffer_load_dwordx4 v[120:123], v240, s[56:59], s78 offen offset:1056\n"
  "buffer_load_dwordx4 v[124:127], v240, s[56:59], s78 offen offset:1072\n"
  "s_waitcnt vmcnt(0)\n"
  "v_mfma_f32_16x16x32_bf16 v[140:143], v[112:115], v[0:3], v[196:199]\n"
  "v_mfma_f32_16x16x32_bf16 v[144:147], v[112:115], v[16:19], v[200:203]\n"
  "v_mfma_f32_16x16x32_bf16 v[148:151], v[112:115], v[32:35], v[204:207]\n"
  "v_mfma_f32_16x16x32_bf16 v[152:155], v[112:115], v[48:51], v[208:211]\n"
  "v_mfma_f32_16x16x32_bf16 v[156:159], v[112:115], v[64:67], v[212:215]\n"
  "v_mfma_f32_16x16x32_bf16 v[160:163], v[112:115], v[80:83], v[216:219]\n"
  "v_mfma_f32_16x16x32_bf16 v[164:167], v[112:115], v[96:99], v[220:223]\n"
  "v_mfma_f32_16x16x32_bf16 v[140:143], v[116:119], v[4:7], v[140:143]\n"
  "v_mfma_f32_16x16x32_bf16 v[144:147], v[116:119], v[20:23], v[144:147]\n"
  "v_mfma_f32_16x16x32_bf16 v[148:151], v[116:119], v[36:39], v[148:151]\n"
  "v_mfma_f32_16x16x32_bf16 v[152:155], v[116:119], v[52:55], v[152:155]\n"
  "v_mfma_f32_16x16x32_bf16 v[156:159], v[116:119], v[68:71], v[156:159]\n"
  "v_mfma_f32_16x16x32_bf16 v[160:163], v[116:119], v[84:87], v[160:163]\n"
  "v_mfma_f32_16x16x32_bf16 v[164:167], v[116:119], v[100:103], v[164:167]\n"
  "v_mfma_f32_16x16x32_bf16 v[140:143], v[120:123], v[8:11], v[140:143]\n"
  "v_mfma_f32_16x16x32_bf16 v[144:147], v[120:123], v[24:27], v[144:147]\n"
  "v_mfma_f32_16x16x32_bf16 v[148:151], v[120:123], v[40:43], v[148:151]\n"
  "v_mfma_f32_16x16x32_bf16 v[152:155], v[120:123], v[56:59], v[152:155]\n"
  "v_mfma_f32_16x16x32_bf16 v[156:159], v[120:123], v[72:75], v[156:159]\n"
  "v_mfma_f32_16x16x32_bf16 v[160:163], v[120:123], v[88:91], v[160:163]\n"
  "v_mfma_f32_16x16x32_bf16 v[164:167], v[120:123], v[104:107], v[164:167]\n"
  "v_mfma_f32_16x16x32_bf16 v[140:143], v[124:127], v[12:15], v[140:143]\n"
  "v_mfma_f32_16x16x32_bf16 v[144:147], v[124:127], v[28:31], v[144:147]\n"
  "v_mfma_f32_16x16x32_bf16 v[148:151], v[124:127], v[44:47], v[148:151]\n"
  "v_mfma_f32_16x16x32_bf16 v[152:155], v[124:127], v[60:63], v[152:155]\n"
  "v_mfma_f32_16x16x32_bf16 v[156:159], v[124:127], v[76:79], v[156:159]\n"
  "v_mfma_f32_16x16x32_bf16 v[160:163], v[124:127], v[92:95], v[160:163]\n"
  "v_mfma_f32_16x16x32_bf16 v[164:167], v[124:127], v[108:111], v[164:167]\n"
  "s_nop 7\n"
  "v_max3_f32 v112, v168, v169, v170\n"
  "v_max3_f32 v112, v112, v171, v140\n"
  "v_max3_f32 v112, v112, v141, v142\n"
  "v_max_f32 v112, v112, v143\n"
  "v_max3_f32 v113, v172, v173, v174\n"
  "v_max3_f32 v113, v113, v175, v144\n"
  "v_max3_f32 v113, v113, v145, v146\n"
  "v_max_f32 v113, v113, v147\n"
  "v_max3_f32 v114, v176, v177, v178\n"
  "v_max3_f32 v114, v114, v179, v148\n"
  "v_max3_f32 v114, v114, v149, v150\n"
  "v_max_f32 v114, v114, v151\n"
  "v_max3_f32 v115, v180, v181, v182\n"
  "v_max3_f32 v115, v115, v183, v152\n"
  "v_max3_f32 v115, v115, v153, v154\n"
  "v_max_f32 v115, v115, v155\n"
  "v_max3_f32 v116, v184, v185, v186\n"
  "v_max3_f32 v116, v116, v187, v156\n"
  "v_max3_f32 v116, v116, v157, v158\n"
  "v_max_f32 v116, v116, v159\n"
  "v_max3_f32 v117, v188, v189, v190\n"
  "v_max3_f32 v117, v117, v191, v160\n"
  "v_max3_f32 v117, v117, v161, v162\n"
  "v_max_f32 v117, v117, v163\n"
  "v_max3_f32 v118, v192, v193, v194\n"
  "v_max3_f32 v118, v118, v195, v164\n"
  "v_max3_f32 v118, v118, v165, v166\n"
  "v_max_f32 v118, v118, v167\n"
  "v_mov_b32 v120, v112\n"
  "v_mov_b32 v121, v113\n"
  "v_mov_b32 v122, v114\n"
  "v_mov_b32 v123, v115\n"
  "v_mov_b32 v124, v116\n"
  "v_mov_b32 v125, v117\n"
  "v_mov_b32 v126, v118\n"
  "v_permlane16_swap_b32 v112, v120\n"
  "v_permlane16_swap_b32 v113, v121\n"
  "v_permlane16_swap_b32 v114, v122\n"
  "v_permlane16_swap_b32 v115, v123\n"
  "v_permlane16_swap_b32 v116, v124\n"
  "v_permlane16_swap_b32 v117, v125\n"
  "v_permlane16_swap_b32 v118, v126\n"
  "v_max_f32 v112, v112, v120\n"
  "v_max_f32 v113, v113, v121\n"
  "v_max_f32 v114, v114, v122\n"
  "v_max_f32 v115, v115, v123\n"
  "v_max_f32 v116, v116, v124\n"
  "v_max_f32 v117, v117, v125\n"
  "v_max_f32 v118, v118, v126\n"
  "v_mov_b32 v120, v112\n"
  "v_mov_b32 v121, v113\n"
  "v_mov_b32 v122, v114\n"
  "v_mov_b32 v123, v115\n"
  "v_mov_b32 v124, v116\n"
  "v_mov_b32 v125, v117\n"
  "v_mov_b32 v126, v118\n"
  "v_permlane32_swap_b32 v112, v120\n"
  "v_permlane32_swap_b32 v113, v121\n"
  "v_permlane32_swap_b32 v114, v122\n"
  "v_permlane32_swap_b32 v115, v123\n"
  "v_permlane32_swap_b32 v116, v124\n"
  "v_permlane32_swap_b32 v117, v125\n"
  "v_permlane32_swap_b32 v118, v126\n"
  "v_max_f32 v112, v112, v120\n"
  "v_max_f32 v113, v113, v121\n"
  "v_max_f32 v114, v114, v122\n"
  "v_max_f32 v115, v115, v123\n"
  "v_max_f32 v116, v116, v124\n"
  "v_max_f32 v117, v117, v125\n"
  "v_max_f32 v118, v118, v126\n"
  "v_add_f32 v112, 0x41800000, v112\n"
  "v_add_f32 v113, 0x41800000, v113\n"
  "v_add_f32 v114, 0x41800000, v114\n"
  "v_add_f32 v115, 0x41800000, v115\n"
  "v_add_f32 v116, 0x41800000, v116\n"
  "v_add_f32 v117, 0x41800000, v117\n"
  "v_add_f32 v118, 0x41800000, v118\n"
  "v_max_f32 v112, 0, v112\n"
  "v_max_f32 v113, 0, v113\n"
  "v_max_f32 v114, 0, v114\n"
  "v_max_f32 v115, 0, v115\n"
  "v_max_f32 v116, 0, v116\n"
  "v_max_f32 v117, 0, v117\n"
  "v_max_f32 v118, 0, v118\n"
  "v_sub_f32 v168, v168, v112\n"
  "v_sub_f32 v169, v169, v112\n"
  "v_sub_f32 v170, v170, v112\n"
  "v_sub_f32 v171, v171, v112\n"
  "v_sub_f32 v140, v140, v112\n"
  "v_sub_f32 v141, v141, v112\n"
  "v_sub_f32 v142, v142, v112\n"
  "v_sub_f32 v143, v143, v112\n"
  "v_sub_f32 v196, v196, v112\n"
  "v_sub_f32 v197, v197, v112\n"
  "v_sub_f32 v198, v198, v112\n"
  "v_sub_f32 v199, v199, v112\n"
  "v_sub_f32 v172, v172, v113\n"
  "v_sub_f32 v173, v173, v113\n"
  "v_sub_f32 v174, v174, v113\n"
  "v_sub_f32 v175, v175, v113\n"
  "v_sub_f32 v144, v144, v113\n"
  "v_sub_f32 v145, v145, v113\n"
  "v_sub_f32 v146, v146, v113\n"
  "v_sub_f32 v147, v147, v113\n"
  "v_sub_f32 v200, v200, v113\n"
  "v_sub_f32 v201, v201, v113\n"
  "v_sub_f32 v202, v202, v113\n"
  "v_sub_f32 v203, v203, v113\n"
  "v_sub_f32 v176, v176, v114\n"
  "v_sub_f32 v177, v177, v114\n"
  "v_sub_f32 v178, v178, v114\n"
  "v_sub_f32 v179, v179, v114\n"
  "v_sub_f32 v148, v148, v114\n"
  "v_sub_f32 v149, v149, v114\n"
  "v_sub_f32 v150, v150, v114\n"
  "v_sub_f32 v151, v151, v114\n"
  "v_sub_f32 v204, v204, v114\n"
  "v_sub_f32 v205, v205, v114\n"
  "v_sub_f32 v206, v206, v114\n"
  "v_sub_f32 v207, v207, v114\n"
  "v_sub_f32 v180, v180, v115\n"
  "v_sub_f32 v181, v181, v115\n"
  "v_sub_f32 v182, v182, v115\n"
  "v_sub_f32 v183, v183, v115\n"
  "v_sub_f32 v152, v152, v115\n"
  "v_sub_f32 v153, v153, v115\n"
  "v_sub_f32 v154, v154, v115\n"
  "v_sub_f32 v155, v155, v115\n"
  "v_sub_f32 v208, v208, v115\n"
  "v_sub_f32 v209, v209, v115\n"
  "v_sub_f32 v210, v210, v115\n"
  "v_sub_f32 v211, v211, v115\n"
  "v_sub_f32 v184, v184, v116\n"
  "v_sub_f32 v185, v185, v116\n"
  "v_sub_f32 v186, v186, v116\n"
  "v_sub_f32 v187, v187, v116\n"
  "v_sub_f32 v156, v156, v116\n"
  "v_sub_f32 v157, v157, v116\n"
  "v_sub_f32 v158, v158, v116\n"
  "v_sub_f32 v159, v159, v116\n"
  "v_sub_f32 v212, v212, v116\n"
  "v_sub_f32 v213, v213, v116\n"
  "v_sub_f32 v214, v214, v116\n"
  "v_sub_f32 v215, v215, v116\n"
  "v_sub_f32 v188, v188, v117\n"
  "v_sub_f32 v189, v189, v117\n"
  "v_sub_f32 v190, v190, v117\n"
  "v_sub_f32 v191, v191, v117\n"
  "v_sub_f32 v160, v160, v117\n"
  "v_sub_f32 v161, v161, v117\n"
  "v_sub_f32 v162, v162, v117\n"
  "v_sub_f32 v163, v163, v117\n"
  "v_sub_f32 v216, v216, v117\n"
  "v_sub_f32 v217, v217, v117\n"
  "v_sub_f32 v218, v218, v117\n"
  "v_sub_f32 v219, v219, v117\n"
  "v_sub_f32 v192, v192, v118\n"
  "v_sub_f32 v193, v193, v118\n"
  "v_sub_f32 v194, v194, v118\n"
  "v_sub_f32 v195, v195, v118\n"
  "v_sub_f32 v164, v164, v118\n"
  "v_sub_f32 v165, v165, v118\n"
  "v_sub_f32 v166, v166, v118\n"
  "v_sub_f32 v167, v167, v118\n"
  "v_sub_f32 v220, v220, v118\n"
  "v_sub_f32 v221, v221, v118\n"
  "v_sub_f32 v222, v222, v118\n"
  "v_sub_f32 v223, v223, v118\n"
  "v_exp_f32 v168, v168\n"
  "v_exp_f32 v169, v169\n"
  "v_exp_f32 v170, v170\n"
  "v_exp_f32 v171, v171\n"
  "v_exp_f32 v140, v140\n"
  "v_exp_f32 v141, v141\n"
  "v_exp_f32 v142, v142\n"
  "v_exp_f32 v143, v143\n"
  "v_cvt_pk_bf16_f32 v168, v168, v169\n"
  "v_cvt_pk_bf16_f32 v169, v170, v171\n"
  "v_cvt_pk_bf16_f32 v170, v140, v141\n"
  "v_cvt_pk_bf16_f32 v171, v142, v143\n"
  "v_exp_f32 v172, v172\n"
  "v_exp_f32 v173, v173\n"
  "v_exp_f32 v174, v174\n"
  "v_exp_f32 v175, v175\n"
  "v_exp_f32 v144, v144\n"
  "v_exp_f32 v145, v145\n"
  "v_exp_f32 v146, v146\n"
  "v_exp_f32 v147, v147\n"
  "v_cvt_pk_bf16_f32 v172, v172, v173\n"
  "v_cvt_pk_bf16_f32 v173, v174, v175\n"
  "v_cvt_pk_bf16_f32 v174, v144, v145\n"
  "v_cvt_pk_bf16_f32 v175, v146, v147\n"
  "v_exp_f32 v176, v176\n"
  "v_exp_f32 v177, v177\n"
  "v_exp_f32 v178, v178\n"
  "v_exp_f32 v179, v179\n"
  "v_exp_f32 v148, v148\n"
  "v_exp_f32 v149, v149\n"
  "v_exp_f32 v150, v150\n"
  "v_exp_f32 v151, v151\n"
  "v_cvt_pk_bf16_f32 v176, v176, v177\n"
  "v_cvt_pk_bf16_f32 v177, v178, v179\n"
  "v_cvt_pk_bf16_f32 v178, v148, v149\n"
  "v_cvt_pk_bf16_f32 v179, v150, v151\n"
  "v_exp_f32 v180, v180\n"
  "v_exp_f32 v181, v181\n"
  "v_exp_f32 v182, v182\n"
  "v_exp_f32 v183, v183\n"
  "v_exp_f32 v152, v152\n"
  "v_exp_f32 v153, v153\n"
  "v_exp_f32 v154, v154\n"
  "v_exp_f32 v155, v155\n"
  "v_cvt_pk_bf16_f32 v180, v180, v181\n"
  "v_cvt_pk_bf16_f32 v181, v182, v183\n"
  "v_cvt_pk_bf16_f32 v182, v152, v153\n"
  "v_cvt_pk_bf16_f32 v183, v154, v155\n"
  "v_exp_f32 v184, v184\n"
  "v_exp_f32 v185, v185\n"
  "v_exp_f32 v186, v186\n"
  "v_exp_f32 v187, v187\n"
  "v_exp_f32 v156, v156\n"
  "v_exp_f32 v157, v157\n"
  "v_exp_f32 v158, v158\n"
  "v_exp_f32 v159, v159\n"
  "v_cvt_pk_bf16_f32 v184, v184, v185\n"
  "v_cvt_pk_bf16_f32 v185, v186, v187\n"
  "v_cvt_pk_bf16_f32 v186, v156, v157\n"
  "v_cvt_pk_bf16_f32 v187, v158, v159\n"
  "v_exp_f32 v188, v188\n"
  "v_exp_f32 v189, v189\n"
  "v_exp_f32 v190, v190\n"
  "v_exp_f32 v191, v191\n"
  "v_exp_f32 v160, v160\n"
  "v_exp_f32 v161, v161\n"
  "v_exp_f32 v162, v162\n"
  "v_exp_f32 v163, v163\n"
  "v_cvt_pk_bf16_f32 v188, v188, v189\n"
  "v_cvt_pk_bf16_f32 v189, v190, v191\n"
  "v_cvt_pk_bf16_f32 v190, v160, v161\n"
  "v_cvt_pk_bf16_f32 v191, v162, v163\n"
  "v_exp_f32 v192, v192\n"
  "v_exp_f32 v193, v193\n"
  "v_exp_f32 v194, v194\n"
  "v_exp_f32 v195, v195\n"
  "v_exp_f32 v164, v164\n"
  "v_exp_f32 v165, v165\n"
  "v_exp_f32 v166, v166\n"
  "v_exp_f32 v167, v167\n"
  "v_cvt_pk_bf16_f32 v192, v192, v193\n"
  "v_cvt_pk_bf16_f32 v193, v194, v195\n"
  "v_cvt_pk_bf16_f32 v194, v164, v165\n"
  "v_cvt_pk_bf16_f32 v195, v166, v167\n"
  "v_exp_f32 v140, -v112\n"
  "v_exp_f32 v141, -v113\n"
  "v_exp_f32 v142, -v114\n"
  "v_exp_f32 v143, -v115\n"
  "v_exp_f32 v144, -v116\n"
  "v_exp_f32 v145, -v117\n"
  "v_exp_f32 v146, -v118\n"
  "s_call_b64 s[84:85], L_rescale\n"
  "s_cmp_eq_u32 s87, 1\n"
  "s_nop 1\n"
  "s_branch L_ret_odd\n"
  "L_rescale:\n"
  "s_nop 1\n"
  "v_accvgpr_read_b32 v148, a0\n"
  "v_accvgpr_read_b32 v149, a1\n"
  "v_accvgpr_read_b32 v150, a2\n"
  "v_accvgpr_read_b32 v151, a3\n"
  "v_accvgpr_read_b32 v152, a4\n"
  "v_accvgpr_read_b32 v153, a5\n"
  "v_accvgpr_read_b32 v154, a6\n"
  "v_accvgpr_read_b32 v155, a7\n"
  "v_accvgpr_read_b32 v156, a8\n"
  "v_accvgpr_read_b32 v157, a9\n"
  "v_accvgpr_read_b32 v158, a10\n"
  "v_accvgpr_read_b32 v159, a11\n"
  "v_mul_f32 v148, v140, v148\n"
  "v_mul_f32 v149, v140, v149\n"
  "v_mul_f32 v150, v140, v150\n"
  "v_mul_f32 v151, v140, v151\n"
  "v_mul_f32 v152, v140, v152\n"
  "v_mul_f32 v153, v140, v153\n"
  "v_mul_f32 v154, v140, v154\n"
  "v_mul_f32 v155, v140, v155\n"
  "v_mul_f32 v156, v140, v156\n"
  "v_mul_f32 v157, v140, v157\n"
  "v_mul_f32 v158, v140, v158\n"
  "v_mul_f32 v159, v140, v159\n"
  "v_accvgpr_write_b32 a0, v148\n"
  "v_accvgpr_write_b32 a1, v149\n"
  "v_accvgpr_write_b32 a2, v150\n"
  "v_accvgpr_write_b32 a3, v151\n"
  "v_accvgpr_write_b32 a4, v152\n"
  "v_accvgpr_write_b32 a5, v153\n"
  "v_accvgpr_write_b32 a6, v154\n"
  "v_accvgpr_write_b32 a7, v155\n"
  "v_accvgpr_write_b32 a8, v156\n"
  "v_accvgpr_write_b32 a9, v157\n"
  "v_accvgpr_write_b32 a10, v158\n"
  "v_accvgpr_write_b32 a11, v159\n"
  "v_accvgpr_read_b32 v148, a12\n"
  "v_accvgpr_read_b32 v149, a13\n"
  "v_accvgpr_read_b32 v150, a14\n"
  "v_accvgpr_read_b32 v151, a15\n"
  "v_accvgpr_read_b32 v152, a16\n"
  "v_accvgpr_read_b32 v153, a17\n"
  "v_accvgpr_read_b32 v154, a18\n"
  "v_accvgpr_read_b32 v155, a19\n"
  "v_accvgpr_read_b32 v156, a20\n"
  "v_accvgpr_read_b32 v157, a21\n"
  "v_accvgpr_read_b32 v158, a22\n"
  "v_accvgpr_read_b32 v159, a23\n"
  "v_mul_f32 v148, v140, v148\n"
  "v_mul_f32 v149, v140, v149\n"
  "v_mul_f32 v150, v140, v150\n"
  "v_mul_f32 v151, v140, v151\n"
  "v_mul_f32 v152, v140, v152\n"
  "v_mul_f32 v153, v140, v153\n"
  "v_mul_f32 v154, v140, v154\n"
  "v_mul_f32 v155, v140, v155\n"
  "v_mul_f32 v156, v140, v156\n"
  "v_mul_f32 v157, v140, v157\n"
  "v_mul_f32 v158, v140, v158\n"
  "v_mul_f32 v159, v140, v159\n"
  "v_accvgpr_write_b32 a12, v148\n"
  "v_accvgpr_write_b32 a13, v149\n"
  "v_accvgpr_write_b32 a14, v150\n"
  "v_accvgpr_write_b32 a15, v151\n"
  "v_accvgpr_write_b32 a16, v152\n"
  "v_accvgpr_write_b32 a17, v153\n"
  "v_accvgpr_write_b32 a18, v154\n"
  "v_accvgpr_write_b32 a19, v155\n"
  "v_accvgpr_write_b32 a20, v156\n"
  "v_accvgpr_write_b32 a21, v157\n"
  "v_accvgpr_write_b32 a22, v158\n"
  "v_accvgpr_write_b32 a23, v159\n"
  "v_accvgpr_read_b32 v148, a24\n"
  "v_accvgpr_read_b32 v149, a25\n"
  "v_accvgpr_read_b32 v150, a26\n"
  "v_accvgpr_read_b32 v151, a27\n"
  "v_accvgpr_read_b32 v152, a28\n"
  "v_accvgpr_read_b32 v153, a29\n"
  "v_accvgpr_read_b32 v154, a30\n"
  "v_accvgpr_read_b32 v155, a31\n"
  "v_accvgpr_read_b32 v156, a224\n"
  "v_accvgpr_read_b32 v157, a225\n"
  "v_accvgpr_read_b32 v158, a226\n"
  "v_accvgpr_read_b32 v159, a227\n"
  "v_mul_f32 v148, v140, v148\n"
  "v_mul_f32 v149, v140, v149\n"
  "v_mul_f32 v150, v140, v150\n"
  "v_mul_f32 v151, v140, v151\n"
  "v_mul_f32 v152, v140, v152\n"
  "v_mul_f32 v153, v140, v153\n"
  "v_mul_f32 v154, v140, v154\n"
  "v_mul_f32 v155, v140, v155\n"
  "v_mul_f32 v156, v140, v156\n"
  "v_mul_f32 v157, v140, v157\n"
  "v_mul_f32 v158, v140, v158\n"
  "v_mul_f32 v159, v140, v159\n"
  "v_accvgpr_write_b32 a24, v148\n"
  "v_accvgpr_write_b32 a25, v149\n"
  "v_accvgpr_write_b32 a26, v150\n"
  "v_accvgpr_write_b32 a27, v151\n"
  "v_accvgpr_write_b32 a28, v152\n"
  "v_accvgpr_write_b32 a29, v153\n"
  "v_accvgpr_write_b32 a30, v154\n"
  "v_accvgpr_write_b32 a31, v155\n"
  "v_accvgpr_write_b32 a224, v156\n"
  "v_accvgpr_write_b32 a225, v157\n"
  "v_accvgpr_write_b32 a226, v158\n"
  "v_accvgpr_write_b32 a227, v159\n"
  "v_accvgpr_read_b32 v148, a32\n"
  "v_accvgpr_read_b32 v149, a33\n"
  "v_accvgpr_read_b32 v150, a34\n"
  "v_accvgpr_read_b32 v151, a35\n"
  "v_accvgpr_read_b32 v152, a36\n"
  "v_accvgpr_read_b32 v153, a37\n"
  "v_accvgpr_read_b32 v154, a38\n"
  "v_accvgpr_read_b32 v155, a39\n"
  "v_accvgpr_read_b32 v156, a40\n"
  "v_accvgpr_read_b32 v157, a41\n"
  "v_accvgpr_read_b32 v158, a42\n"
  "v_accvgpr_read_b32 v159, a43\n"
  "v_mul_f32 v148, v141, v148\n"
  "v_mul_f32 v149, v141, v149\n"
  "v_mul_f32 v150, v141, v150\n"
  "v_mul_f32 v151, v141, v151\n"
  "v_mul_f32 v152, v141, v152\n"
  "v_mul_f32 v153, v141, v153\n"
  "v_mul_f32 v154, v141, v154\n"
  "v_mul_f32 v155, v141, v155\n"
  "v_mul_f32 v156, v141, v156\n"
  "v_mul_f32 v157, v141, v157\n"
  "v_mul_f32 v158, v141, v158\n"
  "v_mul_f32 v159, v141, v159\n"
  "v_accvgpr_write_b32 a32, v148\n"
  "v_accvgpr_write_b32 a33, v149\n"
  "v_accvgpr_write_b32 a34, v150\n"
  "v_accvgpr_write_b32 a35, v151\n"
  "v_accvgpr_write_b32 a36, v152\n"
  "v_accvgpr_write_b32 a37, v153\n"
  "v_accvgpr_write_b32 a38, v154\n"
  "v_accvgpr_write_b32 a39, v155\n"
  "v_accvgpr_write_b32 a40, v156\n"
  "v_accvgpr_write_b32 a41, v157\n"
  "v_accvgpr_write_b32 a42, v158\n"
  "v_accvgpr_write_b32 a43, v159\n"
  "v_accvgpr_read_b32 v148, a44\n"
  "v_accvgpr_read_b32 v149, a45\n"
  "v_accvgpr_read_b32 v150, a46\n"
  "v_accvgpr_read_b32 v151, a47\n"
  "v_accvgpr_read_b32 v152, a48\n"
  "v_accvgpr_read_b32 v153, a49\n"
  "v_accvgpr_read_b32 v154, a50\n"
  "v_accvgpr_read_b32 v155, a51\n"
  "v_accvgpr_read_b32 v156, a52\n"
  "v_accvgpr_read_b32 v157, a53\n"
  "v_accvgpr_read_b32 v158, a54\n"
  "v_accvgpr_read_b32 v159, a55\n"
  "v_mul_f32 v148, v141, v148\n"
  "v_mul_f32 v149, v141, v149\n"
  "v_mul_f32 v150, v141, v150\n"
  "v_mul_f32 v151, v141, v151\n"
  "v_mul_f32 v152, v141, v152\n"
  "v_mul_f32 v153, v141, v153\n"
  "v_mul_f32 v154, v141, v154\n"
  "v_mul_f32 v155, v141, v155\n"
  "v_mul_f32 v156, v141, v156\n"
  "v_mul_f32 v157, v141, v157\n"
  "v_mul_f32 v158, v141, v158\n"
  "v_mul_f32 v159, v141, v159\n"
  "v_accvgpr_write_b32 a44, v148\n"
  "v_accvgpr_write_b32 a45, v149\n"
  "v_accvgpr_write_b32 a46, v150\n"
  "v_accvgpr_write_b32 a47, v151\n"
  "v_accvgpr_write_b32 a48, v152\n"
  "v_accvgpr_write_b32 a49, v153\n"
  "v_accvgpr_write_b32 a50, v154\n"
  "v_accvgpr_write_b32 a51, v155\n"
  "v_accvgpr_write_b32 a52, v156\n"
  "v_accvgpr_write_b32 a53, v157\n"
  "v_accvgpr_write_b32 a54, v158\n"
  "v_accvgpr_write_b32 a55, v159\n"
  "v_accvgpr_read_b32 v148, a56\n"
  "v_accvgpr_read_b32 v149, a57\n"
  "v_accvgpr_read_b32 v150, a58\n"
  "v_accvgpr_read_b32 v151, a59\n"
  "v_accvgpr_read_b32 v152, a60\n"
  "v_accvgpr_read_b32 v153, a61\n"
  "v_accvgpr_read_b32 v154, a62\n"
  "v_accvgpr_read_b32 v155, a63\n"
  "v_accvgpr_read_b32 v156, a228\n"
  "v_accvgpr_read_b32 v157, a229\n"
  "v_accvgpr_read_b32 v158, a230\n"
  "v_accvgpr_read_b32 v159, a231\n"
  "v_mul_f32 v148, v141, v148\n"
  "v_mul_f32 v149, v141, v149\n"
  "v_mul_f32 v150, v141, v150\n"
  "v_mul_f32 v151, v141, v151\n"
  "v_mul_f32 v152, v141, v152\n"
  "v_mul_f32 v153, v141, v153\n"
  "v_mul_f32 v154, v141, v154\n"
  "v_mul_f32 v155, v141, v155\n"
  "v_mul_f32 v156, v141, v156\n"
  "v_mul_f32 v157, v141, v157\n"
  "v_mul_f32 v158, v141, v158\n"
  "v_mul_f32 v159, v141, v159\n"
  "v_accvgpr_write_b32 a56, v148\n"
  "v_accvgpr_write_b32 a57, v149\n"
  "v_accvgpr_write_b32 a58, v150\n"
  "v_accvgpr_write_b32 a59, v151\n"
  "v_accvgpr_write_b32 a60, v152\n"
  "v_accvgpr_write_b32 a61, v153\n"
  "v_accvgpr_write_b32 a62, v154\n"
  "v_accvgpr_write_b32 a63, v155\n"
  "v_accvgpr_write_b32 a228, v156\n"
  "v_accvgpr_write_b32 a229, v157\n"
  "v_accvgpr_write_b32 a230, v158\n"
  "v_accvgpr_write_b32 a231, v159\n"
  "v_accvgpr_read_b32 v148, a64\n"
  "v_accvgpr_read_b32 v149, a65\n"
  "v_accvgpr_read_b32 v150, a66\n"
  "v_accvgpr_read_b32 v151, a67\n"
  "v_accvgpr_read_b32 v152, a68\n"
  "v_accvgpr_read_b32 v153, a69\n"
  "v_accvgpr_read_b32 v154, a70\n"
  "v_accvgpr_read_b32 v155, a71\n"
  "v_accvgpr_read_b32 v156, a72\n"
  "v_accvgpr_read_b32 v157, a73\n"
  "v_accvgpr_read_b32 v158, a74\n"
  "v_accvgpr_read_b32 v159, a75\n"
  "v_mul_f32 v148, v142, v148\n"
  "v_mul_f32 v149, v142, v149\n"
  "v_mul_f32 v150, v142, v150\n"
  "v_mul_f32 v151, v142, v151\n"
  "v_mul_f32 v152, v142, v152\n"
  "v_mul_f32 v153, v142, v153\n"
  "v_mul_f32 v154, v142, v154\n"
  "v_mul_f32 v155, v142, v155\n"
  "v_mul_f32 v156, v142, v156\n"
  "v_mul_f32 v157, v142, v157\n"
  "v_mul_f32 v158, v142, v158\n"
  "v_mul_f32 v159, v142, v159\n"
  "v_accvgpr_write_b32 a64, v148\n"
  "v_accvgpr_write_b32 a65, v149\n"
  "v_accvgpr_write_b32 a66, v150\n"
  "v_accvgpr_write_b32 a67, v151\n"
  "v_accvgpr_write_b32 a68, v152\n"
  "v_accvgpr_write_b32 a69, v153\n"
  "v_accvgpr_write_b32 a70, v154\n"
  "v_accvgpr_write_b32 a71, v155\n"
  "v_accvgpr_write_b32 a72, v156\n"
  "v_accvgpr_write_b32 a73, v157\n"
  "v_accvgpr_write_b32 a74, v158\n"
  "v_accvgpr_write_b32 a75, v159\n"
  "v_accvgpr_read_b32 v148, a76\n"
  "v_accvgpr_read_b32 v149, a77\n"
  "v_accvgpr_read_b32 v150, a78\n"
  "v_accvgpr_read_b32 v151, a79\n"
  "v_accvgpr_read_b32 v152, a80\n"
  "v_accvgpr_read_b32 v153, a81\n"
  "v_accvgpr_read_b32 v154, a82\n"
  "v_accvgpr_read_b32 v155, a83\n"
  "v_accvgpr_read_b32 v156, a84\n"
  "v_accvgpr_read_b32 v157, a85\n"
  "v_accvgpr_read_b32 v158, a86\n"
  "v_accvgpr_read_b32 v159, a87\n"
  "v_mul_f32 v148, v142, v148\n"
  "v_mul_f32 v149, v142, v149\n"
  "v_mul_f32 v150, v142, v150\n"
  "v_mul_f32 v151, v142, v151\n"
  "v_mul_f32 v152, v142, v152\n"
  "v_mul_f32 v153, v142, v153\n"
  "v_mul_f32 v154, v142, v154\n"
  "v_mul_f32 v155, v142, v155\n"
  "v_mul_f32 v156, v142, v156\n"
  "v_mul_f32 v157, v142, v157\n"
  "v_mul_f32 v158, v142, v158\n"
  "v_mul_f32 v159, v142, v159\n"
  "v_accvgpr_write_b32 a76, v148\n"
  "v_accvgpr_write_b32 a77, v149\n"
  "v_accvgpr_write_b32 a78, v150\n"
  "v_accvgpr_write_b32 a79, v151\n"
  "v_accvgpr_write_b32 a80, v152\n"
  "v_accvgpr_write_b32 a81, v153\n"
  "v_accvgpr_write_b32 a82, v154\n"
  "v_accvgpr_write_b32 a83, v155\n"
  "v_accvgpr_write_b32 a84, v156\n"
  "v_accvgpr_write_b32 a85, v157\n"
  "v_accvgpr_write_b32 a86, v158\n"
  "v_accvgpr_write_b32 a87, v159\n"
  "v_accvgpr_read_b32 v148, a88\n"
  "v_accvgpr_read_b32 v149, a89\n"
  "v_accvgpr_read_b32 v150, a90\n"
  "v_accvgpr_read_b32 v151, a91\n"
  "v_accvgpr_read_b32 v152, a92\n"
  "v_accvgpr_read_b32 v153, a93\n"
  "v_accvgpr_read_b32 v154, a94\n"
  "v_accvgpr_read_b32 v155, a95\n"
  "v_accvgpr_read_b32 v156, a232\n"
  "v_accvgpr_read_b32 v157, a233\n"
  "v_accvgpr_read_b32 v158, a234\n"
  "v_accvgpr_read_b32 v159, a235\n"
  "v_mul_f32 v148, v142, v148\n"
  "v_mul_f32 v149, v142, v149\n"
  "v_mul_f32 v150, v142, v150\n"
  "v_mul_f32 v151, v142, v151\n"
  "v_mul_f32 v152, v142, v152\n"
  "v_mul_f32 v153, v142, v153\n"
  "v_mul_f32 v154, v142, v154\n"
  "v_mul_f32 v155, v142, v155\n"
  "v_mul_f32 v156, v142, v156\n"
  "v_mul_f32 v157, v142, v157\n"
  "v_mul_f32 v158, v142, v158\n"
  "v_mul_f32 v159, v142, v159\n"
  "v_accvgpr_write_b32 a88, v148\n"
  "v_accvgpr_write_b32 a89, v149\n"
  "v_accvgpr_write_b32 a90, v150\n"
  "v_accvgpr_write_b32 a91, v151\n"
  "v_accvgpr_write_b32 a92, v152\n"
  "v_accvgpr_write_b32 a93, v153\n"
  "v_accvgpr_write_b32 a94, v154\n"
  "v_accvgpr_write_b32 a95, v155\n"
  "v_accvgpr_write_b32 a232, v156\n"
  "v_accvgpr_write_b32 a233, v157\n"
  "v_accvgpr_write_b32 a234, v158\n"
  "v_accvgpr_write_b32 a235, v159\n"
  "v_accvgpr_read_b32 v148, a96\n"
  "v_accvgpr_read_b32 v149, a97\n"
  "v_accvgpr_read_b32 v150, a98\n"
  "v_accvgpr_read_b32 v151, a99\n"
  "v_accvgpr_read_b32 v152, a100\n"
  "v_accvgpr_read_b32 v153, a101\n"
  "v_accvgpr_read_b32 v154, a102\n"
  "v_accvgpr_read_b32 v155, a103\n"
  "v_accvgpr_read_b32 v156, a104\n"
  "v_accvgpr_read_b32 v157, a105\n"
  "v_accvgpr_read_b32 v158, a106\n"
  "v_accvgpr_read_b32 v159, a107\n"
  "v_mul_f32 v148, v143, v148\n"
  "v_mul_f32 v149, v143, v149\n"
  "v_mul_f32 v150, v143, v150\n"
  "v_mul_f32 v151, v143, v151\n"
  "v_mul_f32 v152, v143, v152\n"
  "v_mul_f32 v153, v143, v153\n"
  "v_mul_f32 v154, v143, v154\n"
  "v_mul_f32 v155, v143, v155\n"
  "v_mul_f32 v156, v143, v156\n"
  "v_mul_f32 v157, v143, v157\n"
  "v_mul_f32 v158, v143, v158\n"
  "v_mul_f32 v159, v143, v159\n"
  "v_accvgpr_write_b32 a96, v148\n"
  "v_accvgpr_write_b32 a97, v149\n"
  "v_accvgpr_write_b32 a98, v150\n"
  "v_accvgpr_write_b32 a99, v151\n"
  "v_accvgpr_write_b32 a100, v152\n"
  "v_accvgpr_write_b32 a101, v153\n"
  "v_accvgpr_write_b32 a102, v154\n"
  "v_accvgpr_write_b32 a103, v155\n"
  "v_accvgpr_write_b32 a104, v156\n"
  "v_accvgpr_write_b32 a105, v157\n"
  "v_accvgpr_write_b32 a106, v158\n"
  "v_accvgpr_write_b32 a107, v159\n"
  "v_accvgpr_read_b32 v148, a108\n"
  "v_accvgpr_read_b32 v149, a109\n"
  "v_accvgpr_read_b32 v150, a110\n"
  "v_accvgpr_read_b32 v151, a111\n"
  "v_accvgpr_read_b32 v152, a112\n"
  "v_accvgpr_read_b32 v153, a113\n"
  "v_accvgpr_read_b32 v154, a114\n"
  "v_accvgpr_read_b32 v155, a115\n"
  "v_accvgpr_read_b32 v156, a116\n"
  "v_accvgpr_read_b32 v157, a117\n"
  "v_accvgpr_read_b32 v158, a118\n"
  "v_accvgpr_read_b32 v159, a119\n"
  "v_mul_f32 v148, v143, v148\n"
  "v_mul_f32 v149, v143, v149\n"
  "v_mul_f32 v150, v143, v150\n"
  "v_mul_f32 v151, v143, v151\n"
  "v_mul_f32 v152, v143, v152\n"
  "v_mul_f32 v153, v143, v153\n"
  "v_mul_f32 v154, v143, v154\n"
  "v_mul_f32 v155, v143, v155\n"
  "v_mul_f32 v156, v143, v156\n"
  "v_mul_f32 v157, v143, v157\n"
  "v_mul_f32 v158, v143, v158\n"
  "v_mul_f32 v159, v143, v159\n"
  "v_accvgpr_write_b32 a108, v148\n"
  "v_accvgpr_write_b32 a109, v149\n"
  "v_accvgpr_write_b32 a110, v150\n"
  "v_accvgpr_write_b32 a111, v151\n"
  "v_accvgpr_write_b32 a112, v152\n"
  "v_accvgpr_write_b32 a113, v153\n"
  "v_accvgpr_write_b32 a114, v154\n"
  "v_accvgpr_write_b32 a115, v155\n"
  "v_accvgpr_write_b32 a116, v156\n"
  "v_accvgpr_write_b32 a117, v157\n"
  "v_accvgpr_write_b32 a118, v158\n"
  "v_accvgpr_write_b32 a119, v159\n"
  "v_accvgpr_read_b32 v148, a120\n"
  "v_accvgpr_read_b32 v149, a121\n"
  "v_accvgpr_read_b32 v150, a122\n"
  "v_accvgpr_read_b32 v151, a123\n"
  "v_accvgpr_read_b32 v152, a124\n"
  "v_accvgpr_read_b32 v153, a125\n"
  "v_accvgpr_read_b32 v154, a126\n"
  "v_accvgpr_read_b32 v155, a127\n"
  "v_accvgpr_read_b32 v156, a236\n"
  "v_accvgpr_read_b32 v157, a237\n"
  "v_accvgpr_read_b32 v158, a238\n"
  "v_accvgpr_read_b32 v159, a239\n"
  "v_mul_f32 v148, v143, v148\n"
  "v_mul_f32 v149, v143, v149\n"
  "v_mul_f32 v150, v143, v150\n"
  "v_mul_f32 v151, v143, v151\n"
  "v_mul_f32 v152, v143, v152\n"
  "v_mul_f32 v153, v143, v153\n"
  "v_mul_f32 v154, v143, v154\n"
  "v_mul_f32 v155, v143, v155\n"
  "v_mul_f32 v156, v143, v156\n"
  "v_mul_f32 v157, v143, v157\n"
  "v_mul_f32 v158, v143, v158\n"
  "v_mul_f32 v159, v143, v159\n"
  "v_accvgpr_write_b32 a120, v148\n"
  "v_accvgpr_write_b32 a121, v149\n"
  "v_accvgpr_write_b32 a122, v150\n"
  "v_accvgpr_write_b32 a123, v151\n"
  "v_accvgpr_write_b32 a124, v152\n"
  "v_accvgpr_write_b32 a125, v153\n"
  "v_accvgpr_write_b32 a126, v154\n"
  "v_accvgpr_write_b32 a127, v155\n"
  "v_accvgpr_write_b32 a236, v156\n"
  "v_accvgpr_write_b32 a237, v157\n"
  "v_accvgpr_write_b32 a238, v158\n"
  "v_accvgpr_write_b32 a239, v159\n"
  "v_accvgpr_read_b32 v148, a128\n"
  "v_accvgpr_read_b32 v149, a129\n"
  "v_accvgpr_read_b32 v150, a130\n"
  "v_accvgpr_read_b32 v151, a131\n"
  "v_accvgpr_read_b32 v152, a132\n"
  "v_accvgpr_read_b32 v153, a133\n"
  "v_accvgpr_read_b32 v154, a134\n"
  "v_accvgpr_read_b32 v155, a135\n"
  "v_accvgpr_read_b32 v156, a136\n"
  "v_accvgpr_read_b32 v157, a137\n"
  "v_accvgpr_read_b32 v158, a138\n"
  "v_accvgpr_read_b32 v159, a139\n"
  "v_mul_f32 v148, v144, v148\n"
  "v_mul_f32 v149, v144, v149\n"
  "v_mul_f32 v150, v144, v150\n"
  "v_mul_f32 v151, v144, v151\n"
  "v_mul_f32 v152, v144, v152\n"
  "v_mul_f32 v153, v144, v153\n"
  "v_mul_f32 v154, v144, v154\n"
  "v_mul_f32 v155, v144, v155\n"
  "v_mul_f32 v156, v144, v156\n"
  "v_mul_f32 v157, v144, v157\n"
  "v_mul_f32 v158, v144, v158\n"
  "v_mul_f32 v159, v144, v159\n"
  "v_accvgpr_write_b32 a128, v148\n"
  "v_accvgpr_write_b32 a129, v149\n"
  "v_accvgpr_write_b32 a130, v150\n"
  "v_accvgpr_write_b32 a131, v151\n"
  "v_accvgpr_write_b32 a132, v152\n"
  "v_accvgpr_write_b32 a133, v153\n"
  "v_accvgpr_write_b32 a134, v154\n"
  "v_accvgpr_write_b32 a135, v155\n"
  "v_accvgpr_write_b32 a136, v156\n"
  "v_accvgpr_write_b32 a137, v157\n"
  "v_accvgpr_write_b32 a138, v158\n"
  "v_accvgpr_write_b32 a139, v159\n"
  "v_accvgpr_read_b32 v148, a140\n"
  "v_accvgpr_read_b32 v149, a141\n"
  "v_accvgpr_read_b32 v150, a142\n"
  "v_accvgpr_read_b32 v151, a143\n"
  "v_accvgpr_read_b32 v152, a144\n"
  "v_accvgpr_read_b32 v153, a145\n"
  "v_accvgpr_read_b32 v154, a146\n"
  "v_accvgpr_read_b32 v155, a147\n"
  "v_accvgpr_read_b32 v156, a148\n"
  "v_accvgpr_read_b32 v157, a149\n"
  "v_accvgpr_read_b32 v158, a150\n"
  "v_accvgpr_read_b32 v159, a151\n"
  "v_mul_f32 v148, v144, v148\n"
  "v_mul_f32 v149, v144, v149\n"
  "v_mul_f32 v150, v144, v150\n"
  "v_mul_f32 v151, v144, v151\n"
  "v_mul_f32 v152, v144, v152\n"
  "v_mul_f32 v153, v144, v153\n"
  "v_mul_f32 v154, v144, v154\n"
  "v_mul_f32 v155, v144, v155\n"
  "v_mul_f32 v156, v144, v156\n"
  "v_mul_f32 v157, v144, v157\n"
  "v_mul_f32 v158, v144, v158\n"
  "v_mul_f32 v159, v144, v159\n"
  "v_accvgpr_write_b32 a140, v148\n"
  "v_accvgpr_write_b32 a141, v149\n"
  "v_accvgpr_write_b32 a142, v150\n"
  "v_accvgpr_write_b32 a143, v151\n"
  "v_accvgpr_write_b32 a144, v152\n"
  "v_accvgpr_write_b32 a145, v153\n"
  "v_accvgpr_write_b32 a146, v154\n"
  "v_accvgpr_write_b32 a147, v155\n"
  "v_accvgpr_write_b32 a148, v156\n"
  "v_accvgpr_write_b32 a149, v157\n"
  "v_accvgpr_write_b32 a150, v158\n"
  "v_accvgpr_write_b32 a151, v159\n"
  "v_accvgpr_read_b32 v148, a152\n"
  "v_accvgpr_read_b32 v149, a153\n"
  "v_accvgpr_read_b32 v150, a154\n"
  "v_accvgpr_read_b32 v151, a155\n"
  "v_accvgpr_read_b32 v152, a156\n"
  "v_accvgpr_read_b32 v153, a157\n"
  "v_accvgpr_read_b32 v154, a158\n"
  "v_accvgpr_read_b32 v155, a159\n"
  "v_accvgpr_read_b32 v156, a240\n"
  "v_accvgpr_read_b32 v157, a241\n"
  "v_accvgpr_read_b32 v158, a242\n"
  "v_accvgpr_read_b32 v159, a243\n"
  "v_mul_f32 v148, v144, v148\n"
  "v_mul_f32 v149, v144, v149\n"
  "v_mul_f32 v150, v144, v150\n"
  "v_mul_f32 v151, v144, v151\n"
  "v_mul_f32 v152, v144, v152\n"
  "v_mul_f32 v153, v144, v153\n"
  "v_mul_f32 v154, v144, v154\n"
  "v_mul_f32 v155, v144, v155\n"
  "v_mul_f32 v156, v144, v156\n"
  "v_mul_f32 v157, v144, v157\n"
  "v_mul_f32 v158, v144, v158\n"
  "v_mul_f32 v159, v144, v159\n"
  "v_accvgpr_write_b32 a152, v148\n"
  "v_accvgpr_write_b32 a153, v149\n"
  "v_accvgpr_write_b32 a154, v150\n"
  "v_accvgpr_write_b32 a155, v151\n"
  "v_accvgpr_write_b32 a156, v152\n"
  "v_accvgpr_write_b32 a157, v153\n"
  "v_accvgpr_write_b32 a158, v154\n"
  "v_accvgpr_write_b32 a159, v155\n"
  "v_accvgpr_write_b32 a240, v156\n"
  "v_accvgpr_write_b32 a241, v157\n"
  "v_accvgpr_write_b32 a242, v158\n"
  "v_accvgpr_write_b32 a243, v159\n"
  "v_accvgpr_read_b32 v148, a160\n"
  "v_accvgpr_read_b32 v149, a161\n"
  "v_accvgpr_read_b32 v150, a162\n"
  "v_accvgpr_read_b32 v151, a163\n"
  "v_accvgpr_read_b32 v152, a164\n"
  "v_accvgpr_read_b32 v153, a165\n"
  "v_accvgpr_read_b32 v154, a166\n"
  "v_accvgpr_read_b32 v155, a167\n"
  "v_accvgpr_read_b32 v156, a168\n"
  "v_accvgpr_read_b32 v157, a169\n"
  "v_accvgpr_read_b32 v158, a170\n"
  "v_accvgpr_read_b32 v159, a171\n"
  "v_mul_f32 v148, v145, v148\n"
  "v_mul_f32 v149, v145, v149\n"
  "v_mul_f32 v150, v145, v150\n"
  "v_mul_f32 v151, v145, v151\n"
  "v_mul_f32 v152, v145, v152\n"
  "v_mul_f32 v153, v145, v153\n"
  "v_mul_f32 v154, v145, v154\n"
  "v_mul_f32 v155, v145, v155\n"
  "v_mul_f32 v156, v145, v156\n"
  "v_mul_f32 v157, v145, v157\n"
  "v_mul_f32 v158, v145, v158\n"
  "v_mul_f32 v159, v145, v159\n"
  "v_accvgpr_write_b32 a160, v148\n"
  "v_accvgpr_write_b32 a161, v149\n"
  "v_accvgpr_write_b32 a162, v150\n"
  "v_accvgpr_write_b32 a163, v151\n"
  "v_accvgpr_write_b32 a164, v152\n"
  "v_accvgpr_write_b32 a165, v153\n"
  "v_accvgpr_write_b32 a166, v154\n"
  "v_accvgpr_write_b32 a167, v155\n"
  "v_accvgpr_write_b32 a168, v156\n"
  "v_accvgpr_write_b32 a169, v157\n"
  "v_accvgpr_write_b32 a170, v158\n"
  "v_accvgpr_write_b32 a171, v159\n"
  "v_accvgpr_read_b32 v148, a172\n"
  "v_accvgpr_read_b32 v149, a173\n"
  "v_accvgpr_read_b32 v150, a174\n"
  "v_accvgpr_read_b32 v151, a175\n"
  "v_accvgpr_read_b32 v152, a176\n"
  "v_accvgpr_read_b32 v153, a177\n"
  "v_accvgpr_read_b32 v154, a178\n"
  "v_accvgpr_read_b32 v155, a179\n"
  "v_accvgpr_read_b32 v156, a180\n"
  "v_accvgpr_read_b32 v157, a181\n"
  "v_accvgpr_read_b32 v158, a182\n"
  "v_accvgpr_read_b32 v159, a183\n"
  "v_mul_f32 v148, v145, v148\n"
  "v_mul_f32 v149, v145, v149\n"
  "v_mul_f32 v150, v145, v150\n"
  "v_mul_f32 v151, v145, v151\n"
  "v_mul_f32 v152, v145, v152\n"
  "v_mul_f32 v153, v145, v153\n"
  "v_mul_f32 v154, v145, v154\n"
  "v_mul_f32 v155, v145, v155\n"
  "v_mul_f32 v156, v145, v156\n"
  "v_mul_f32 v157, v145, v157\n"
  "v_mul_f32 v158, v145, v158\n"
  "v_mul_f32 v159, v145, v159\n"
  "v_accvgpr_write_b32 a172, v148\n"
  "v_accvgpr_write_b32 a173, v149\n"
  "v_accvgpr_write_b32 a174, v150\n"
  "v_accvgpr_write_b32 a175, v151\n"
  "v_accvgpr_write_b32 a176, v152\n"
  "v_accvgpr_write_b32 a177, v153\n"
  "v_accvgpr_write_b32 a178, v154\n"
  "v_accvgpr_write_b32 a179, v155\n"
  "v_accvgpr_write_b32 a180, v156\n"
  "v_accvgpr_write_b32 a181, v157\n"
  "v_accvgpr_write_b32 a182, v158\n"
  "v_accvgpr_write_b32 a183, v159\n"
  "v_accvgpr_read_b32 v148, a184\n"
  "v_accvgpr_read_b32 v149, a185\n"
  "v_accvgpr_read_b32 v150, a186\n"
  "v_accvgpr_read_b32 v151, a187\n"
  "v_accvgpr_read_b32 v152, a188\n"
  "v_accvgpr_read_b32 v153, a189\n"
  "v_accvgpr_read_b32 v154, a190\n"
  "v_accvgpr_read_b32 v155, a191\n"
  "v_accvgpr_read_b32 v156, a244\n"
  "v_accvgpr_read_b32 v157, a245\n"
  "v_accvgpr_read_b32 v158, a246\n"
  "v_accvgpr_read_b32 v159, a247\n"
  "v_mul_f32 v148, v145, v148\n"
  "v_mul_f32 v149, v145, v149\n"
  "v_mul_f32 v150, v145, v150\n"
  "v_mul_f32 v151, v145, v151\n"
  "v_mul_f32 v152, v145, v152\n"
  "v_mul_f32 v153, v145, v153\n"
  "v_mul_f32 v154, v145, v154\n"
  "v_mul_f32 v155, v145, v155\n"
  "v_mul_f32 v156, v145, v156\n"
  "v_mul_f32 v157, v145, v157\n"
  "v_mul_f32 v158, v145, v158\n"
  "v_mul_f32 v159, v145, v159\n"
  "v_accvgpr_write_b32 a184, v148\n"
  "v_accvgpr_write_b32 a185, v149\n"
  "v_accvgpr_write_b32 a186, v150\n"
  "v_accvgpr_write_b32 a187, v151\n"
  "v_accvgpr_write_b32 a188, v152\n"
  "v_accvgpr_write_b32 a189, v153\n"
  "v_accvgpr_write_b32 a190, v154\n"
  "v_accvgpr_write_b32 a191, v155\n"
  "v_accvgpr_write_b32 a244, v156\n"
  "v_accvgpr_write_b32 a245, v157\n"
  "v_accvgpr_write_b32 a246, v158\n"
  "v_accvgpr_write_b32 a247, v159\n"
  "v_accvgpr_read_b32 v148, a192\n"
  "v_accvgpr_read_b32 v149, a193\n"
  "v_accvgpr_read_b32 v150, a194\n"
  "v_accvgpr_read_b32 v151, a195\n"
  "v_accvgpr_read_b32 v152, a196\n"
  "v_accvgpr_read_b32 v153, a197\n"
  "v_accvgpr_read_b32 v154, a198\n"
  "v_accvgpr_read_b32 v155, a199\n"
  "v_accvgpr_read_b32 v156, a200\n"
  "v_accvgpr_read_b32 v157, a201\n"
  "v_accvgpr_read_b32 v158, a202\n"
  "v_accvgpr_read_b32 v159, a203\n"
  "v_mul_f32 v148, v146, v148\n"
  "v_mul_f32 v149, v146, v149\n"
  "v_mul_f32 v150, v146, v150\n"
  "v_mul_f32 v151, v146, v151\n"
  "v_mul_f32 v152, v146, v152\n"
  "v_mul_f32 v153, v146, v153\n"
  "v_mul_f32 v154, v146, v154\n"
  "v_mul_f32 v155, v146, v155\n"
  "v_mul_f32 v156, v146, v156\n"
  "v_mul_f32 v157, v146, v157\n"
  "v_mul_f32 v158, v146, v158\n"
  "v_mul_f32 v159, v146, v159\n"
  "v_accvgpr_write_b32 a192, v148\n"
  "v_accvgpr_write_b32 a193, v149\n"
  "v_accvgpr_write_b32 a194, v150\n"
  "v_accvgpr_write_b32 a195, v151\n"
  "v_accvgpr_write_b32 a196, v152\n"
  "v_accvgpr_write_b32 a197, v153\n"
  "v_accvgpr_write_b32 a198, v154\n"
  "v_accvgpr_write_b32 a199, v155\n"
  "v_accvgpr_write_b32 a200, v156\n"
  "v_accvgpr_write_b32 a201, v157\n"
  "v_accvgpr_write_b32 a202, v158\n"
  "v_accvgpr_write_b32 a203, v159\n"
  "v_accvgpr_read_b32 v148, a204\n"
  "v_accvgpr_read_b32 v149, a205\n"
  "v_accvgpr_read_b32 v150, a206\n"
  "v_accvgpr_read_b32 v151, a207\n"
  "v_accvgpr_read_b32 v152, a208\n"
  "v_accvgpr_read_b32 v153, a209\n"
  "v_accvgpr_read_b32 v154, a210\n"
  "v_accvgpr_read_b32 v155, a211\n"
  "v_accvgpr_read_b32 v156, a212\n"
  "v_accvgpr_read_b32 v157, a213\n"
  "v_accvgpr_read_b32 v158, a214\n"
  "v_accvgpr_read_b32 v159, a215\n"
  "v_mul_f32 v148, v146, v148\n"
  "v_mul_f32 v149, v146, v149\n"
  "v_mul_f32 v150, v146, v150\n"
  "v_mul_f32 v151, v146, v151\n"
  "v_mul_f32 v152, v146, v152\n"
  "v_mul_f32 v153, v146, v153\n"
  "v_mul_f32 v154, v146, v154\n"
  "v_mul_f32 v155, v146, v155\n"
  "v_mul_f32 v156, v146, v156\n"
  "v_mul_f32 v157, v146, v157\n"
  "v_mul_f32 v158, v146, v158\n"
  "v_mul_f32 v159, v146, v159\n"
  "v_accvgpr_write_b32 a204, v148\n"
  "v_accvgpr_write_b32 a205, v149\n"
  "v_accvgpr_write_b32 a206, v150\n"
  "v_accvgpr_write_b32 a207, v151\n"
  "v_accvgpr_write_b32 a208, v152\n"
  "v_accvgpr_write_b32 a209, v153\n"
  "v_accvgpr_write_b32 a210, v154\n"
  "v_accvgpr_write_b32 a211, v155\n"
  "v_accvgpr_write_b32 a212, v156\n"
  "v_accvgpr_write_b32 a213, v157\n"
  "v_accvgpr_write_b32 a214, v158\n"
  "v_accvgpr_write_b32 a215, v159\n"
  "v_accvgpr_read_b32 v148, a216\n"
  "v_accvgpr_read_b32 v149, a217\n"
  "v_accvgpr_read_b32 v150, a218\n"
  "v_accvgpr_read_b32 v151, a219\n"
  "v_accvgpr_read_b32 v152, a220\n"
  "v_accvgpr_read_b32 v153, a221\n"
  "v_accvgpr_read_b32 v154, a222\n"
  "v_accvgpr_read_b32 v155, a223\n"
  "v_accvgpr_read_b32 v156, a248\n"
  "v_accvgpr_read_b32 v157, a249\n"
  "v_accvgpr_read_b32 v158, a250\n"
  "v_accvgpr_read_b32 v159, a251\n"
  "v_mul_f32 v148, v146, v148\n"
  "v_mul_f32 v149, v146, v149\n"
  "v_mul_f32 v150, v146, v150\n"
  "v_mul_f32 v151, v146, v151\n"
  "v_mul_f32 v152, v146, v152\n"
  "v_mul_f32 v153, v146, v153\n"
  "v_mul_f32 v154, v146, v154\n"
  "v_mul_f32 v155, v146, v155\n"
  "v_mul_f32 v156, v146, v156\n"
  "v_mul_f32 v157, v146, v157\n"
  "v_mul_f32 v158, v146, v158\n"
  "v_mul_f32 v159, v146, v159\n"
  "v_accvgpr_write_b32 a216, v148\n"
  "v_accvgpr_write_b32 a217, v149\n"
  "v_accvgpr_write_b32 a218, v150\n"
  "v_accvgpr_write_b32 a219, v151\n"
  "v_accvgpr_write_b32 a220, v152\n"
  "v_accvgpr_write_b32 a221, v153\n"
  "v_accvgpr_write_b32 a222, v154\n"
  "v_accvgpr_write_b32 a223, v155\n"
  "v_accvgpr_write_b32 a248, v156\n"
  "v_accvgpr_write_b32 a249, v157\n"
  "v_accvgpr_write_b32 a250, v158\n"
  "v_accvgpr_write_b32 a251, v159\n"
  "s_nop 1\n"
  "s_setpc_b64 s[84:85]\n"
  "L_end:\n"
  :: "s"(px), "s"(pw), "s"(pk), "s"(pv), "s"(pu), "s"((unsigned long long)p.ws), "s"(po), "s"(blockIdx.x * 4 + wave),
     "s"((((br * 16 + b) * 8 + role) << 24) | (576u * 8192u - h * 256u) | (n0 << 4) | c0)
  : "v0", "v1", "v2", "v3", "v4", "v5", "v6", "v7", "v8", "v9", "v10", "v11", "v12", "v13", "v14", "v15", "v16", "v17", "v18", "v19", "v20", "v21", "v22", "v23", "v24", "v25", "v26", "v27", "v28", "v29", "v30", "v31", "v32", "v33", "v34", "v35", "v36", "v37", "v38", "v39", "v40", "v41", "v42", "v43", "v44", "v45", "v46", "v47", "v48", "v49", "v50", "v51", "v52", "v53", "v54", "v55", "v56", "v57", "v58", "v59", "v60", "v61", "v62", "v63", "v64", "v65", "v66", "v67", "v68", "v69", "v70", "v71", "v72", "v73", "v74", "v75", "v76", "v77", "v78", "v79", "v80", "v81", "v82", "v83", "v84", "v85", "v86", "v87", "v88", "v89", "v90", "v91", "v92", "v93", "v94", "v95", "v96", "v97", "v98", "v99", "v100", "v101", "v102", "v103", "v104", "v105", "v106", "v107", "v108", "v109", "v110", "v111", "v112", "v113", "v114", "v115", "v116", "v117", "v118", "v119", "v120", "v121", "v122", "v123", "v124", "v125", "v126", "v127", "v128", "v129", "v130", "v131", "v132", "v133", "v134", "v135", "v136", "v137", "v138", "v139", "v140", "v141", "v142", "v143", "v144", "v145", "v146", "v147", "v148", "v149", "v150", "v151", "v152", "v153", "v154", "v155", "v156", "v157", "v158", "v159", "v160", "v161", "v162", "v163", "v164", "v165", "v166", "v167", "v168", "v169", "v170", "v171", "v172", "v173", "v174", "v175", "v176", "v177", "v178", "v179", "v180", "v181", "v182", "v183", "v184", "v185", "v186", "v187", "v188", "v189", "v190", "v191", "v192", "v193", "v194", "v195", "v196", "v197", "v198", "v199", "v200", "v201", "v202", "v203", "v204", "v205", "v206", "v207", "v208", "v209", "v210", "v211", "v212", "v213", "v214", "v215", "v216", "v217", "v218", "v219", "v220", "v221", "v222", "v223", "v224", "v225", "v226", "v227", "v228", "v229", "v230", "v231", "v232", "v233", "v234", "v235", "v236", "v237", "v238", "v239", "v240", "v241", "v242", "v243", "v244", "v245", "v246", "v247", "v248", "v249", "v250", "v251", "v252", "v253", "v254", "v255", "a0", "a1", "a2", "a3", "a4", "a5", "a6", "a7", "a8", "a9", "a10", "a11", "a12", "a13", "a14", "a15", "a16", "a17", "a18", "a19", "a20", "a21", "a22", "a23", "a24", "a25", "a26", "a27", "a28", "a29", "a30", "a31", "a32", "a33", "a34", "a35", "a36", "a37", "a38", "a39", "a40", "a41", "a42", "a43", "a44", "a45", "a46", "a47", "a48", "a49", "a50", "a51", "a52", "a53", "a54", "a55", "a56", "a57", "a58", "a59", "a60", "a61", "a62", "a63", "a64", "a65", "a66", "a67", "a68", "a69", "a70", "a71", "a72", "a73", "a74", "a75", "a76", "a77", "a78", "a79", "a80", "a81", "a82", "a83", "a84", "a85", "a86", "a87", "a88", "a89", "a90", "a91", "a92", "a93", "a94", "a95", "a96", "a97", "a98", "a99", "a100", "a101", "a102", "a103", "a104", "a105", "a106", "a107", "a108", "a109", "a110", "a111", "a112", "a113", "a114", "a115", "a116", "a117", "a118", "a119", "a120", "a121", "a122", "a123", "a124", "a125", "a126", "a127", "a128", "a129", "a130", "a131", "a132", "a133", "a134", "a135", "a136", "a137", "a138", "a139", "a140", "a141", "a142", "a143", "a144", "a145", "a146", "a147", "a148", "a149", "a150", "a151", "a152", "a153", "a154", "a155", "a156", "a157", "a158", "a159", "a160", "a161", "a162", "a163", "a164", "a165", "a166", "a167", "a168", "a169", "a170", "a171", "a172", "a173", "a174", "a175", "a176", "a177", "a178", "a179", "a180", "a181", "a182", "a183", "a184", "a185", "a186", "a187", "a188", "a189", "a190", "a191", "a192", "a193", "a194", "a195", "a196", "a197", "a198", "a199", "a200", "a201", "a202", "a203", "a204", "a205", "a206", "a207", "a208", "a209", "a210", "a211", "a212", "a213", "a214", "a215", "a216", "a217", "a218", "a219", "a220", "a221", "a222", "a223", "a224", "a225", "a226", "a227", "a228", "a229", "a230", "a231", "a232", "a233", "a234", "a235", "a236", "a237", "a238", "a239", "a240", "a241", "a242", "a243", "a244", "a245", "a246", "a247", "a248", "a249", "a250", "a251", "a252", "a253", "a254", "a255", "s36", "s37", "s38", "s39", "s40", "s41", "s42", "s43", "s44", "s45", "s46", "s47", "s48", "s49", "s50", "s51", "s52", "s53", "s54", "s55", "s56", "s57", "s58", "s59", "s60", "s61", "s62", "s63", "s64", "s65", "s66", "s67", "s68", "s69", "s70", "s71", "s72", "s73", "s74", "s75", "s76", "s77", "s78", "s79", "s80", "s81", "s82", "s83", "s84", "s85", "s86", "s87", "s88", "s89", "s90", "s91", "s92", "s93", "s94", "s95", "s96", "s97", "s98", "s99", "vcc", "scc", "memory");
}

extern "C" void kernel_launch(void* const* d_in, const int* in_sizes, int n_in, void* d_out, int out_size, void* d_ws, size_t ws_size, hipStream_t stream) {
  P1 p;
  p.x[0] = (const char*)d_in[0]; p.k[0] = (const char*)d_in[1]; p.v[0] = (const char*)d_in[2]; p.wq[0] = (const char*)d_in[3]; p.wo[0] = (const char*)d_in[4];
  p.x[1] = (const char*)d_in[5]; p.k[1] = (const char*)d_in[6]; p.v[1] = (const char*)d_in[7]; p.wq[1] = (const char*)d_in[8]; p.wo[1] = (const char*)d_in[9];
  p.ws = (char*)d_ws; p.out = (char*)d_out;
  (void)hipFuncSetAttribute((const void*)k_attn, hipFuncAttributeMaxDynamicSharedMemorySize, 163840);
  hipLaunchKernelGGL(k_attn, dim3(256), dim3(256), 163840, stream, p);
}
```
